```python
import torch

def _softmax(x, axis=-1):
    return torch.softmax(x, dim=axis)
def _gelu(x):
    return torch.nn.functional.gelu(x, approximate="tanh")
def _silu(x):
    return torch.nn.functional.silu(x)
def _sigmoid(x):
    return torch.sigmoid(x)
def _layernorm(x, axis=-1, eps=1e-5):
    m = x.mean(dim=axis, keepdim=True)
    v = x.var(dim=axis, keepdim=True, unbiased=False)
    return (x - m) / torch.sqrt(v + eps)
def _scatter_add(tgt, idx, src, axis=0):
    return tgt.clone().index_add_(axis, idx, src)
def _segment_sum(src, seg, n):
    out = torch.zeros((n,) + src.shape[1:], dtype=src.dtype)
    return out.index_add_(0, seg, src)
def _pad(x, pw):
    flat = tuple(v for pair in reversed(pw) for v in pair)
    return torch.nn.functional.pad(x, flat)

kernel_name = 'recurrent_scan__gqa_block_cached__mixer_channel_mlp__decode'

def setup_inputs():

    _w = (lambda a: (lambda t: t.to(torch.bfloat16) if t.is_floating_point() else t)(torch.as_tensor(a)))
    def _A_setup():
        B, Sq, D = 2, 1, 512
        Hq, Hkv, Sk, Dh = 32, 8, 32768, 128
        assert Hq % Hkv == 0, (Hq, Hkv)
        return {
            "x": _w((lambda *s: torch.randn(*s, dtype=torch.bfloat16) * 0.1)(B, Sq, D)),
            "k_cache": _w((lambda *s: torch.randn(*s, dtype=torch.bfloat16) * 0.8)(B, Hkv, Sk, Dh)),
            "v_cache": _w((lambda *s: torch.randn(*s, dtype=torch.bfloat16) * 3.2)(B, Hkv, Sk, Dh)),
            "wq": _w((lambda *s: torch.randn(*s, dtype=torch.bfloat16) * 0.8)(D, Hq * Dh) * (D ** -0.5)),
            "wo": _w((lambda *s: torch.randn(*s, dtype=torch.bfloat16) * 3.2)(Hq * Dh, D) * ((Hq * Dh) ** -0.5)),
        }
    _A_in = _A_setup()

    def _B_setup():
        B, S, D, F = 2, 1, 512, 1792
        return {
            "x": _w((lambda *s: torch.randn(*s, dtype=torch.bfloat16) * 0.1)(B, S, D)),
            "w1": _w((lambda *s: torch.randn(*s, dtype=torch.bfloat16) * 3.2)(D, F) * (D ** -0.5)),
            "b1": _w((lambda *s: torch.randn(*s, dtype=torch.bfloat16) * 3.2)(F)),
            "w2": _w((lambda *s: torch.randn(*s, dtype=torch.bfloat16) * 3.2)(F, D) * (F ** -0.5)),
            "b2": _w((lambda *s: torch.randn(*s, dtype=torch.bfloat16) * 3.2)(D)),
        }
    _B_in = _B_setup()

    return {
        "A_x": _A_in["x"],
        "A_k_cache": _A_in["k_cache"],
        "A_v_cache": _A_in["v_cache"],
        "A_wq": _A_in["wq"],
        "A_wo": _A_in["wo"],
        "B_x": _B_in["x"],
        "B_w1": _B_in["w1"],
        "B_b1": _B_in["b1"],
        "B_w2": _B_in["w2"],
        "B_b2": _B_in["b2"],
    }

def reference(A_x, A_k_cache, A_v_cache, A_wq, A_wo, B_x, B_w1, B_b1, B_w2, B_b2):
    def _A_ref(x, k_cache, v_cache, wq, wo):
        B, Sq, D = x.shape
        Hq, Dh = 32, 128
        Hkv = k_cache.shape[1]
        rep = Hq // Hkv
        q = (lambda x, *d: x.permute(*d))((x @ wq).reshape(B, Sq, Hq, Dh), 0, 2, 1, 3)
        k = (lambda x, n, axis: torch.repeat_interleave(x, n, dim=axis))(k_cache, rep, axis=1)
        v = (lambda x, n, axis: torch.repeat_interleave(x, n, dim=axis))(v_cache, rep, axis=1)
        scores = torch.einsum("bhqd,bhkd->bhqk", q, k) * (Dh ** -0.5)
        p = _softmax(scores, axis=-1)
        o = torch.einsum("bhqk,bhkd->bhqd", p, v)
        return (lambda x, *d: x.permute(*d))(o, 0, 2, 1, 3).reshape(B, Sq, Hq * Dh) @ wo

    def _B_ref(x, w1, b1, w2, b2):
        y = _layernorm(x, axis=-1)
        h = _gelu(torch.einsum("bsd,df->bsf", y, w1) + b1)
        return torch.einsum("bsf,fd->bsd", h, w2) + b2

    x = A_x
    xn = _layernorm(x, axis=-1)
    s = _A_ref(xn, A_k_cache, A_v_cache, A_wq, A_wo)
    g = _silu(_B_ref(xn, B_w1, B_b1, B_w2, B_b2))
    return x + s * g
```

```cpp
#include <hip/hip_runtime.h>
#include <hip/hip_bf16.h>
#include <stdint.h>

#define GAS __attribute__((address_space(1)))
#define LAS __attribute__((address_space(3)))
typedef unsigned u32;
typedef unsigned long long u64;
typedef unsigned short u16;
typedef unsigned v4u __attribute__((ext_vector_type(4)));
typedef unsigned v2u __attribute__((ext_vector_type(2)));
typedef float f32x4 __attribute__((ext_vector_type(4)));
typedef float f32x2 __attribute__((ext_vector_type(2)));
typedef short s16x4 __attribute__((ext_vector_type(4)));
typedef __bf16 bf16x8 __attribute__((ext_vector_type(8)));
typedef __bf16 bf16x2 __attribute__((ext_vector_type(2)));

#define K_DEPTH 6
#define K_PACE 2
#define K_INTER 0
#define K_TS 0

constexpr int DM = 512, HD = 128, SK = 32768, FF = 1792;
constexpr int NWG = 256, NSW = 4, NTHR = (NSW + 1) * 64;
constexpr int DEPTH = K_DEPTH, PACE = K_PACE;
constexpr int NTILE = SK / 16 / 16 / NSW;
constexpr float SC = 0.08838834764831845f * 1.4426950408889634f;
constexpr u32 SENT = 0xFFFFFFFFu;
constexpr u32 SPIN_CAP = 1u << 18;

constexpr u32 WS_EPOCH = 0;
constexpr u32 WS_QG = 4096;
constexpr u32 WS_HG = WS_QG + 8 * 2 * 512 * 8;
constexpr u32 WS_SLOT = 131072;
constexpr u32 SL_PS = 2048, SL_PM = 2560, SL_ML = 3072;
constexpr u32 WS_END = WS_SLOT + NWG * 4096;
constexpr u32 WS_TS = WS_END;
static_assert(WS_HG + 2 * FF * 8 <= WS_SLOT, "");

constexpr u32 L_WQ = 0, L_W1 = 16384, L_WO = 32768, L_W2 = 49152;
constexpr u32 L_XN = 57344, L_Y = L_XN + 2048, L_Q = L_Y + 2048, L_H = L_Q + 1024, L_O = L_H + 256;
constexpr u32 L_MRG = L_O + 512;
constexpr u32 L_MML = L_MRG + NSW * 4 * 132 * 4;
constexpr u32 L_END = L_MML + NSW * 4 * 2 * 4;

struct Params {
    const u16 *x, *k, *v, *wq, *wo, *w1, *b1, *w2, *b2;
    u16* out;
    unsigned char* ws;
};

__device__ __forceinline__ f32x4 mfma32(v4u a, v4u b, f32x4 c) {
    return __builtin_amdgcn_mfma_f32_16x16x32_bf16(__builtin_bit_cast(bf16x8, a), __builtin_bit_cast(bf16x8, b), c, 0, 0, 0);
}
__device__ __forceinline__ f32x4 mfma16(u32 a0, u32 a1, u32 b0, u32 b1, f32x4 c) {
    return __builtin_amdgcn_mfma_f32_16x16x16bf16_1k(__builtin_bit_cast(s16x4, v2u{a0, a1}), __builtin_bit_cast(s16x4, v2u{b0, b1}), c, 0, 0, 0);
}
__device__ __forceinline__ u32 pk_bf16(float lo, float hi) {
    return __builtin_bit_cast(u32, __builtin_convertvector(f32x2{lo, hi}, bf16x2));
}
__device__ __forceinline__ float bf_lo(u32 w) { return __builtin_bit_cast(float, w << 16); }
__device__ __forceinline__ float bf_hi(u32 w) { return __builtin_bit_cast(float, w & 0xffff0000u); }
__device__ __forceinline__ float rbf(float f) { return bf_lo(pk_bf16(f, f)); }
__device__ __forceinline__ float ex2(float x) { return __builtin_amdgcn_exp2f(x); }
template <int CTRL> __device__ __forceinline__ float dpp(float x) {
    return __builtin_bit_cast(float, __builtin_amdgcn_mov_dpp(__builtin_bit_cast(int, x), CTRL, 0xf, 0xf, true));
}
constexpr int XOR1 = 0xB1, XOR2 = 0x4E, XOR7 = 0x141, ROR8 = 0x128;
__device__ __forceinline__ float x16_max(float x) {
    auto s = __builtin_amdgcn_permlane16_swap(__float_as_uint(x), __float_as_uint(x), false, false);
    return fmaxf(__uint_as_float(s[0]), __uint_as_float(s[1]));
}
__device__ __forceinline__ float x32_max(float x) {
    auto s = __builtin_amdgcn_permlane32_swap(__float_as_uint(x), __float_as_uint(x), false, false);
    return fmaxf(__uint_as_float(s[0]), __uint_as_float(s[1]));
}
__device__ __forceinline__ float x16_sum(float x) {
    auto s = __builtin_amdgcn_permlane16_swap(__float_as_uint(x), __float_as_uint(x), false, false);
    return __uint_as_float(s[0]) + __uint_as_float(s[1]);
}
__device__ __forceinline__ float x32_sum(float x) {
    auto s = __builtin_amdgcn_permlane32_swap(__float_as_uint(x), __float_as_uint(x), false, false);
    return __uint_as_float(s[0]) + __uint_as_float(s[1]);
}
__device__ __forceinline__ float row_sum(float x) {
    x += dpp<XOR1>(x); x += dpp<XOR2>(x); x += dpp<XOR7>(x); x += dpp<ROR8>(x); return x;
}
__device__ __forceinline__ float wave_sum(float x) { return x32_sum(x16_sum(row_sum(x))); }
__device__ __forceinline__ u32 umax4(v4u a) { return max(max(a.x, a.y), max(a.z, a.w)); }
__device__ __forceinline__ u32 no_sent(float f) { const u32 u = __float_as_uint(f); return u == SENT ? 0x7FC00000u : u; }

#define RLX_AGENT __ATOMIC_RELAXED, __HIP_MEMORY_SCOPE_AGENT
constexpr int AUX_NT = 2, AUX_SC1 = 16;
__device__ __forceinline__ __amdgpu_buffer_rsrc_t mk_rsrc(const void* p, u32 bytes) {
    return __builtin_amdgcn_make_buffer_rsrc((void*)p, 0, bytes, 0x00020000);
}
__device__ __forceinline__ void glds_nt(const void* src, u32 dst) {
    asm volatile("s_mov_b32 m0, %1\n\ts_nop 0\n\tglobal_load_lds_dwordx4 %0, off nt" ::"v"(src), "s"(dst) : "memory");
}
template <int N> __device__ __forceinline__ void wait_vm() { asm volatile("s_waitcnt vmcnt(%0)" ::"i"(N) : "memory"); }
__device__ __forceinline__ void wg_barrier() { asm volatile("s_waitcnt lgkmcnt(0)\n\ts_barrier" ::: "memory"); }
__device__ __forceinline__ v2u tr_read(const LAS unsigned char* p) {
    return __builtin_bit_cast(v2u, __builtin_amdgcn_ds_read_tr16_b64_v4i16((LAS s16x4*)p));
}
__device__ __forceinline__ v4u tr_frag(const LAS unsigned char* img, int pitch, int row0, int lane) {
    const int g = lane >> 4, q = (lane >> 2) & 3, p = lane & 3;
    const LAS unsigned char* a = img + (row0 + 8 * g + q) * pitch + 8 * p;
    const v2u lo = tr_read(a), hi = tr_read(a + 4 * pitch);
    return v4u{lo.x, lo.y, hi.x, hi.y};
}
#define STAMP(i) do { } while (0)
#define STAMPS_OUT(N) do { } while (0)

__device__ __forceinline__ f32x4 gemv16(const LAS unsigned char* img, const LAS unsigned char* act, int lane) {
    const int g = lane >> 4, n = lane & 15;
    f32x4 a0 = {0.f, 0.f, 0.f, 0.f}, a1 = {0.f, 0.f, 0.f, 0.f};
    const LAS unsigned char* ap = act + (n & 1) * 1024 + g * 16;
#pragma unroll
    for (int s = 0; s < 16; s += 2) {
        a0 = mfma32(tr_frag(img, 32, 32 * s, lane), *(const LAS v4u*)(ap + 64 * s), a0);
        a1 = mfma32(tr_frag(img, 32, 32 * s + 32, lane), *(const LAS v4u*)(ap + 64 * s + 64), a1);
    }
    return a0 + a1;
}
__device__ __forceinline__ float sel4(f32x4 v, int r) { return r == 0 ? v[0] : r == 1 ? v[1] : r == 2 ? v[2] : v[3]; }

__global__ void __launch_bounds__(NTHR, 2) fused_decode(Params p) {
    extern __shared__ __attribute__((aligned(16))) unsigned char lds_raw[];
    LAS unsigned char* L = (LAS unsigned char*)lds_raw;
    const int tid = threadIdx.x, lane = tid & 63, w = __builtin_amdgcn_readfirstlane(tid >> 6);
    const int bid = blockIdx.x;
    const int pair = bid >> 4, b = pair >> 3, kvh = pair & 7, s = bid & 15;
    const int hh = 2 * b + (s >> 3), cg = s & 7, hi = 4 * kvh + hh;
    const int g = lane >> 4, n = lane & 15;
    const __amdgpu_buffer_rsrc_t rws = mk_rsrc(p.ws, WS_END);
    const u32 myslot = WS_SLOT + bid * 4096;
    STAMP(0);

    if (w < NSW) {
        const __amdgpu_buffer_rsrc_t rk = K_INTER ? mk_rsrc(p.k + (size_t)pair * SK * HD, SK * HD * 2) : mk_rsrc(p.k + ((size_t)pair * SK + s * 2048) * HD, 2048 * HD * 2);
        const __amdgpu_buffer_rsrc_t rv = K_INTER ? mk_rsrc(p.v + (size_t)pair * SK * HD, SK * HD * 2) : mk_rsrc(p.v + ((size_t)pair * SK + s * 2048) * HD, 2048 * HD * 2);
        const u32 t0 = (K_INTER ? s * NSW + w : w) * 4096;
        constexpr u32 TSTR = (K_INTER ? 16 * NSW : NSW) * 4096;
        u32 ko = t0 + (4 * (n & 3) + (n >> 2)) * 256 + g * 16, vo = t0 + lane * 16;
        v4u Kr[DEPTH][4], Vr[DEPTH][4];
#define ISSUE(d)                                                                                               \
    do {                                                                                                       \
        _Pragma("unroll") for (int j = 0; j < 4; ++j) Kr[d][j] = __builtin_amdgcn_raw_buffer_load_b128(rk, ko + 64 * j, 0, AUX_NT);   \
        _Pragma("unroll") for (int j = 0; j < 4; ++j) Vr[d][j] = __builtin_amdgcn_raw_buffer_load_b128(rv, vo + 1024 * j, 0, AUX_NT); \
        ko += TSTR; vo += TSTR;                                                                                \
        __builtin_amdgcn_sched_barrier(0);                                                                     \
    } while (0)
        wg_barrier();
#pragma unroll
        for (int d = 0; d < DEPTH; ++d) {
            if (d >= PACE) { asm volatile("" ::"v"(Vr[d - PACE][3])); __builtin_amdgcn_sched_barrier(0); }
            ISSUE(d);
        }
        STAMP(1);
        wg_barrier();
        STAMP(2);
        v4u Q[4];
#pragma unroll
        for (int j = 0; j < 4; ++j) Q[j] = *(const LAS v4u*)(L + L_Q + (n >> 2) * 256 + 64 * j + 16 * g);
        const int jj = n & 3;
        const u32 mask0 = jj == 0 ? 0x0000ffffu : jj == 1 ? 0xffff0000u : 0u, mask1 = jj == 2 ? 0x0000ffffu : jj == 3 ? 0xffff0000u : 0u;
        float m = -INFINITY, l = 0.f;
        f32x4 acc0 = {0.f, 0.f, 0.f, 0.f}, acc1 = {0.f, 0.f, 0.f, 0.f};
#define TILE(d)                                                                                                \
    do {                                                                                                       \
        f32x4 sc = {0.f, 0.f, 0.f, 0.f};                                                                       \
        _Pragma("unroll") for (int j = 0; j < 4; ++j) sc = mfma32(Kr[d][j], Q[j], sc);                         \
        const float tm = x32_max(x16_max(fmaxf(fmaxf(sc[0], sc[1]), fmaxf(sc[2], sc[3]))));                    \
        const float mn = fmaxf(m, tm), mc = mn * SC, alpha = ex2((m - mn) * SC);                               \
        float pr[4];                                                                                           \
        _Pragma("unroll") for (int r = 0; r < 4; ++r) pr[r] = ex2(fmaf(sc[r], SC, -mc));                       \
        l = fmaf(l, alpha, (pr[0] + pr[1]) + (pr[2] + pr[3]));                                                 \
        m = mn;                                                                                                \
        acc0 *= alpha; acc1 *= alpha;                                                                          \
        _Pragma("unroll") for (int r = 0; r < 4; ++r) {                                                        \
            const u32 pb = pk_bf16(pr[r], pr[r]), x0 = pb & mask0, x1 = pb & mask1;                            \
            acc0 = mfma16(Vr[d][r].x, Vr[d][r].y, x0, x1, acc0);                                               \
            acc1 = mfma16(Vr[d][r].z, Vr[d][r].w, x0, x1, acc1);                                               \
        }                                                                                                      \
        __builtin_amdgcn_sched_barrier(0);                                                                     \
    } while (0)
#pragma unroll 1
        for (int it = 0; it < NTILE / DEPTH; ++it) {
#pragma unroll
            for (int d = 0; d < DEPTH; ++d) { TILE(d); ISSUE(d); }
        }
#pragma unroll
        for (int d = 0; d < NTILE % DEPTH; ++d) TILE(d);
#undef TILE
#undef ISSUE
        STAMP(3);
        l = x32_sum(x16_sum(l));
        {
            LAS float* mr = (LAS float*)(L + L_MRG) + (w * 4 + (n >> 2)) * 132 + 32 * g + jj;
#pragma unroll
            for (int r = 0; r < 4; ++r) { mr[8 * r] = acc0[r]; mr[8 * r + 4] = acc1[r]; }
            if (g == 0 && jj == 0) *(LAS f32x2*)(L + L_MML + (w * 4 + (n >> 2)) * 8) = f32x2{m * SC, l};
        }
        wg_barrier();
        {
            f32x2 ml[NSW];
#pragma unroll
            for (int u = 0; u < NSW; ++u) ml[u] = *(const LAS f32x2*)(L + L_MML + (u * 4 + w) * 8);
            float M = ml[0].x;
#pragma unroll
            for (int u = 1; u < NSW; ++u) M = fmaxf(M, ml[u].x);
            float Ls = 0.f; f32x4 o = {0.f, 0.f, 0.f, 0.f};
#pragma unroll
            for (int u = 0; u < NSW; ++u) {
                const float f = ex2(ml[u].x - M);
                Ls = fmaf(f, ml[u].y, Ls);
                o += f * *(const LAS f32x4*)(L + L_MRG + ((u * 4 + w) * 132 + 4 * (lane & 31)) * 4);
            }
            if (lane < 32) __builtin_amdgcn_raw_buffer_store_b128(v4u{no_sent(o[0]), no_sent(o[1]), no_sent(o[2]), no_sent(o[3])}, rws, myslot + w * 512 + lane * 16, 0, AUX_SC1);
            else if (lane == 32) __builtin_amdgcn_raw_buffer_store_b64(v2u{no_sent(M), no_sent(Ls)}, rws, myslot + SL_ML + w * 8, 0, AUX_SC1);
        }
        STAMP(4);
        v4u WO[4];
#pragma unroll
        for (int j = 0; j < 4; ++j) WO[j] = tr_frag(L + L_WO + 32 * w, 128, 32 * j, lane);
        {
            const int bb = w & 1, dh = w >> 1;
            const u32 src = WS_SLOT + (((bb * 8 + kvh) * 16) + 4 * g) * 4096;
            const u32 ao = src + hh * 512 + dh * 256 + n * 16, mo = src + SL_ML + hh * 8;
            v4u a[4]; v2u q2[4];
#pragma unroll 1
            for (u32 spin = 0; spin < SPIN_CAP; ++spin) {
                u32 mx = 0;
#pragma unroll
                for (int k = 0; k < 4; ++k) { a[k] = __builtin_amdgcn_raw_buffer_load_b128(rws, ao + k * 4096, 0, AUX_SC1); q2[k] = __builtin_amdgcn_raw_buffer_load_b64(rws, mo + k * 4096, 0, AUX_SC1); }
#pragma unroll
                for (int k = 0; k < 4; ++k) mx = max(mx, max(umax4(a[k]), max(q2[k].x, q2[k].y)));
                if (__all(mx != SENT)) break;
                __builtin_amdgcn_s_sleep(2);
            }
            STAMP(5);
            float M = fmaxf(fmaxf(__uint_as_float(q2[0].x), __uint_as_float(q2[1].x)), fmaxf(__uint_as_float(q2[2].x), __uint_as_float(q2[3].x)));
            M = x32_max(x16_max(M));
            float Ls = 0.f; f32x4 o = {0.f, 0.f, 0.f, 0.f};
#pragma unroll
            for (int k = 0; k < 4; ++k) {
                const float f = ex2(__uint_as_float(q2[k].x) - M);
                Ls = fmaf(f, __uint_as_float(q2[k].y), Ls);
                o += f * __builtin_bit_cast(f32x4, a[k]);
            }
            Ls = x32_sum(x16_sum(Ls));
            const float inv = 1.f / Ls;
#pragma unroll
            for (int r = 0; r < 4; ++r) o[r] = x32_sum(x16_sum(o[r])) * inv;
            if (g == 0) *(LAS v2u*)(L + L_O + bb * 256 + dh * 128 + n * 8) = v2u{pk_bf16(o[0], o[1]), pk_bf16(o[2], o[3])};
        }
        wg_barrier();
        {
            f32x4 sp = {0.f, 0.f, 0.f, 0.f};
#pragma unroll
            for (int j = 0; j < 4; ++j) sp = mfma32(WO[j], *(const LAS v4u*)(L + L_O + (n & 1) * 256 + 64 * j + 16 * g), sp);
            if (n < 2) __builtin_amdgcn_raw_buffer_store_b128(v4u{no_sent(sp[0]), no_sent(sp[1]), no_sent(sp[2]), no_sent(sp[3])}, rws, myslot + SL_PS + n * 256 + w * 64 + g * 16, 0, AUX_SC1);
        }
        STAMP(6);
        STAMPS_OUT(K_NSTAMP_S);
    } else {
        __builtin_amdgcn_s_setprio(3);
        const u32 lb = (u32)(size_t)L;
        v4u xr0, xr1; v2u ep;
        {
            const void* xa = p.x + 8 * lane;
            const void* ea = p.ws + WS_EPOCH;
            asm volatile("global_load_dwordx4 %0, %3, off\n\tglobal_load_dwordx4 %1, %3, off offset:1024\n\tglobal_load_dwordx2 %2, %4, off sc1"
                         : "=&v"(xr0), "=&v"(xr1), "=&v"(ep) : "v"(xa), "v"(ea) : "memory");
        }
        {
            const int jq = 4 * cg + hh;
            const u16* src = p.wq + (size_t)(lane >> 1) * 4096 + kvh * 512 + 16 * jq + 8 * (lane & 1);
#pragma unroll
            for (int u = 0; u < 16; ++u) glds_nt(src + (size_t)u * 32 * 4096, lb + L_WQ + u * 1024);
        }
        {
            const v4u sv = {SENT, SENT, SENT, SENT};
#pragma unroll
            for (int u = 0; u < 3; ++u) __builtin_amdgcn_raw_buffer_store_b128(sv, rws, myslot + u * 1024 + lane * 16, 0, AUX_SC1);
            __builtin_amdgcn_raw_buffer_store_b128(sv, rws, lane < 2 ? myslot + SL_ML + lane * 16 : 0xFFFFFF00u, 0, AUX_SC1);
        }
        asm volatile("s_barrier" ::: "memory");
        const bool do_m1 = hi >= 16 && cg < 7, do_m2 = hi < 28;
        const int jm = (cg * 4 + ((hi - 16) >> 2)) * 4 + (hi & 3);
        asm volatile("s_waitcnt vmcnt(20)" : "+v"(xr0), "+v"(xr1), "+v"(ep)::"memory");
        STAMP(1);
        const u64 tag = ((((u64)ep.y << 32) | ep.x) + 1) & 0xFFFFFFFFFFFFull;
        {
            float v[2][8];
#pragma unroll
            for (int j = 0; j < 4; ++j) { v[0][2 * j] = bf_lo(xr0[j]); v[0][2 * j + 1] = bf_hi(xr0[j]); v[1][2 * j] = bf_lo(xr1[j]); v[1][2 * j + 1] = bf_hi(xr1[j]); }
#pragma unroll
            for (int pass = 0; pass < 2; ++pass) {
#pragma unroll
                for (int bb = 0; bb < 2; ++bb) {
                    float su = 0.f;
#pragma unroll
                    for (int j = 0; j < 8; ++j) su += v[bb][j];
                    const float mean = wave_sum(su) * (1.f / DM);
                    float sq = 0.f;
#pragma unroll
                    for (int j = 0; j < 8; ++j) { const float d = v[bb][j] - mean; sq = fmaf(d, d, sq); }
                    const float var = wave_sum(sq) * (1.f / DM);
                    const float rs = 1.f / sqrtf(var + 1e-5f);
                    v4u o;
#pragma unroll
                    for (int j = 0; j < 4; ++j) {
                        o[j] = pk_bf16((v[bb][2 * j] - mean) * rs, (v[bb][2 * j + 1] - mean) * rs);
                        v[bb][2 * j] = bf_lo(o[j]); v[bb][2 * j + 1] = bf_hi(o[j]);
                    }
                    *(LAS v4u*)(L + (pass ? L_Y : L_XN) + bb * 1024 + lane * 16) = o;
                }
            }
        }
        wait_vm<4>();
        STAMP(2);
        {
            const f32x4 qv = gemv16(L + L_WQ, L + L_XN, lane);
            const u32 qb = pk_bf16(sel4(qv, n >> 1), 0.f) & 0xffffu;
            GAS u64* dst = (GAS u64*)(p.ws + WS_QG) + (kvh * 2 + (n & 1)) * 512 + 16 * (4 * cg + hh) + 4 * g + (n >> 1);
            STAMP(9);
            wait_vm<0>();
            STAMP(10);
            if (n < 8) __hip_atomic_store(dst, (tag << 16) | qb, RLX_AGENT);
        }
        STAMP(3);
        {
            GAS u64* src = (GAS u64*)(p.ws + WS_QG) + (kvh * 2 + b) * 512 + lane;
            u64 A[8], B[8], C[8];
#define POLL(X) do { _Pragma("unroll") for (int k = 0; k < 8; ++k) X[k] = __hip_atomic_load(src + 64 * k, RLX_AGENT); __builtin_amdgcn_sched_barrier(0); } while (0)
#define GOOD(X) ({ bool ok_ = true; _Pragma("unroll") for (int k = 0; k < 8; ++k) ok_ &= (X[k] >> 16) == tag; __all(ok_); })
#define TAKE(X) do { _Pragma("unroll") for (int k = 0; k < 8; ++k) *(LAS u16*)(L + L_Q + (64 * k + lane) * 2) = (u16)X[k]; } while (0)
            POLL(A); __builtin_amdgcn_s_sleep(12); POLL(B); __builtin_amdgcn_s_sleep(12); POLL(C);
#pragma unroll 1
            for (u32 spin = 0; spin < SPIN_CAP; ++spin) {
                if (GOOD(A)) { TAKE(A); break; }
                POLL(A);
                if (GOOD(B)) { TAKE(B); break; }
                POLL(B);
                if (GOOD(C)) { TAKE(C); break; }
                POLL(C);
            }
#undef POLL
#undef GOOD
#undef TAKE
        }
        STAMP(4);
        wg_barrier();
        if (do_m1) {
            const u16* src = p.w1 + (size_t)(lane >> 1) * FF + 16 * jm + 8 * (lane & 1);
#pragma unroll
            for (int u = 0; u < 16; ++u) glds_nt(src + (size_t)u * 32 * FF, lb + L_W1 + u * 1024);
        }
        {
            const u16* src = p.wo + (size_t)(hi * 128 + (lane >> 3)) * DM + 64 * cg + 8 * (lane & 7);
#pragma unroll
            for (int u = 0; u < 16; ++u) glds_nt(src + (size_t)u * 8 * DM, lb + L_WO + u * 1024);
        }
        if (do_m2) {
            const u16* src = p.w2 + (size_t)(hi * 64 + (lane >> 3)) * DM + 64 * cg + 8 * (lane & 7);
#pragma unroll
            for (int u = 0; u < 8; ++u) glds_nt(src + (size_t)u * 8 * DM, lb + L_W2 + u * 1024);
        }
        if (do_m1) {
            const int col = 16 * jm + 4 * g + ((n >> 1) & 3);
            const float bias = bf_lo(p.b1[col]);
            wait_vm<0>();
            const float u = rbf(rbf(sel4(gemv16(L + L_W1, L + L_Y, lane), n >> 1)) + bias);
            const float z = 1.5957691216057308f * fmaf(0.044715f * u * u, u, u);
            const float h = u / (1.f + __expf(-z));
            if (n < 8) __hip_atomic_store((GAS u64*)(p.ws + WS_HG) + (n & 1) * FF + col, (tag << 16) | (pk_bf16(h, 0.f) & 0xffffu), RLX_AGENT);
        }
        const int fb = hi >> 4, fc = 64 * cg + 4 * (hi & 15);
        const v2u xo = *(const GAS v2u*)(p.x + fb * DM + fc), b2o = *(const GAS v2u*)(p.b2 + fc);
        {
            const int i = lane & 31;
            GAS u64* ev = (GAS u64*)(p.ws + WS_QG) + ((i >> 2) * 2) * 512 + 16 * (4 * cg + (i & 3));
            GAS u64* hs = do_m2 ? (GAS u64*)(p.ws + WS_HG) + 64 * hi + lane : ev;
            const int hstr = do_m2 ? FF : 0;
            u64 e, h0, h1;
#pragma unroll 1
            for (u32 spin = 0; spin < SPIN_CAP; ++spin) {
                e = __hip_atomic_load(ev, RLX_AGENT); h0 = __hip_atomic_load(hs, RLX_AGENT); h1 = __hip_atomic_load(hs + hstr, RLX_AGENT);
                if (__all(((e >> 16) == tag) & ((h0 >> 16) == tag) & ((h1 >> 16) == tag))) break;
                __builtin_amdgcn_s_sleep(8);
            }
            *(LAS u16*)(L + L_H + lane * 2) = (u16)h0;
            *(LAS u16*)(L + L_H + 128 + lane * 2) = (u16)h1;
        }
        wait_vm<0>();
        if (do_m2) {
#pragma unroll
            for (int cb = 0; cb < 4; ++cb) {
                f32x4 mp = {0.f, 0.f, 0.f, 0.f};
#pragma unroll
                for (int j = 0; j < 2; ++j) mp = mfma32(tr_frag(L + L_W2 + 32 * cb, 128, 32 * j, lane), *(const LAS v4u*)(L + L_H + (n & 1) * 128 + 64 * j + 16 * g), mp);
                if (n < 2) __builtin_amdgcn_raw_buffer_store_b128(v4u{no_sent(mp[0]), no_sent(mp[1]), no_sent(mp[2]), no_sent(mp[3])}, rws, myslot + SL_PM + n * 256 + cb * 64 + g * 16, 0, AUX_SC1);
            }
        }
        STAMP(5);
        wg_barrier();
        wg_barrier();
        STAMP(6);
        {
            const int i = lane & 31;
            const bool live = lane < 60;
            const int pb = ((i >> 1) & 1) * 8 + (i >> 2), ps = cg + 8 * (i & 1);
            const u32 off = live ? WS_SLOT + (pb * 16 + ps) * 4096 + (lane < 32 ? SL_PS : SL_PM) + fb * 256 + (hi & 15) * 16 : 0xFFFFFF00u;
            v4u a;
#pragma unroll 1
            for (u32 spin = 0; spin < SPIN_CAP; ++spin) {
                a = __builtin_amdgcn_raw_buffer_load_b128(rws, off, 0, AUX_SC1);
                if (__all(umax4(a) != SENT)) break;
                __builtin_amdgcn_s_sleep(1);
            }
            STAMP(7);
            float r[4];
#pragma unroll
            for (int k = 0; k < 4; ++k) {
                const float t = x16_sum(row_sum(__uint_as_float(a[k])));
                auto sw = __builtin_amdgcn_permlane32_swap(__float_as_uint(t), __float_as_uint(t), false, false);
                const float sv = rbf(__uint_as_float(sw[0])), mv = __uint_as_float(sw[1]);
                const u32 xw = k < 2 ? xo.x : xo.y, bw = k < 2 ? b2o.x : b2o.y;
                const float xv = (k & 1) ? bf_hi(xw) : bf_lo(xw), bv = (k & 1) ? bf_hi(bw) : bf_lo(bw);
                const float mm = rbf(rbf(mv) + bv);
                const float gt = rbf(mm / (1.f + __expf(-mm)));
                r[k] = xv + rbf(sv * gt);
            }
            if (lane == 0) {
                *(GAS v2u*)(p.out + fb * DM + fc) = v2u{pk_bf16(r[0], r[1]), pk_bf16(r[2], r[3])};
                if (bid == 0) __hip_atomic_store((GAS u64*)(p.ws + WS_EPOCH), (((u64)ep.y << 32) | ep.x) + 1, RLX_AGENT);
            }
        }
        STAMP(8);
        STAMPS_OUT(K_NSTAMP_H);
    }
}

extern "C" void kernel_launch(void* const* d_in, const int* in_sizes, int n_in, void* d_out, int out_size, void* d_ws, size_t ws_size,
                              hipStream_t stream) {
    Params p;
    p.x = (const u16*)d_in[0]; p.k = (const u16*)d_in[1]; p.v = (const u16*)d_in[2]; p.wq = (const u16*)d_in[3]; p.wo = (const u16*)d_in[4];
    p.w1 = (const u16*)d_in[6]; p.b1 = (const u16*)d_in[7]; p.w2 = (const u16*)d_in[8]; p.b2 = (const u16*)d_in[9];
    p.out = (u16*)d_out; p.ws = (unsigned char*)d_ws;
    constexpr int LDS_BYTES = 96 * 1024;
    static_assert(L_END <= LDS_BYTES, "");
    (void)hipFuncSetAttribute((const void*)fused_decode, hipFuncAttributeMaxDynamicSharedMemorySize, LDS_BYTES);
    hipLaunchKernelGGL(fused_decode, dim3(NWG), dim3(NTHR), LDS_BYTES, stream, p);
}
```

```cpp
#include <hip/hip_runtime.h>
#include <hip/hip_bf16.h>
#include <stdint.h>

#define GAS __attribute__((address_space(1)))
#define LAS __attribute__((address_space(3)))
typedef unsigned u32;
typedef unsigned long long u64;
typedef unsigned short u16;
typedef unsigned v4u __attribute__((ext_vector_type(4)));
typedef unsigned v2u __attribute__((ext_vector_type(2)));
typedef float f32x4 __attribute__((ext_vector_type(4)));
typedef float f32x2 __attribute__((ext_vector_type(2)));
typedef short s16x4 __attribute__((ext_vector_type(4)));
typedef __bf16 bf16x8 __attribute__((ext_vector_type(8)));
typedef __bf16 bf16x2 __attribute__((ext_vector_type(2)));

#define K_DEPTH 6
#define K_PACE 2
#define K_INTER 0
#define K_AUXK 2
#define K_AUXV 2
#define K_TS 0

constexpr int DM = 512, HD = 128, SK = 32768, FF = 1792;
constexpr int NWG = 256, NSW = 4, NTHR = (NSW + 1) * 64;
constexpr int DEPTH = K_DEPTH, PACE = K_PACE;
constexpr int NTILE = SK / 16 / 16 / NSW;
constexpr float SC = 0.08838834764831845f * 1.4426950408889634f;
constexpr u32 SENT = 0xFFFFFFFFu;
constexpr u32 SPIN_CAP = 1u << 18;

constexpr u32 WS_EPOCH = 0;
constexpr u32 WS_QG = 4096;
constexpr u32 WS_HG = WS_QG + 8 * 2 * 512 * 8;
constexpr u32 WS_SLOT = 131072;
constexpr u32 SL_PS = 2048, SL_PM = 2560, SL_ML = 3072;
constexpr u32 WS_END = WS_SLOT + NWG * 4096;
constexpr u32 WS_TS = WS_END;
static_assert(WS_HG + 2 * FF * 8 <= WS_SLOT, "");

constexpr u32 L_WQ = 0, L_W1 = 16384, L_WO = 32768, L_W2 = 49152;
constexpr u32 L_XN = 57344, L_Y = L_XN + 2048, L_Q = L_Y + 2048, L_H = L_Q + 1024, L_O = L_H + 256;
constexpr u32 L_MRG = L_O + 512;
constexpr u32 L_MML = L_MRG + NSW * 4 * 132 * 4;
constexpr u32 L_END = L_MML + NSW * 4 * 2 * 4;

struct Params {
    const u16 *x, *k, *v, *wq, *wo, *w1, *b1, *w2, *b2;
    u16* out;
    unsigned char* ws;
};

__device__ __forceinline__ f32x4 mfma32(v4u a, v4u b, f32x4 c) {
    return __builtin_amdgcn_mfma_f32_16x16x32_bf16(__builtin_bit_cast(bf16x8, a), __builtin_bit_cast(bf16x8, b), c, 0, 0, 0);
}
__device__ __forceinline__ f32x4 mfma16(u32 a0, u32 a1, u32 b0, u32 b1, f32x4 c) {
    return __builtin_amdgcn_mfma_f32_16x16x16bf16_1k(__builtin_bit_cast(s16x4, v2u{a0, a1}), __builtin_bit_cast(s16x4, v2u{b0, b1}), c, 0, 0, 0);
}
__device__ __forceinline__ u32 pk_bf16(float lo, float hi) {
    return __builtin_bit_cast(u32, __builtin_convertvector(f32x2{lo, hi}, bf16x2));
}
__device__ __forceinline__ float bf_lo(u32 w) { return __builtin_bit_cast(float, w << 16); }
__device__ __forceinline__ float bf_hi(u32 w) { return __builtin_bit_cast(float, w & 0xffff0000u); }
__device__ __forceinline__ float rbf(float f) { return bf_lo(pk_bf16(f, f)); }
__device__ __forceinline__ float ex2(float x) { return __builtin_amdgcn_exp2f(x); }
template <int CTRL> __device__ __forceinline__ float dpp(float x) {
    return __builtin_bit_cast(float, __builtin_amdgcn_mov_dpp(__builtin_bit_cast(int, x), CTRL, 0xf, 0xf, true));
}
constexpr int XOR1 = 0xB1, XOR2 = 0x4E, XOR7 = 0x141, ROR8 = 0x128;
__device__ __forceinline__ float x16_max(float x) {
    auto s = __builtin_amdgcn_permlane16_swap(__float_as_uint(x), __float_as_uint(x), false, false);
    return fmaxf(__uint_as_float(s[0]), __uint_as_float(s[1]));
}
__device__ __forceinline__ float x32_max(float x) {
    auto s = __builtin_amdgcn_permlane32_swap(__float_as_uint(x), __float_as_uint(x), false, false);
    return fmaxf(__uint_as_float(s[0]), __uint_as_float(s[1]));
}
__device__ __forceinline__ float x16_sum(float x) {
    auto s = __builtin_amdgcn_permlane16_swap(__float_as_uint(x), __float_as_uint(x), false, false);
    return __uint_as_float(s[0]) + __uint_as_float(s[1]);
}
__device__ __forceinline__ float x32_sum(float x) {
    auto s = __builtin_amdgcn_permlane32_swap(__float_as_uint(x), __float_as_uint(x), false, false);
    return __uint_as_float(s[0]) + __uint_as_float(s[1]);
}
__device__ __forceinline__ float row_sum(float x) {
    x += dpp<XOR1>(x); x += dpp<XOR2>(x); x += dpp<XOR7>(x); x += dpp<ROR8>(x); return x;
}
__device__ __forceinline__ float wave_sum(float x) { return x32_sum(x16_sum(row_sum(x))); }
__device__ __forceinline__ u32 umax4(v4u a) { return max(max(a.x, a.y), max(a.z, a.w)); }
__device__ __forceinline__ u32 no_sent(float f) { const u32 u = __float_as_uint(f); return u == SENT ? 0x7FC00000u : u; }

#define RLX_AGENT __ATOMIC_RELAXED, __HIP_MEMORY_SCOPE_AGENT
constexpr int AUX_NT = 2, AUX_SC1 = 16;
__device__ __forceinline__ __amdgpu_buffer_rsrc_t mk_rsrc(const void* p, u32 bytes) {
    return __builtin_amdgcn_make_buffer_rsrc((void*)p, 0, bytes, 0x00020000);
}
__device__ __forceinline__ void glds_nt(const void* src, u32 dst) {
    asm volatile("s_mov_b32 m0, %1\n\ts_nop 0\n\tglobal_load_lds_dwordx4 %0, off nt" ::"v"(src), "s"(dst) : "memory");
}
template <int N> __device__ __forceinline__ void wait_vm() { asm volatile("s_waitcnt vmcnt(%0)" ::"i"(N) : "memory"); }
__device__ __forceinline__ void wg_barrier() { asm volatile("s_waitcnt lgkmcnt(0)\n\ts_barrier" ::: "memory"); }
__device__ __forceinline__ v2u tr_read(const LAS unsigned char* p) {
    return __builtin_bit_cast(v2u, __builtin_amdgcn_ds_read_tr16_b64_v4i16((LAS s16x4*)p));
}
__device__ __forceinline__ v4u tr_frag(const LAS unsigned char* img, int pitch, int row0, int lane) {
    const int g = lane >> 4, q = (lane >> 2) & 3, p = lane & 3;
    const LAS unsigned char* a = img + (row0 + 8 * g + q) * pitch + 8 * p;
    const v2u lo = tr_read(a), hi = tr_read(a + 4 * pitch);
    return v4u{lo.x, lo.y, hi.x, hi.y};
}
#define STAMP(i) do { } while (0)
#define STAMPS_OUT(N) do { } while (0)

__device__ __forceinline__ f32x4 gemv16(const LAS unsigned char* img, const LAS unsigned char* act, int lane) {
    const int g = lane >> 4, n = lane & 15;
    f32x4 a0 = {0.f, 0.f, 0.f, 0.f}, a1 = {0.f, 0.f, 0.f, 0.f};
    const LAS unsigned char* ap = act + (n & 1) * 1024 + g * 16;
#pragma unroll
    for (int s = 0; s < 16; s += 2) {
        a0 = mfma32(tr_frag(img, 32, 32 * s, lane), *(const LAS v4u*)(ap + 64 * s), a0);
        a1 = mfma32(tr_frag(img, 32, 32 * s + 32, lane), *(const LAS v4u*)(ap + 64 * s + 64), a1);
    }
    return a0 + a1;
}
__device__ __forceinline__ float sel4(f32x4 v, int r) { return r == 0 ? v[0] : r == 1 ? v[1] : r == 2 ? v[2] : v[3]; }

__global__ void __launch_bounds__(NTHR, 2) fused_decode(Params p) {
    extern __shared__ __attribute__((aligned(16))) unsigned char lds_raw[];
    LAS unsigned char* L = (LAS unsigned char*)lds_raw;
    const int tid = threadIdx.x, lane = tid & 63, w = __builtin_amdgcn_readfirstlane(tid >> 6);
    const int bid = blockIdx.x;
    const int pair = bid >> 4, b = pair >> 3, kvh = pair & 7, s = bid & 15;
    const int hh = 2 * b + (s >> 3), cg = s & 7, hi = 4 * kvh + hh;
    const int g = lane >> 4, n = lane & 15;
    const __amdgpu_buffer_rsrc_t rws = mk_rsrc(p.ws, WS_END);
    const u32 myslot = WS_SLOT + bid * 4096;
    STAMP(0);

    if (w < NSW) {
        const __amdgpu_buffer_rsrc_t rk = K_INTER ? mk_rsrc(p.k + (size_t)pair * SK * HD, SK * HD * 2) : mk_rsrc(p.k + ((size_t)pair * SK + s * 2048) * HD, 2048 * HD * 2);
        const __amdgpu_buffer_rsrc_t rv = K_INTER ? mk_rsrc(p.v + (size_t)pair * SK * HD, SK * HD * 2) : mk_rsrc(p.v + ((size_t)pair * SK + s * 2048) * HD, 2048 * HD * 2);
        const u32 t0 = (K_INTER ? s * NSW + w : w) * 4096;
        constexpr u32 TSTR = (K_INTER ? 16 * NSW : NSW) * 4096;
        u32 ko = t0 + (4 * (n & 3) + (n >> 2)) * 256 + g * 16, vo = t0 + lane * 16;
        v4u Kr[DEPTH][4], Vr[DEPTH][4];
#define ISSUE(d)                                                                                               \
    do {                                                                                                       \
        _Pragma("unroll") for (int j = 0; j < 4; ++j) Kr[d][j] = __builtin_amdgcn_raw_buffer_load_b128(rk, ko + 64 * j, 0, K_AUXK);   \
        _Pragma("unroll") for (int j = 0; j < 4; ++j) Vr[d][j] = __builtin_amdgcn_raw_buffer_load_b128(rv, vo + 1024 * j, 0, K_AUXV); \
        ko += TSTR; vo += TSTR;                                                                                \
        __builtin_amdgcn_sched_barrier(0);                                                                     \
    } while (0)
        wg_barrier();
#pragma unroll
        for (int d = 0; d < DEPTH; ++d) {
            if (d >= PACE) { asm volatile("" ::"v"(Vr[d - PACE][3])); __builtin_amdgcn_sched_barrier(0); }
            ISSUE(d);
        }
        STAMP(1);
        wg_barrier();
        STAMP(2);
        v4u Q[4];
#pragma unroll
        for (int j = 0; j < 4; ++j) Q[j] = *(const LAS v4u*)(L + L_Q + (n >> 2) * 256 + 64 * j + 16 * g);
        const int jj = n & 3;
        const u32 mask0 = jj == 0 ? 0x0000ffffu : jj == 1 ? 0xffff0000u : 0u, mask1 = jj == 2 ? 0x0000ffffu : jj == 3 ? 0xffff0000u : 0u;
        float m = -INFINITY, l = 0.f;
        f32x4 acc0 = {0.f, 0.f, 0.f, 0.f}, acc1 = {0.f, 0.f, 0.f, 0.f};
#define TILE(d)                                                                                                \
    do {                                                                                                       \
        f32x4 sc = {0.f, 0.f, 0.f, 0.f};                                                                       \
        _Pragma("unroll") for (int j = 0; j < 4; ++j) sc = mfma32(Kr[d][j], Q[j], sc);                         \
        const float tm = x32_max(x16_max(fmaxf(fmaxf(sc[0], sc[1]), fmaxf(sc[2], sc[3]))));                    \
        const float mn = fmaxf(m, tm), mc = mn * SC, alpha = ex2((m - mn) * SC);                               \
        float pr[4];                                                                                           \
        _Pragma("unroll") for (int r = 0; r < 4; ++r) pr[r] = ex2(fmaf(sc[r], SC, -mc));                       \
        l = fmaf(l, alpha, (pr[0] + pr[1]) + (pr[2] + pr[3]));                                                 \
        m = mn;                                                                                                \
        acc0 *= alpha; acc1 *= alpha;                                                                          \
        _Pragma("unroll") for (int r = 0; r < 4; ++r) {                                                        \
            const u32 pb = pk_bf16(pr[r], pr[r]), x0 = pb & mask0, x1 = pb & mask1;                            \
            acc0 = mfma16(Vr[d][r].x, Vr[d][r].y, x0, x1, acc0);                                               \
            acc1 = mfma16(Vr[d][r].z, Vr[d][r].w, x0, x1, acc1);                                               \
        }                                                                                                      \
        __builtin_amdgcn_sched_barrier(0);                                                                     \
    } while (0)
#pragma unroll 1
        for (int it = 0; it < NTILE / DEPTH; ++it) {
#pragma unroll
            for (int d = 0; d < DEPTH; ++d) { TILE(d); ISSUE(d); }
        }
#pragma unroll
        for (int d = 0; d < NTILE % DEPTH; ++d) TILE(d);
#undef TILE
#undef ISSUE
        STAMP(3);
        l = x32_sum(x16_sum(l));
        {
            LAS float* mr = (LAS float*)(L + L_MRG) + (w * 4 + (n >> 2)) * 132 + 32 * g + jj;
#pragma unroll
            for (int r = 0; r < 4; ++r) { mr[8 * r] = acc0[r]; mr[8 * r + 4] = acc1[r]; }
            if (g == 0 && jj == 0) *(LAS f32x2*)(L + L_MML + (w * 4 + (n >> 2)) * 8) = f32x2{m * SC, l};
        }
        wg_barrier();
        {
            f32x2 ml[NSW];
#pragma unroll
            for (int u = 0; u < NSW; ++u) ml[u] = *(const LAS f32x2*)(L + L_MML + (u * 4 + w) * 8);
            float M = ml[0].x;
#pragma unroll
            for (int u = 1; u < NSW; ++u) M = fmaxf(M, ml[u].x);
            float Ls = 0.f; f32x4 o = {0.f, 0.f, 0.f, 0.f};
#pragma unroll
            for (int u = 0; u < NSW; ++u) {
                const float f = ex2(ml[u].x - M);
                Ls = fmaf(f, ml[u].y, Ls);
                o += f * *(const LAS f32x4*)(L + L_MRG + ((u * 4 + w) * 132 + 4 * (lane & 31)) * 4);
            }
            if (lane < 32) __builtin_amdgcn_raw_buffer_store_b128(v4u{no_sent(o[0]), no_sent(o[1]), no_sent(o[2]), no_sent(o[3])}, rws, myslot + w * 512 + lane * 16, 0, AUX_SC1);
            else if (lane == 32) __builtin_amdgcn_raw_buffer_store_b64(v2u{no_sent(M), no_sent(Ls)}, rws, myslot + SL_ML + w * 8, 0, AUX_SC1);
        }
        STAMP(4);
        v4u WO[4];
#pragma unroll
        for (int j = 0; j < 4; ++j) WO[j] = tr_frag(L + L_WO + 32 * w, 128, 32 * j, lane);
        {
            const int bb = w & 1, dh = w >> 1;
            const u32 src = WS_SLOT + (((bb * 8 + kvh) * 16) + 4 * g) * 4096;
            const u32 ao = src + hh * 512 + dh * 256 + n * 16, mo = src + SL_ML + hh * 8;
            v4u a[4]; v2u q2[4];
#pragma unroll 1
            for (u32 spin = 0; spin < SPIN_CAP; ++spin) {
                u32 mx = 0;
#pragma unroll
                for (int k = 0; k < 4; ++k) { a[k] = __builtin_amdgcn_raw_buffer_load_b128(rws, ao + k * 4096, 0, AUX_SC1); q2[k] = __builtin_amdgcn_raw_buffer_load_b64(rws, mo + k * 4096, 0, AUX_SC1); }
#pragma unroll
                for (int k = 0; k < 4; ++k) mx = max(mx, max(umax4(a[k]), max(q2[k].x, q2[k].y)));
                if (__all(mx != SENT)) break;
                __builtin_amdgcn_s_sleep(2);
            }
            STAMP(5);
            float M = fmaxf(fmaxf(__uint_as_float(q2[0].x), __uint_as_float(q2[1].x)), fmaxf(__uint_as_float(q2[2].x), __uint_as_float(q2[3].x)));
            M = x32_max(x16_max(M));
            float Ls = 0.f; f32x4 o = {0.f, 0.f, 0.f, 0.f};
#pragma unroll
            for (int k = 0; k < 4; ++k) {
                const float f = ex2(__uint_as_float(q2[k].x) - M);
                Ls = fmaf(f, __uint_as_float(q2[k].y), Ls);
                o += f * __builtin_bit_cast(f32x4, a[k]);
            }
            Ls = x32_sum(x16_sum(Ls));
            const float inv = 1.f / Ls;
#pragma unroll
            for (int r = 0; r < 4; ++r) o[r] = x32_sum(x16_sum(o[r])) * inv;
            if (g == 0) *(LAS v2u*)(L + L_O + bb * 256 + dh * 128 + n * 8) = v2u{pk_bf16(o[0], o[1]), pk_bf16(o[2], o[3])};
        }
        wg_barrier();
        {
            f32x4 sp = {0.f, 0.f, 0.f, 0.f};
#pragma unroll
            for (int j = 0; j < 4; ++j) sp = mfma32(WO[j], *(const LAS v4u*)(L + L_O + (n & 1) * 256 + 64 * j + 16 * g), sp);
            if (n < 2) __builtin_amdgcn_raw_buffer_store_b128(v4u{no_sent(sp[0]), no_sent(sp[1]), no_sent(sp[2]), no_sent(sp[3])}, rws, myslot + SL_PS + n * 256 + w * 64 + g * 16, 0, AUX_SC1);
        }
        STAMP(6);
        STAMPS_OUT(K_NSTAMP_S);
    } else {
        __builtin_amdgcn_s_setprio(3);
        const u32 lb = (u32)(size_t)L;
        v4u xr0, xr1; v2u ep;
        {
            const void* xa = p.x + 8 * lane;
            const void* ea = p.ws + WS_EPOCH;
            asm volatile("global_load_dwordx4 %0, %3, off\n\tglobal_load_dwordx4 %1, %3, off offset:1024\n\tglobal_load_dwordx2 %2, %4, off sc1"
                         : "=&v"(xr0), "=&v"(xr1), "=&v"(ep) : "v"(xa), "v"(ea) : "memory");
        }
        {
            const int jq = 4 * cg + hh;
            const u16* src = p.wq + (size_t)(lane >> 1) * 4096 + kvh * 512 + 16 * jq + 8 * (lane & 1);
#pragma unroll
            for (int u = 0; u < 16; ++u) glds_nt(src + (size_t)u * 32 * 4096, lb + L_WQ + u * 1024);
        }
        {
            const v4u sv = {SENT, SENT, SENT, SENT};
#pragma unroll
            for (int u = 0; u < 3; ++u) __builtin_amdgcn_raw_buffer_store_b128(sv, rws, myslot + u * 1024 + lane * 16, 0, AUX_SC1);
            __builtin_amdgcn_raw_buffer_store_b128(sv, rws, lane < 2 ? myslot + SL_ML + lane * 16 : 0xFFFFFF00u, 0, AUX_SC1);
        }
        asm volatile("s_barrier" ::: "memory");
        const bool do_m1 = hi >= 16 && cg < 7, do_m2 = hi < 28;
        const int jm = (cg * 4 + ((hi - 16) >> 2)) * 4 + (hi & 3);
        asm volatile("s_waitcnt vmcnt(20)" : "+v"(xr0), "+v"(xr1), "+v"(ep)::"memory");
        STAMP(1);
        const u64 tag = ((((u64)ep.y << 32) | ep.x) + 1) & 0xFFFFFFFFFFFFull;
        {
            float v[2][8];
#pragma unroll
            for (int j = 0; j < 4; ++j) { v[0][2 * j] = bf_lo(xr0[j]); v[0][2 * j + 1] = bf_hi(xr0[j]); v[1][2 * j] = bf_lo(xr1[j]); v[1][2 * j + 1] = bf_hi(xr1[j]); }
#pragma unroll
            for (int pass = 0; pass < 2; ++pass) {
#pragma unroll
                for (int bb = 0; bb < 2; ++bb) {
                    float su = 0.f;
#pragma unroll
                    for (int j = 0; j < 8; ++j) su += v[bb][j];
                    const float mean = wave_sum(su) * (1.f / DM);
                    float sq = 0.f;
#pragma unroll
                    for (int j = 0; j < 8; ++j) { const float d = v[bb][j] - mean; sq = fmaf(d, d, sq); }
                    const float var = wave_sum(sq) * (1.f / DM);
                    const float rs = 1.f / sqrtf(var + 1e-5f);
                    v4u o;
#pragma unroll
                    for (int j = 0; j < 4; ++j) {
                        o[j] = pk_bf16((v[bb][2 * j] - mean) * rs, (v[bb][2 * j + 1] - mean) * rs);
                        v[bb][2 * j] = bf_lo(o[j]); v[bb][2 * j + 1] = bf_hi(o[j]);
                    }
                    *(LAS v4u*)(L + (pass ? L_Y : L_XN) + bb * 1024 + lane * 16) = o;
                }
            }
        }
        wait_vm<4>();
        STAMP(2);
        {
            const f32x4 qv = gemv16(L + L_WQ, L + L_XN, lane);
            const u32 qb = pk_bf16(sel4(qv, n >> 1), 0.f) & 0xffffu;
            GAS u64* dst = (GAS u64*)(p.ws + WS_QG) + (kvh * 2 + (n & 1)) * 512 + 16 * (4 * cg + hh) + 4 * g + (n >> 1);
            STAMP(9);
            wait_vm<0>();
            STAMP(10);
            if (n < 8) __hip_atomic_store(dst, (tag << 16) | qb, RLX_AGENT);
        }
        STAMP(3);
        {
            GAS u64* src = (GAS u64*)(p.ws + WS_QG) + (kvh * 2 + b) * 512 + lane;
            u64 A[8], B[8], C[8];
#define POLL(X) do { _Pragma("unroll") for (int k = 0; k < 8; ++k) X[k] = __hip_atomic_load(src + 64 * k, RLX_AGENT); __builtin_amdgcn_sched_barrier(0); } while (0)
#define GOOD(X) ({ bool ok_ = true; _Pragma("unroll") for (int k = 0; k < 8; ++k) ok_ &= (X[k] >> 16) == tag; __all(ok_); })
#define TAKE(X) do { _Pragma("unroll") for (int k = 0; k < 8; ++k) *(LAS u16*)(L + L_Q + (64 * k + lane) * 2) = (u16)X[k]; } while (0)
            POLL(A); __builtin_amdgcn_s_sleep(12); POLL(B); __builtin_amdgcn_s_sleep(12); POLL(C);
#pragma unroll 1
            for (u32 spin = 0; spin < SPIN_CAP; ++spin) {
                if (GOOD(A)) { TAKE(A); break; }
                POLL(A);
                if (GOOD(B)) { TAKE(B); break; }
                POLL(B);
                if (GOOD(C)) { TAKE(C); break; }
                POLL(C);
            }
#undef POLL
#undef GOOD
#undef TAKE
        }
        STAMP(4);
        wg_barrier();
        if (do_m1) {
            const u16* src = p.w1 + (size_t)(lane >> 1) * FF + 16 * jm + 8 * (lane & 1);
#pragma unroll
            for (int u = 0; u < 16; ++u) glds_nt(src + (size_t)u * 32 * FF, lb + L_W1 + u * 1024);
        }
        {
            const u16* src = p.wo + (size_t)(hi * 128 + (lane >> 3)) * DM + 64 * cg + 8 * (lane & 7);
#pragma unroll
            for (int u = 0; u < 16; ++u) glds_nt(src + (size_t)u * 8 * DM, lb + L_WO + u * 1024);
        }
        if (do_m2) {
            const u16* src = p.w2 + (size_t)(hi * 64 + (lane >> 3)) * DM + 64 * cg + 8 * (lane & 7);
#pragma unroll
            for (int u = 0; u < 8; ++u) glds_nt(src + (size_t)u * 8 * DM, lb + L_W2 + u * 1024);
        }
        if (do_m1) {
            const int col = 16 * jm + 4 * g + ((n >> 1) & 3);
            const float bias = bf_lo(p.b1[col]);
            wait_vm<0>();
            const float u = rbf(rbf(sel4(gemv16(L + L_W1, L + L_Y, lane), n >> 1)) + bias);
            const float z = 1.5957691216057308f * fmaf(0.044715f * u * u, u, u);
            const float h = u / (1.f + __expf(-z));
            if (n < 8) __hip_atomic_store((GAS u64*)(p.ws + WS_HG) + (n & 1) * FF + col, (tag << 16) | (pk_bf16(h, 0.f) & 0xffffu), RLX_AGENT);
        }
        const int fb = hi >> 4, fc = 64 * cg + 4 * (hi & 15);
        const v2u xo = *(const GAS v2u*)(p.x + fb * DM + fc), b2o = *(const GAS v2u*)(p.b2 + fc);
        {
            const int i = lane & 31;
            GAS u64* ev = (GAS u64*)(p.ws + WS_QG) + ((i >> 2) * 2) * 512 + 16 * (4 * cg + (i & 3));
            GAS u64* hs = do_m2 ? (GAS u64*)(p.ws + WS_HG) + 64 * hi + lane : ev;
            const int hstr = do_m2 ? FF : 0;
            u64 e, h0, h1;
#pragma unroll 1
            for (u32 spin = 0; spin < SPIN_CAP; ++spin) {
                e = __hip_atomic_load(ev, RLX_AGENT); h0 = __hip_atomic_load(hs, RLX_AGENT); h1 = __hip_atomic_load(hs + hstr, RLX_AGENT);
                if (__all(((e >> 16) == tag) & ((h0 >> 16) == tag) & ((h1 >> 16) == tag))) break;
                __builtin_amdgcn_s_sleep(8);
            }
            *(LAS u16*)(L + L_H + lane * 2) = (u16)h0;
            *(LAS u16*)(L + L_H + 128 + lane * 2) = (u16)h1;
        }
        wait_vm<0>();
        if (do_m2) {
#pragma unroll
            for (int cb = 0; cb < 4; ++cb) {
                f32x4 mp = {0.f, 0.f, 0.f, 0.f};
#pragma unroll
                for (int j = 0; j < 2; ++j) mp = mfma32(tr_frag(L + L_W2 + 32 * cb, 128, 32 * j, lane), *(const LAS v4u*)(L + L_H + (n & 1) * 128 + 64 * j + 16 * g), mp);
                if (n < 2) __builtin_amdgcn_raw_buffer_store_b128(v4u{no_sent(mp[0]), no_sent(mp[1]), no_sent(mp[2]), no_sent(mp[3])}, rws, myslot + SL_PM + n * 256 + cb * 64 + g * 16, 0, AUX_SC1);
            }
        }
        STAMP(5);
        wg_barrier();
        wg_barrier();
        STAMP(6);
        {
            const int i = lane & 31;
            const bool live = lane < 60;
            const int pb = ((i >> 1) & 1) * 8 + (i >> 2), ps = cg + 8 * (i & 1);
            const u32 off = live ? WS_SLOT + (pb * 16 + ps) * 4096 + (lane < 32 ? SL_PS : SL_PM) + fb * 256 + (hi & 15) * 16 : 0xFFFFFF00u;
            v4u a;
#pragma unroll 1
            for (u32 spin = 0; spin < SPIN_CAP; ++spin) {
                a = __builtin_amdgcn_raw_buffer_load_b128(rws, off, 0, AUX_SC1);
                if (__all(umax4(a) != SENT)) break;
                __builtin_amdgcn_s_sleep(1);
            }
            STAMP(7);
            float r[4];
#pragma unroll
            for (int k = 0; k < 4; ++k) {
                const float t = x16_sum(row_sum(__uint_as_float(a[k])));
                auto sw = __builtin_amdgcn_permlane32_swap(__float_as_uint(t), __float_as_uint(t), false, false);
                const float sv = rbf(__uint_as_float(sw[0])), mv = __uint_as_float(sw[1]);
                const u32 xw = k < 2 ? xo.x : xo.y, bw = k < 2 ? b2o.x : b2o.y;
                const float xv = (k & 1) ? bf_hi(xw) : bf_lo(xw), bv = (k & 1) ? bf_hi(bw) : bf_lo(bw);
                const float mm = rbf(rbf(mv) + bv);
                const float gt = rbf(mm / (1.f + __expf(-mm)));
                r[k] = xv + rbf(sv * gt);
            }
            if (lane == 0) {
                *(GAS v2u*)(p.out + fb * DM + fc) = v2u{pk_bf16(r[0], r[1]), pk_bf16(r[2], r[3])};
                if (bid == 0) __hip_atomic_store((GAS u64*)(p.ws + WS_EPOCH), (((u64)ep.y << 32) | ep.x) + 1, RLX_AGENT);
            }
        }
        STAMP(8);
        STAMPS_OUT(K_NSTAMP_H);
    }
}

extern "C" void kernel_launch(void* const* d_in, const int* in_sizes, int n_in, void* d_out, int out_size, void* d_ws, size_t ws_size,
                              hipStream_t stream) {
    Params p;
    p.x = (const u16*)d_in[0]; p.k = (const u16*)d_in[1]; p.v = (const u16*)d_in[2]; p.wq = (const u16*)d_in[3]; p.wo = (const u16*)d_in[4];
    p.w1 = (const u16*)d_in[6]; p.b1 = (const u16*)d_in[7]; p.w2 = (const u16*)d_in[8]; p.b2 = (const u16*)d_in[9];
    p.out = (u16*)d_out; p.ws = (unsigned char*)d_ws;
    constexpr int LDS_BYTES = 96 * 1024;
    static_assert(L_END <= LDS_BYTES, "");
    (void)hipFuncSetAttribute((const void*)fused_decode, hipFuncAttributeMaxDynamicSharedMemorySize, LDS_BYTES);
    hipLaunchKernelGGL(fused_decode, dim3(NWG), dim3(NTHR), LDS_BYTES, stream, p);
}
```

```cpp
#include <hip/hip_runtime.h>
#include <hip/hip_bf16.h>
#include <stdint.h>

#define GAS __attribute__((address_space(1)))
#define LAS __attribute__((address_space(3)))
typedef unsigned u32;
typedef unsigned long long u64;
typedef unsigned short u16;
typedef unsigned v4u __attribute__((ext_vector_type(4)));
typedef unsigned v2u __attribute__((ext_vector_type(2)));
typedef float f32x4 __attribute__((ext_vector_type(4)));
typedef float f32x2 __attribute__((ext_vector_type(2)));
typedef short s16x4 __attribute__((ext_vector_type(4)));
typedef __bf16 bf16x8 __attribute__((ext_vector_type(8)));
typedef __bf16 bf16x2 __attribute__((ext_vector_type(2)));

#define K_WK 6
#define K_WV 7
#define K_WQ 8
#define K_W0 6
#define K_S 25
#define K_SQ0 100
#define K_SQ1 24
#define K_SH0 64
#define K_SH1 64
#define K_WW1 3
#define K_WWO 4
#define K_SHNT 0
#define K_NOWQ 0
#define K_B0 0
#define K_TS 0

constexpr int DM = 512, HD = 128, SK = 32768, FF = 1792;
constexpr int NWG = 256, NSW = 4, NTHR = (NSW + 1) * 64;
constexpr int RP = 4, PARK = 2;
constexpr int NST = K_S;
constexpr int NROUND = (NST - RP - PARK - 3) / 2;
constexpr int POOL = SK / 16 / NSW - 16 * NST;
static_assert(NROUND * 2 + RP + PARK + 3 == NST && NROUND >= 1 && POOL > 0, "");
constexpr float SC = 0.08838834764831845f * 1.4426950408889634f;
constexpr u32 SENT = 0xFFFFFFFFu;
constexpr u32 SPIN_CAP = 1u << 18;
constexpr u32 OOR = 0xFFFFFF00u;

constexpr u32 WS_EPOCH = 0;
constexpr u32 WS_QG = 4096;
constexpr u32 WS_HG = WS_QG + 8 * 2 * 512 * 8;
constexpr u32 WS_TK = 102400;
constexpr u32 WS_SLOT = 131072;
constexpr u32 SL_PS = 2048, SL_PM = 2560, SL_ML = 3072;
constexpr u32 WS_END = WS_SLOT + NWG * 4096;
constexpr u32 WS_TS = WS_END;
static_assert(WS_HG + 2 * FF * 8 <= WS_TK && WS_TK + 64 * 256 <= WS_SLOT, "");

constexpr u32 L_WQ = 0, L_W1 = 16384, L_WO = 32768, L_W2 = 49152;
constexpr u32 L_XN = 57344, L_Y = L_XN + 2048, L_Q = L_Y + 2048, L_H = L_Q + 1024, L_O = L_H + 256;
constexpr u32 L_MRG = L_O + 512;
constexpr u32 L_MML = L_MRG + NSW * 4 * 132 * 4;
constexpr u32 L_PARK = 73728;
constexpr u32 L_END = L_PARK + NSW * PARK * 8192;
static_assert(L_MML + NSW * 4 * 2 * 4 <= L_PARK, "");

struct Params {
    const u16 *x, *k, *v, *wq, *wo, *w1, *b1, *w2, *b2;
    u16* out;
    unsigned char* ws;
};

__device__ __forceinline__ f32x4 mfma32(v4u a, v4u b, f32x4 c) {
    return __builtin_amdgcn_mfma_f32_16x16x32_bf16(__builtin_bit_cast(bf16x8, a), __builtin_bit_cast(bf16x8, b), c, 0, 0, 0);
}
__device__ __forceinline__ f32x4 mfma16(u32 a0, u32 a1, u32 b0, u32 b1, f32x4 c) {
    return __builtin_amdgcn_mfma_f32_16x16x16bf16_1k(__builtin_bit_cast(s16x4, v2u{a0, a1}), __builtin_bit_cast(s16x4, v2u{b0, b1}), c, 0, 0, 0);
}
__device__ __forceinline__ u32 pk_bf16(float lo, float hi) {
    return __builtin_bit_cast(u32, __builtin_convertvector(f32x2{lo, hi}, bf16x2));
}
__device__ __forceinline__ float bf_lo(u32 w) { return __builtin_bit_cast(float, w << 16); }
__device__ __forceinline__ float bf_hi(u32 w) { return __builtin_bit_cast(float, w & 0xffff0000u); }
__device__ __forceinline__ float rbf(float f) { return bf_lo(pk_bf16(f, f)); }
__device__ __forceinline__ float ex2(float x) { return __builtin_amdgcn_exp2f(x); }
template <int CTRL> __device__ __forceinline__ float dpp(float x) {
    return __builtin_bit_cast(float, __builtin_amdgcn_mov_dpp(__builtin_bit_cast(int, x), CTRL, 0xf, 0xf, true));
}
constexpr int XOR1 = 0xB1, XOR2 = 0x4E, XOR7 = 0x141, ROR8 = 0x128;
__device__ __forceinline__ float x16_max(float x) {
    auto s = __builtin_amdgcn_permlane16_swap(__float_as_uint(x), __float_as_uint(x), false, false);
    return fmaxf(__uint_as_float(s[0]), __uint_as_float(s[1]));
}
__device__ __forceinline__ float x32_max(float x) {
    auto s = __builtin_amdgcn_permlane32_swap(__float_as_uint(x), __float_as_uint(x), false, false);
    return fmaxf(__uint_as_float(s[0]), __uint_as_float(s[1]));
}
__device__ __forceinline__ float x16_sum(float x) {
    auto s = __builtin_amdgcn_permlane16_swap(__float_as_uint(x), __float_as_uint(x), false, false);
    return __uint_as_float(s[0]) + __uint_as_float(s[1]);
}
__device__ __forceinline__ float x32_sum(float x) {
    auto s = __builtin_amdgcn_permlane32_swap(__float_as_uint(x), __float_as_uint(x), false, false);
    return __uint_as_float(s[0]) + __uint_as_float(s[1]);
}
__device__ __forceinline__ float row_sum(float x) {
    x += dpp<XOR1>(x); x += dpp<XOR2>(x); x += dpp<XOR7>(x); x += dpp<ROR8>(x); return x;
}
__device__ __forceinline__ float wave_sum(float x) { return x32_sum(x16_sum(row_sum(x))); }
__device__ __forceinline__ u32 umax4(v4u a) { return max(max(a.x, a.y), max(a.z, a.w)); }
__device__ __forceinline__ u32 no_sent(float f) { const u32 u = __float_as_uint(f); return u == SENT ? 0x7FC00000u : u; }

#define RLX_AGENT __ATOMIC_RELAXED, __HIP_MEMORY_SCOPE_AGENT
constexpr int AUX_NT = 2, AUX_SC1 = 16;
__device__ __forceinline__ __amdgpu_buffer_rsrc_t mk_rsrc(const void* p, u32 bytes) {
    return __builtin_amdgcn_make_buffer_rsrc((void*)p, 0, bytes, 0x00020000);
}
__device__ __forceinline__ void glds_nt(const void* src, u32 dst) {
    asm volatile("s_mov_b32 m0, %1\n\ts_nop 0\n\tglobal_load_lds_dwordx4 %0, off nt" ::"v"(src), "s"(dst) : "memory");
}
__device__ __forceinline__ void glds_sh(const void* src, u32 dst) {
    asm volatile("s_mov_b32 m0, %1\n\ts_nop 0\n\tglobal_load_lds_dwordx4 %0, off" ::"v"(src), "s"(dst) : "memory");
}
template <int OFF> __device__ __forceinline__ void glds_nt_off(const void* src, u32 dst) {
    asm volatile("s_mov_b32 m0, %1\n\ts_nop 0\n\tglobal_load_lds_dwordx4 %0, off offset:%2 nt" ::"v"(src), "s"(dst), "i"(OFF) : "memory");
}
template <int N> __device__ __forceinline__ void wait_vm() { asm volatile("s_waitcnt vmcnt(%0)" ::"i"(N) : "memory"); }
__device__ __forceinline__ void wg_barrier() { asm volatile("s_waitcnt lgkmcnt(0)\n\ts_barrier" ::: "memory"); }
__device__ __forceinline__ v2u tr_read(const LAS unsigned char* p) {
    return __builtin_bit_cast(v2u, __builtin_amdgcn_ds_read_tr16_b64_v4i16((LAS s16x4*)p));
}
__device__ __forceinline__ v4u tr_frag(const LAS unsigned char* img, int pitch, int row0, int lane) {
    const int g = lane >> 4, q = (lane >> 2) & 3, p = lane & 3;
    const LAS unsigned char* a = img + (row0 + 8 * g + q) * pitch + 8 * p;
    const v2u lo = tr_read(a), hi = tr_read(a + 4 * pitch);
    return v4u{lo.x, lo.y, hi.x, hi.y};
}
#define STAMP(i) do { } while (0)
#define STAMPS_OUT(N) do { } while (0)

__device__ __forceinline__ void load_act(v4u (&B)[16], const LAS unsigned char* act, int lane) {
    const LAS unsigned char* ap = act + (lane & 1) * 1024 + (lane >> 4) * 16;
#pragma unroll
    for (int s = 0; s < 16; ++s) B[s] = *(const LAS v4u*)(ap + 64 * s);
}
__device__ __forceinline__ f32x4 gemv16(const LAS unsigned char* img, const v4u (&B)[16], int lane) {
    v4u A[16];
#pragma unroll
    for (int s = 0; s < 16; ++s) A[s] = tr_frag(img, 32, 32 * s, lane);
    __builtin_amdgcn_sched_barrier(0);
    f32x4 a[4];
#pragma unroll
    for (int s = 0; s < 16; ++s) a[s & 3] = mfma32(A[s], B[s], s < 4 ? f32x4{0.f, 0.f, 0.f, 0.f} : a[s & 3]);
    return (a[0] + a[1]) + (a[2] + a[3]);
}
__device__ __forceinline__ void layer_norm(float (&v)[2][8], LAS unsigned char* dst, int lane) {
    float mean[2], rs[2];
#pragma unroll
    for (int bb = 0; bb < 2; ++bb) mean[bb] = wave_sum(((v[bb][0] + v[bb][1]) + (v[bb][2] + v[bb][3])) + ((v[bb][4] + v[bb][5]) + (v[bb][6] + v[bb][7]))) * (1.f / DM);
#pragma unroll
    for (int bb = 0; bb < 2; ++bb) {
        float sq = 0.f;
#pragma unroll
        for (int u = 0; u < 8; ++u) { v[bb][u] -= mean[bb]; sq = fmaf(v[bb][u], v[bb][u], sq); }
        rs[bb] = __builtin_amdgcn_rsqf(wave_sum(sq) * (1.f / DM) + 1e-5f);
    }
#pragma unroll
    for (int bb = 0; bb < 2; ++bb) {
        v4u o;
#pragma unroll
        for (int u = 0; u < 4; ++u) {
            o[u] = pk_bf16(v[bb][2 * u] * rs[bb], v[bb][2 * u + 1] * rs[bb]);
            v[bb][2 * u] = bf_lo(o[u]); v[bb][2 * u + 1] = bf_hi(o[u]);
        }
        *(LAS v4u*)(dst + bb * 1024 + lane * 16) = o;
    }
}
__device__ __forceinline__ float sel4(f32x4 v, int r) {
    const bool b0 = r & 1, b1 = r & 2;
    const float lo = b0 ? v[1] : v[0], hi = b0 ? v[3] : v[2];
    return b1 ? hi : lo;
}
__device__ __forceinline__ v4u clean4(f32x4 v) { return v4u{no_sent(v[0]), no_sent(v[1]), no_sent(v[2]), no_sent(v[3])}; }

__global__ void __launch_bounds__(NTHR, 2) fused_decode(Params p) {
    extern __shared__ __attribute__((aligned(16))) unsigned char lds_raw[];
    LAS unsigned char* L = (LAS unsigned char*)lds_raw;
    const u32 lb = (u32)(size_t)L;
    const int tid = threadIdx.x, lane = tid & 63, w = __builtin_amdgcn_readfirstlane(tid >> 6);
    const int bid = blockIdx.x;
    const int pair = bid >> 4, b = pair >> 3, kvh = pair & 7, s = bid & 15;
    const int hh = 2 * b + (s >> 3), cg = s & 7, hi = 4 * kvh + hh;
    const int jq = 4 * cg + hh;
    const int m1i = (cg - kvh) & 7, jm = 4 * (7 * (kvh >> 1) + 4 * (kvh & 1) + m1i) + hh;
    const bool do_m1 = m1i < 4 - (kvh & 1), do_m2 = hi < 28;
    const int g = lane >> 4, n = lane & 15;
    const __amdgpu_buffer_rsrc_t rws = mk_rsrc(p.ws, WS_END);
    const u32 myslot = WS_SLOT + bid * 4096;
    STAMP(0);

    if (w < NSW) {
        const u16* kb = p.k + (size_t)pair * SK * HD;
        const u16* vb = p.v + (size_t)pair * SK * HD;
        const __amdgpu_buffer_rsrc_t rk = mk_rsrc(kb, SK * HD * 2), rv = mk_rsrc(vb, SK * HD * 2);
        constexpr u32 TSTR = NSW * 4096;
        const u32 kl = (n >> 2) * 256 + g * 64 + (n & 3) * 16, vl = lane * 16;
        u32 ko = (s * NST * NSW + w) * 4096 + kl, vo = (s * NST * NSW + w) * 4096 + vl;
        v4u Ka[4], Va[4], Kb[4], Vb[4], KX[RP][4], VX[RP][4];
#define LK(R, u) do { wait_vm<WK - 1>(); R[u] = __builtin_amdgcn_raw_buffer_load_b128(rk, ko + 1024 * (u), 0, AUX_NT); __builtin_amdgcn_sched_barrier(0); } while (0)
#define LV(R, u) do { wait_vm<WV - 1>(); R[u] = __builtin_amdgcn_raw_buffer_load_b128(rv, vo + 1024 * (u), 0, AUX_NT); __builtin_amdgcn_sched_barrier(0); } while (0)
#define NEXT() do { ko += TSTR; vo += TSTR; } while (0)
#define LOADT(KR, VR) do { LK(KR, 0); LK(KR, 1); LK(KR, 2); LK(KR, 3); LV(VR, 0); LV(VR, 1); LV(VR, 2); LV(VR, 3); NEXT(); } while (0)
        constexpr int WK = K_WK, WV = K_WV;
        if (K_B0) wg_barrier();
        const u32 park = lb + L_PARK + w * PARK * 8192;
#pragma unroll
        for (int t = 0; t < PARK; ++t) {
            const unsigned char* ks = (const unsigned char*)kb + ko;
            const unsigned char* vs = (const unsigned char*)vb + vo;
            wait_vm<K_W0 - 1>(); glds_nt_off<0>(ks, park + t * 8192);
            wait_vm<K_W0 - 1>(); glds_nt_off<1024>(ks, park + t * 8192);
            wait_vm<K_W0 - 1>(); glds_nt_off<2048>(ks, park + t * 8192);
            wait_vm<K_W0 - 1>(); glds_nt_off<3072>(ks, park + t * 8192);
            wait_vm<K_W0 - 1>(); glds_nt_off<0>(vs, park + t * 8192 + 4096);
            wait_vm<K_W0 - 1>(); glds_nt_off<1024>(vs, park + t * 8192 + 4096);
            wait_vm<K_W0 - 1>(); glds_nt_off<2048>(vs, park + t * 8192 + 4096);
            wait_vm<K_W0 - 1>(); glds_nt_off<3072>(vs, park + t * 8192 + 4096);
            NEXT();
            STAMP(7 + t);
        }
#pragma unroll
        for (int t = 0; t < RP; ++t) { LOADT(KX[t], VX[t]); if (t < 3) STAMP(9 + t); }
        LOADT(Ka, Va);
        STAMP(1);
        wg_barrier();
        STAMP(2);
        const int jj = n & 3;
        const v4u Q = *(const LAS v4u*)(L + L_Q + (n >> 2) * 256 + 64 * g + 16 * jj);
        const u32 mask0 = jj == 0 ? 0x0000ffffu : jj == 1 ? 0xffff0000u : 0u, mask1 = jj == 2 ? 0x0000ffffu : jj == 3 ? 0xffff0000u : 0u;
        float m = -INFINITY, l = 0.f, sc[4], pr[4];
        f32x4 acc0 = {0.f, 0.f, 0.f, 0.f}, acc1 = {0.f, 0.f, 0.f, 0.f};
#define QK(KR)                                                                                                 \
    do {                                                                                                       \
        f32x4 dd[4];                                                                                           \
        _Pragma("unroll") for (int u = 0; u < 4; ++u) dd[u] = mfma32(KR[u], Q, f32x4{0.f, 0.f, 0.f, 0.f});     \
        _Pragma("unroll") for (int u = 0; u < 4; ++u) sc[u] = sel4(dd[u], jj);                                 \
        _Pragma("unroll") for (int u = 0; u < 4; ++u) sc[u] += dpp<XOR1>(sc[u]);                               \
        _Pragma("unroll") for (int u = 0; u < 4; ++u) sc[u] += dpp<XOR2>(sc[u]);                               \
        __builtin_amdgcn_sched_barrier(0);                                                                     \
    } while (0)
#define SOFTMAX()                                                                                              \
    do {                                                                                                       \
        const float tm = x32_max(x16_max(fmaxf(fmaxf(sc[0], sc[1]), fmaxf(sc[2], sc[3]))));                    \
        const float mn = fmaxf(m, tm), mc = mn * SC, alpha = ex2((m - mn) * SC);                               \
        _Pragma("unroll") for (int r = 0; r < 4; ++r) pr[r] = ex2(fmaf(sc[r], SC, -mc));                       \
        l = fmaf(l, alpha, (pr[0] + pr[1]) + (pr[2] + pr[3]));                                                 \
        m = mn;                                                                                                \
        acc0 *= alpha; acc1 *= alpha;                                                                          \
        __builtin_amdgcn_sched_barrier(0);                                                                     \
    } while (0)
#define PV(VR, r0, r1)                                                                                         \
    do {                                                                                                       \
        _Pragma("unroll") for (int r = r0; r < r1; ++r) {                                                      \
            const u32 pb = pk_bf16(pr[r], pr[r]), x0 = pb & mask0, x1 = pb & mask1;                            \
            acc0 = mfma16(VR[r].x, VR[r].y, x0, x1, acc0);                                                     \
            acc1 = mfma16(VR[r].z, VR[r].w, x0, x1, acc1);                                                     \
        }                                                                                                      \
        __builtin_amdgcn_sched_barrier(0);                                                                     \
    } while (0)
#define TILE(KR, VR) do { QK(KR); SOFTMAX(); PV(VR, 0, 4); } while (0)
#define UNPARK(t, KR, VR)                                                                                      \
    do {                                                                                                       \
        const LAS unsigned char* pk = L + L_PARK + (w * PARK + (t)) * 8192 + lane * 16;                        \
        _Pragma("unroll") for (int u = 0; u < 4; ++u) { KR[u] = *(const LAS v4u*)(pk + 1024 * u); VR[u] = *(const LAS v4u*)(pk + 4096 + 1024 * u); } \
    } while (0)
#define STEP(KC, VC, KN, VN)                                                                                   \
    do {                                                                                                       \
        LK(KN, 0); QK(KC); LK(KN, 1); SOFTMAX(); LK(KN, 2); PV(VC, 0, 2); LK(KN, 3); LV(VN, 0); PV(VC, 2, 4);  \
        LV(VN, 1); LV(VN, 2); LV(VN, 3); NEXT();                                                               \
    } while (0)
        LK(Kb, 0); TILE(KX[0], VX[0]); LK(Kb, 1); LK(Kb, 2); UNPARK(0, KX[0], VX[0]); TILE(KX[1], VX[1]); LK(Kb, 3); LV(Vb, 0);
        TILE(KX[0], VX[0]); LV(Vb, 1); LV(Vb, 2); TILE(Ka, Va); LV(Vb, 3); NEXT();
        LK(Ka, 0); UNPARK(1, KX[1], VX[1]); TILE(KX[2], VX[2]); LK(Ka, 1); LK(Ka, 2); TILE(KX[3], VX[3]); LK(Ka, 3); LV(Va, 0);
        TILE(KX[1], VX[1]); LV(Va, 1); LV(Va, 2); TILE(Kb, Vb); LV(Va, 3); NEXT();
#pragma unroll 1
        for (int it = 1; it < NROUND; ++it) {
            STEP(Ka, Va, Kb, Vb);
            STEP(Kb, Vb, Ka, Va);
        }
        const u32 tko = lane == 0 ? WS_TK + (pair * NSW + w) * 256 : OOR;
#define TICKET(o) __builtin_amdgcn_raw_ptr_buffer_atomic_add_i32(1, rws, o, 0, AUX_SC1)
#define CLAIM()                                                                                                \
    do {                                                                                                       \
        const u32 t = __builtin_amdgcn_readfirstlane(tk);                                                      \
        more = t < (u32)POOL;                                                                                  \
        const u32 to = more ? (16 * NST * NSW + NSW * t + w) * 4096 : 0x40000000u;                             \
        ko = to + kl; vo = to + vl;                                                                            \
        tk = TICKET(more ? tko : OOR);                                                                         \
    } while (0)
        STEP(Ka, Va, Kb, Vb);
        int tk = TICKET(tko);
        STEP(Kb, Vb, Ka, Va);
        bool more;
#pragma unroll 1
        for (;;) {
            CLAIM();
            STEP(Ka, Va, Kb, Vb);
            if (!more) break;
            CLAIM();
            STEP(Kb, Vb, Ka, Va);
            if (!more) break;
        }
#undef CLAIM
#undef TICKET
#undef STEP
#undef UNPARK
#undef TILE
#undef PV
#undef SOFTMAX
#undef QK
#undef LOADT
#undef NEXT
#undef LV
#undef LK
        STAMP(3);
        l = x32_sum(x16_sum(l));
        {
            LAS float* mr = (LAS float*)(L + L_MRG) + (w * 4 + (n >> 2)) * 132 + 32 * g + jj;
#pragma unroll
            for (int r = 0; r < 4; ++r) { mr[8 * r] = acc0[r]; mr[8 * r + 4] = acc1[r]; }
            if (g == 0 && jj == 0) *(LAS f32x2*)(L + L_MML + (w * 4 + (n >> 2)) * 8) = f32x2{m * SC, l};
        }
        wg_barrier();
        {
            f32x2 ml[NSW]; f32x4 ov[NSW];
#pragma unroll
            for (int u = 0; u < NSW; ++u) { ml[u] = *(const LAS f32x2*)(L + L_MML + (u * 4 + w) * 8); ov[u] = *(const LAS f32x4*)(L + L_MRG + ((u * 4 + w) * 132 + 4 * (lane & 31)) * 4); }
            float M = ml[0].x;
#pragma unroll
            for (int u = 1; u < NSW; ++u) M = fmaxf(M, ml[u].x);
            float Ls = 0.f; f32x4 o = {0.f, 0.f, 0.f, 0.f};
#pragma unroll
            for (int u = 0; u < NSW; ++u) {
                const float f = ex2(ml[u].x - M);
                Ls = fmaf(f, ml[u].y, Ls);
                o += f * ov[u];
            }
            __builtin_amdgcn_raw_buffer_store_b128(clean4(o), rws, lane < 32 ? myslot + w * 512 + lane * 16 : OOR, 0, AUX_SC1);
            __builtin_amdgcn_raw_buffer_store_b64(v2u{no_sent(M), no_sent(Ls)}, rws, lane == 0 ? myslot + SL_ML + w * 8 : OOR, 0, AUX_SC1);
        }
        STAMP(4);
        v4u WO[4];
#pragma unroll
        for (int u = 0; u < 4; ++u) WO[u] = tr_frag(L + L_WO + 32 * w, 128, 32 * u, lane);
        {
            const int bb = w & 1, dh = w >> 1;
            const u32 src = WS_SLOT + ((bb * 8 + kvh) * 16 + 4 * g) * 4096;
            const u32 ao = src + hh * 512 + dh * 256 + n * 16, mo = src + SL_ML + hh * 8;
            v4u a[4]; v2u q2[4];
#pragma unroll 1
            for (u32 spin = 0; spin < SPIN_CAP; ++spin) {
                u32 mx = 0;
                asm volatile("" ::: "memory");
#pragma unroll
                for (int k = 0; k < 4; ++k) { a[k] = __builtin_amdgcn_raw_buffer_load_b128(rws, ao + k * 4096, 0, AUX_SC1); q2[k] = __builtin_amdgcn_raw_buffer_load_b64(rws, mo + k * 4096, 0, AUX_SC1); }
#pragma unroll
                for (int k = 0; k < 4; ++k) mx = max(mx, max(umax4(a[k]), max(q2[k].x, q2[k].y)));
                if (__all(mx != SENT)) break;
            }
            STAMP(5);
            float M = fmaxf(fmaxf(__uint_as_float(q2[0].x), __uint_as_float(q2[1].x)), fmaxf(__uint_as_float(q2[2].x), __uint_as_float(q2[3].x)));
            M = x32_max(x16_max(M));
            float Ls = 0.f; f32x4 o = {0.f, 0.f, 0.f, 0.f};
#pragma unroll
            for (int k = 0; k < 4; ++k) {
                const float f = ex2(__uint_as_float(q2[k].x) - M);
                Ls = fmaf(f, __uint_as_float(q2[k].y), Ls);
                o += f * __builtin_bit_cast(f32x4, a[k]);
            }
            Ls = x32_sum(x16_sum(Ls));
            const float inv = __builtin_amdgcn_rcpf(Ls);
#pragma unroll
            for (int r = 0; r < 4; ++r) o[r] = x32_sum(x16_sum(o[r])) * inv;
            if (g == 0) *(LAS v2u*)(L + L_O + bb * 256 + dh * 128 + n * 8) = v2u{pk_bf16(o[0], o[1]), pk_bf16(o[2], o[3])};
        }
        wg_barrier();
        {
            v4u ob[4];
#pragma unroll
            for (int u = 0; u < 4; ++u) ob[u] = *(const LAS v4u*)(L + L_O + (n & 1) * 256 + 64 * u + 16 * g);
            const f32x4 z = {0.f, 0.f, 0.f, 0.f};
            const f32x4 sp = mfma32(WO[1], ob[1], mfma32(WO[0], ob[0], z)) + mfma32(WO[3], ob[3], mfma32(WO[2], ob[2], z));
            __builtin_amdgcn_raw_buffer_store_b128(clean4(sp), rws, n < 2 ? myslot + SL_PS + n * 256 + w * 64 + g * 16 : OOR, 0, AUX_SC1);
        }
        STAMP(6);
        STAMPS_OUT(K_NSTAMP_S);
    } else {
        __builtin_amdgcn_s_setprio(3);
        v4u xr0, xr1; v2u ep;
        {
            const void* xa = p.x + 8 * lane;
            const void* ea = p.ws + WS_EPOCH;
            asm volatile("global_load_dwordx4 %0, %3, off\n\tglobal_load_dwordx4 %1, %3, off offset:1024\n\tglobal_load_dwordx2 %2, %4, off sc1"
                         : "=&v"(xr0), "=&v"(xr1), "=&v"(ep) : "v"(xa), "v"(ea) : "memory");
        }
        {
            const v4u sv = {SENT, SENT, SENT, SENT};
#pragma unroll
            for (int u = 0; u < 3; ++u) __builtin_amdgcn_raw_buffer_store_b128(sv, rws, myslot + u * 1024 + lane * 16, 0, AUX_SC1);
            const bool zt = s == 0 && (lane >> 2) == 1;
            __builtin_amdgcn_raw_buffer_store_b128(zt ? v4u{0u, 0u, 0u, 0u} : sv, rws, lane < 2 ? myslot + SL_ML + lane * 16 : zt ? WS_TK + (pair * NSW + (lane & 3)) * 256 : OOR, 0, AUX_SC1);
        }
        const u16* wqs = p.wq + (size_t)(lane >> 1) * 4096 + kvh * 512 + 16 * jq + 8 * (lane & 1);
#pragma unroll
        for (int u = 0; u < K_WQ; ++u) glds_sh(wqs + (size_t)u * 32 * 4096, lb + L_WQ + u * 1024);
        if (K_B0) wg_barrier();
        asm volatile("s_waitcnt vmcnt(%3)" : "+v"(xr0), "+v"(xr1), "+v"(ep) : "i"(4 + K_WQ) : "memory");
#pragma unroll
        for (int u = K_WQ; u < (K_NOWQ ? 0 : 16); ++u) { wait_vm<K_WQ - 1>(); glds_sh(wqs + (size_t)u * 32 * 4096, lb + L_WQ + u * 1024); }
        STAMP(1);
        const u64 tag = ((((u64)ep.y << 32) | ep.x) + 1) & 0xFFFFFFFFFFFFull;
        float xv[2][8];
#pragma unroll
        for (int u = 0; u < 4; ++u) { xv[0][2 * u] = bf_lo(xr0[u]); xv[0][2 * u + 1] = bf_hi(xr0[u]); xv[1][2 * u] = bf_lo(xr1[u]); xv[1][2 * u + 1] = bf_hi(xr1[u]); }
        layer_norm(xv, L + L_XN, lane);
        {
            v4u B[16];
            load_act(B, L + L_XN, lane);
            __builtin_amdgcn_sched_barrier(0);
            wait_vm<0>();
            STAMP(2);
            const f32x4 qv = gemv16(L + L_WQ, B, lane);
            const u32 qb = pk_bf16(sel4(qv, n >> 1), 0.f) & 0xffffu;
            GAS u64* dst = (GAS u64*)(p.ws + WS_QG) + (kvh * 2 + (n & 1)) * 512 + 16 * jq + 4 * g + (n >> 1);
            STAMP(9);
            if (n < 8) __hip_atomic_store(dst, (tag << 16) | qb, RLX_AGENT);
        }
        STAMP(3);
        {
            GAS u64* src = (GAS u64*)(p.ws + WS_QG) + (kvh * 2 + b) * 512 + lane;
            u64 A[8];
            __builtin_amdgcn_s_sleep(K_SQ0);
#pragma unroll 1
            for (u32 spin = 0; spin < SPIN_CAP; ++spin) {
                bool ok = true;
#pragma unroll
                for (int k = 0; k < 8; ++k) A[k] = __hip_atomic_load(src + 64 * k, RLX_AGENT);
#pragma unroll
                for (int k = 0; k < 8; ++k) ok &= (A[k] >> 16) == tag;
                if (__all(ok)) break;
                __builtin_amdgcn_s_sleep(K_SQ1);
            }
#pragma unroll
            for (int k = 0; k < 8; ++k) *(LAS u16*)(L + L_Q + (64 * k + lane) * 2) = (u16)A[k];
        }
        STAMP(4);
        wg_barrier();
        __builtin_amdgcn_s_setprio(1);
        layer_norm(xv, L + L_Y, lane);
        const int fb = hi >> 4, hf = lane >> 5, fc = 64 * cg + 4 * (hi & 15) + 2 * hf;
        const u32 xo = *(const GAS u32*)(p.x + fb * DM + fc), b2o = *(const GAS u32*)(p.b2 + fc);
        const int col1 = do_m1 ? 16 * jm + 4 * g + ((n >> 1) & 3) : 0;
        const float bias1 = bf_lo(p.b1[col1]);
        if (do_m1) {
            const u16* s1 = p.w1 + (size_t)(lane >> 1) * FF + 16 * jm + 8 * (lane & 1);
#pragma unroll
            for (int u = 0; u < 16; ++u) { wait_vm<K_WW1 - 1>(); glds_sh(s1 + (size_t)u * 32 * FF, lb + L_W1 + u * 1024); }
        }
        {
            const u16* so = p.wo + (size_t)(hi * 128 + (lane >> 3)) * DM + 64 * cg + 8 * (lane & 7);
#pragma unroll
            for (int u = 0; u < 16; ++u) { wait_vm<K_WWO - 1>(); glds_nt(so + (size_t)u * 8 * DM, lb + L_WO + u * 1024); }
        }
        if (do_m2) {
            const u16* s2 = p.w2 + (size_t)(hi * 64 + (lane >> 3)) * DM + 64 * cg + 8 * (lane & 7);
#pragma unroll
            for (int u = 0; u < 8; ++u) { wait_vm<K_WWO - 1>(); glds_nt(s2 + (size_t)u * 8 * DM, lb + L_W2 + u * 1024); }
        }
        wait_vm<0>();
        STAMP(10);
        if (do_m1) {
            v4u B[16];
            load_act(B, L + L_Y, lane);
            const float u = rbf(rbf(sel4(gemv16(L + L_W1, B, lane), n >> 1)) + bias1);
            const float z = 1.5957691216057308f * fmaf(0.044715f * u * u, u, u);
            const float h = u / (1.f + __expf(-z));
            if (n < 8) __hip_atomic_store((GAS u64*)(p.ws + WS_HG) + (n & 1) * FF + col1, (tag << 16) | (pk_bf16(h, 0.f) & 0xffffu), RLX_AGENT);
        }
        const int pi = lane & 31;
        const u32 pslot = WS_SLOT + (((((pi >> 1) & 1) * 8 + (pi >> 2)) * 16) + cg + 8 * (pi & 1)) * 4096 + fb * 256 + (hi & 15) * 16 + hf * 8;
        {
            GAS u64* ev = (GAS u64*)(p.ws + WS_QG) + ((pi >> 2) * 2) * 512 + 16 * (4 * cg + (pi & 3));
            GAS u64* hs = do_m2 ? (GAS u64*)(p.ws + WS_HG) + 64 * hi + lane : ev;
            const int hstr = do_m2 ? FF : 0;
            u64 e, h0, h1;
            if (!do_m1) { __builtin_amdgcn_s_sleep(K_SH0); __builtin_amdgcn_s_sleep(K_SH0); }
            __builtin_amdgcn_s_sleep(K_SH0);
#pragma unroll 1
            for (u32 spin = 0; spin < SPIN_CAP; ++spin) {
                e = __hip_atomic_load(ev, RLX_AGENT); h0 = __hip_atomic_load(hs, RLX_AGENT); h1 = __hip_atomic_load(hs + hstr, RLX_AGENT);
                if (__all(((e >> 16) == tag) & ((h0 >> 16) == tag) & ((h1 >> 16) == tag))) break;
                __builtin_amdgcn_s_sleep(K_SH1);
            }
            *(LAS u16*)(L + L_H + lane * 2) = (u16)h0;
            *(LAS u16*)(L + L_H + 128 + lane * 2) = (u16)h1;
        }
        if (do_m2) {
#pragma unroll
            for (int cb = 0; cb < 4; ++cb) {
                f32x4 mp = {0.f, 0.f, 0.f, 0.f};
#pragma unroll
                for (int u = 0; u < 2; ++u) mp = mfma32(tr_frag(L + L_W2 + 32 * cb, 128, 32 * u, lane), *(const LAS v4u*)(L + L_H + (n & 1) * 128 + 64 * u + 16 * g), mp);
                __builtin_amdgcn_raw_buffer_store_b128(clean4(mp), rws, n < 2 ? myslot + SL_PM + n * 256 + cb * 64 + g * 16 : OOR, 0, AUX_SC1);
            }
        }
        STAMP(5);
        float gate[2];
        {
            v2u a;
            __builtin_amdgcn_s_sleep(K_SH0);
#pragma unroll 1
            for (u32 spin = 0; spin < SPIN_CAP; ++spin) {
                asm volatile("" ::: "memory");
                a = __builtin_amdgcn_raw_buffer_load_b64(rws, pi < 28 ? pslot + SL_PM : OOR, 0, AUX_SC1);
                if (__all(max(a.x, a.y) != SENT)) break;
                __builtin_amdgcn_s_sleep(K_SH1);
            }
#pragma unroll
            for (int k = 0; k < 2; ++k) {
                const float mm = rbf(rbf(x16_sum(row_sum(__uint_as_float(a[k])))) + (k ? bf_hi(b2o) : bf_lo(b2o)));
                gate[k] = rbf(mm / (1.f + __expf(-mm)));
            }
        }
        STAMP(11);
        __builtin_amdgcn_s_setprio(3);
        wg_barrier();
        wg_barrier();
        STAMP(6);
        {
            v2u a;
#pragma unroll 1
            for (u32 spin = 0; spin < SPIN_CAP; ++spin) {
                asm volatile("" ::: "memory");
                a = __builtin_amdgcn_raw_buffer_load_b64(rws, pslot + SL_PS, 0, AUX_SC1);
                if (__all(max(a.x, a.y) != SENT)) break;
            }
            STAMP(7);
            const float s0 = rbf(x16_sum(row_sum(__uint_as_float(a.x)))), s1 = rbf(x16_sum(row_sum(__uint_as_float(a.y))));
            const u32 r = pk_bf16(bf_lo(xo) + rbf(s0 * gate[0]), bf_hi(xo) + rbf(s1 * gate[1]));
            if (pi == 0) *(GAS u32*)(p.out + fb * DM + fc) = r;
            if (bid == 0 && lane == 0) __hip_atomic_store((GAS u64*)(p.ws + WS_EPOCH), (((u64)ep.y << 32) | ep.x) + 1, RLX_AGENT);
        }
        STAMP(8);
        STAMPS_OUT(K_NSTAMP_H);
    }
}

extern "C" void kernel_launch(void* const* d_in, const int* in_sizes, int n_in, void* d_out, int out_size, void* d_ws, size_t ws_size,
                              hipStream_t stream) {
    Params p;
    p.x = (const u16*)d_in[0]; p.k = (const u16*)d_in[1]; p.v = (const u16*)d_in[2]; p.wq = (const u16*)d_in[3]; p.wo = (const u16*)d_in[4];
    p.w1 = (const u16*)d_in[6]; p.b1 = (const u16*)d_in[7]; p.w2 = (const u16*)d_in[8]; p.b2 = (const u16*)d_in[9];
    p.out = (u16*)d_out; p.ws = (unsigned char*)d_ws;
    constexpr int LDS_BYTES = L_END > 96 * 1024 ? L_END : 96 * 1024;
    static_assert(LDS_BYTES <= 160 * 1024, "");
    (void)hipFuncSetAttribute((const void*)fused_decode, hipFuncAttributeMaxDynamicSharedMemorySize, LDS_BYTES);
    hipLaunchKernelGGL(fused_decode, dim3(NWG), dim3(NTHR), LDS_BYTES, stream, p);
}
```

```cpp
#include <hip/hip_runtime.h>
#include <hip/hip_bf16.h>
#include <stdint.h>

#define GAS __attribute__((address_space(1)))
#define LAS __attribute__((address_space(3)))
typedef unsigned u32;
typedef unsigned long long u64;
typedef unsigned short u16;
typedef unsigned v4u __attribute__((ext_vector_type(4)));
typedef unsigned v2u __attribute__((ext_vector_type(2)));
typedef float f32x4 __attribute__((ext_vector_type(4)));
typedef float f32x2 __attribute__((ext_vector_type(2)));
typedef short s16x4 __attribute__((ext_vector_type(4)));
typedef __bf16 bf16x8 __attribute__((ext_vector_type(8)));
typedef __bf16 bf16x2 __attribute__((ext_vector_type(2)));

#define K_WK 6
#define K_WV 7
#define K_WQ 8
#define K_W0 6
#define K_S 25
#define K_SQ0 100
#define K_SQ1 24
#define K_SH0 64
#define K_SH1 64
#define K_WW1 3
#define K_WWO 4
#define K_OUTSC1 0
#define K_SB 0
#define K_SC 0
#define K_ANN 5
#define K_DA 8
#define K_DB 0
#define K_ANNB 2
#define K_SP 1
#define K_DC 6
#define K_WOLATE 2
#define K_NB 16
#define K_ADJ 0
#define X_SKIP 0
#define K_SHNT 0
#define K_NOWQ 0
#define K_B0 0
#define K_TS 0

constexpr int DM = 512, HD = 128, SK = 32768, FF = 1792;
constexpr int NWG = 256, NSW = 4, NTHR = (NSW + 1) * 64;
constexpr int RP = 4, PARK = 2;
constexpr int NST = K_S;
constexpr int NROUND = (NST - RP - PARK - 3) / 2;
constexpr int POOL = SK / 16 / NSW - 16 * NST;
static_assert(NROUND * 2 + RP + PARK + 3 == NST && NROUND >= 1 && POOL > 0, "");
constexpr float SC = 0.08838834764831845f * 1.4426950408889634f;
constexpr u32 SENT = 0xFFFFFFFFu;
constexpr u32 SPIN_CAP = 1u << 18;
constexpr u32 OOR = 0xFFFFFF00u;

constexpr u32 WS_EPOCH = 0;
constexpr u32 WS_QG = 4096;
constexpr u32 WS_HG = WS_QG + 8 * 2 * 512 * 8;
constexpr u32 WS_TK = 102400;
constexpr u32 WS_ML = 118784;
constexpr u32 WS_SLOT = 131072;
constexpr u32 SL_PS = 2048, SL_PM = 2560, SL_FA = 3072, SL_FB = 3200;
constexpr u32 WS_END = WS_SLOT + NWG * 4096;
constexpr u32 WS_TS = WS_END;
static_assert(WS_HG + 2 * FF * 8 <= WS_TK && WS_TK + 64 * 256 <= WS_ML && WS_ML + 8192 <= WS_SLOT, "");

constexpr u32 L_WQ = 0, L_W1 = 16384, L_WO = 32768, L_W2 = 49152;
constexpr u32 L_XN = 57344, L_Y = L_XN + 2048, L_Q = L_Y + 2048, L_H = L_Q + 1024, L_O = L_H + 256;
constexpr u32 L_MRG = L_O + 512;
constexpr u32 L_MML = L_MRG + NSW * 4 * 132 * 4;
constexpr u32 L_CNT = L_MML + NSW * 4 * 2 * 4;
constexpr u32 L_PARK = 73728;
constexpr u32 L_END = L_PARK + NSW * PARK * 8192;
static_assert(L_CNT + 16 <= L_PARK, "");

struct Params {
    const u16 *x, *k, *v, *wq, *wo, *w1, *b1, *w2, *b2;
    u16* out;
    unsigned char* ws;
};

__device__ __forceinline__ f32x4 mfma32(v4u a, v4u b, f32x4 c) {
    return __builtin_amdgcn_mfma_f32_16x16x32_bf16(__builtin_bit_cast(bf16x8, a), __builtin_bit_cast(bf16x8, b), c, 0, 0, 0);
}
__device__ __forceinline__ f32x4 mfma16(u32 a0, u32 a1, u32 b0, u32 b1, f32x4 c) {
    return __builtin_amdgcn_mfma_f32_16x16x16bf16_1k(__builtin_bit_cast(s16x4, v2u{a0, a1}), __builtin_bit_cast(s16x4, v2u{b0, b1}), c, 0, 0, 0);
}
__device__ __forceinline__ u32 pk_bf16(float lo, float hi) {
    return __builtin_bit_cast(u32, __builtin_convertvector(f32x2{lo, hi}, bf16x2));
}
__device__ __forceinline__ float bf_lo(u32 w) { return __builtin_bit_cast(float, w << 16); }
__device__ __forceinline__ float bf_hi(u32 w) { return __builtin_bit_cast(float, w & 0xffff0000u); }
__device__ __forceinline__ float rbf(float f) { return bf_lo(pk_bf16(f, f)); }
__device__ __forceinline__ float ex2(float x) { return __builtin_amdgcn_exp2f(x); }
template <int CTRL> __device__ __forceinline__ float dpp(float x) {
    return __builtin_bit_cast(float, __builtin_amdgcn_mov_dpp(__builtin_bit_cast(int, x), CTRL, 0xf, 0xf, true));
}
constexpr int XOR1 = 0xB1, XOR2 = 0x4E, XOR7 = 0x141, ROR8 = 0x128;
__device__ __forceinline__ float x16_max(float x) {
    auto s = __builtin_amdgcn_permlane16_swap(__float_as_uint(x), __float_as_uint(x), false, false);
    return fmaxf(__uint_as_float(s[0]), __uint_as_float(s[1]));
}
__device__ __forceinline__ float x32_max(float x) {
    auto s = __builtin_amdgcn_permlane32_swap(__float_as_uint(x), __float_as_uint(x), false, false);
    return fmaxf(__uint_as_float(s[0]), __uint_as_float(s[1]));
}
__device__ __forceinline__ float x16_sum(float x) {
    auto s = __builtin_amdgcn_permlane16_swap(__float_as_uint(x), __float_as_uint(x), false, false);
    return __uint_as_float(s[0]) + __uint_as_float(s[1]);
}
__device__ __forceinline__ float x32_sum(float x) {
    auto s = __builtin_amdgcn_permlane32_swap(__float_as_uint(x), __float_as_uint(x), false, false);
    return __uint_as_float(s[0]) + __uint_as_float(s[1]);
}
__device__ __forceinline__ float row_sum(float x) {
    x += dpp<XOR1>(x); x += dpp<XOR2>(x); x += dpp<XOR7>(x); x += dpp<ROR8>(x); return x;
}
__device__ __forceinline__ float row_max(float x) {
    float r;
    asm("s_nop 1\n\tv_max_f32_dpp %0, %1, %1 quad_perm:[1,0,3,2] row_mask:0xf bank_mask:0xf\n\ts_nop 1\n\t"
        "v_max_f32_dpp %0, %0, %0 quad_perm:[2,3,0,1] row_mask:0xf bank_mask:0xf\n\ts_nop 1\n\t"
        "v_max_f32_dpp %0, %0, %0 row_half_mirror row_mask:0xf bank_mask:0xf\n\ts_nop 1\n\t"
        "v_max_f32_dpp %0, %0, %0 row_ror:8 row_mask:0xf bank_mask:0xf" : "=&v"(r) : "v"(x));
    return r;
}
__device__ __forceinline__ float wave_sum(float x) { return x32_sum(x16_sum(row_sum(x))); }
__device__ __forceinline__ u32 umax4(v4u a) { return max(max(a.x, a.y), max(a.z, a.w)); }
__device__ __forceinline__ u32 no_sent(float f) { return min(__float_as_uint(f), 0xFFFFFFFEu); }

#define RLX_AGENT __ATOMIC_RELAXED, __HIP_MEMORY_SCOPE_AGENT
constexpr int AUX_NT = 2, AUX_SC1 = 16;
__device__ __forceinline__ __amdgpu_buffer_rsrc_t mk_rsrc(const void* p, u32 bytes) {
    return __builtin_amdgcn_make_buffer_rsrc((void*)p, 0, bytes, 0x00020000);
}
__device__ __forceinline__ void glds_nt(const void* src, u32 dst) {
    asm volatile("s_mov_b32 m0, %1\n\ts_nop 0\n\tglobal_load_lds_dwordx4 %0, off nt" ::"v"(src), "s"(dst) : "memory");
}
__device__ __forceinline__ void glds_sh(const void* src, u32 dst) {
    asm volatile("s_mov_b32 m0, %1\n\ts_nop 0\n\tglobal_load_lds_dwordx4 %0, off" ::"v"(src), "s"(dst) : "memory");
}
template <int OFF> __device__ __forceinline__ void glds_nt_off(const void* src, u32 dst) {
    asm volatile("s_mov_b32 m0, %1\n\ts_nop 0\n\tglobal_load_lds_dwordx4 %0, off offset:%2 nt" ::"v"(src), "s"(dst), "i"(OFF) : "memory");
}
template <int N> __device__ __forceinline__ void wait_vm() { asm volatile("s_waitcnt vmcnt(%0)" ::"i"(N) : "memory"); }
__device__ __forceinline__ void wg_barrier() { asm volatile("s_waitcnt lgkmcnt(0)\n\ts_barrier" ::: "memory"); }
__device__ __forceinline__ v2u tr_read(const LAS unsigned char* p) {
    return __builtin_bit_cast(v2u, __builtin_amdgcn_ds_read_tr16_b64_v4i16((LAS s16x4*)p));
}
__device__ __forceinline__ v4u tr_frag(const LAS unsigned char* img, int pitch, int row0, int lane) {
    const int g = lane >> 4, q = (lane >> 2) & 3, p = lane & 3;
    const LAS unsigned char* a = img + (row0 + 8 * g + q) * pitch + 8 * p;
    const v2u lo = tr_read(a), hi = tr_read(a + 4 * pitch);
    return v4u{lo.x, lo.y, hi.x, hi.y};
}
#define STAMP(i) do { } while (0)
#define STAMPS_OUT(N) do { } while (0)
#define COUNT0(i) do { } while (0)
#define COUNT(i) do { } while (0)

__device__ __forceinline__ void load_act(v4u (&B)[16], const LAS unsigned char* act, int lane) {
    const LAS unsigned char* ap = act + (lane & 1) * 1024 + (lane >> 4) * 16;
#pragma unroll
    for (int s = 0; s < 16; ++s) B[s] = *(const LAS v4u*)(ap + 64 * s);
}
__device__ __forceinline__ f32x4 gemv16(const LAS unsigned char* img, const v4u (&B)[16], int lane) {
    v4u A[16];
#pragma unroll
    for (int s = 0; s < 16; ++s) A[s] = tr_frag(img, 32, 32 * s, lane);
    __builtin_amdgcn_sched_barrier(0);
    f32x4 a[4];
#pragma unroll
    for (int s = 0; s < 16; ++s) a[s & 3] = mfma32(A[s], B[s], s < 4 ? f32x4{0.f, 0.f, 0.f, 0.f} : a[s & 3]);
    return (a[0] + a[1]) + (a[2] + a[3]);
}
__device__ __forceinline__ void layer_norm(float (&v)[2][8], LAS unsigned char* dst, int lane) {
    float mean[2], rs[2];
#pragma unroll
    for (int bb = 0; bb < 2; ++bb) mean[bb] = wave_sum(((v[bb][0] + v[bb][1]) + (v[bb][2] + v[bb][3])) + ((v[bb][4] + v[bb][5]) + (v[bb][6] + v[bb][7]))) * (1.f / DM);
#pragma unroll
    for (int bb = 0; bb < 2; ++bb) {
        float sq = 0.f;
#pragma unroll
        for (int u = 0; u < 8; ++u) { v[bb][u] -= mean[bb]; sq = fmaf(v[bb][u], v[bb][u], sq); }
        rs[bb] = __builtin_amdgcn_rsqf(wave_sum(sq) * (1.f / DM) + 1e-5f);
    }
#pragma unroll
    for (int bb = 0; bb < 2; ++bb) {
        v4u o;
#pragma unroll
        for (int u = 0; u < 4; ++u) {
            o[u] = pk_bf16(v[bb][2 * u] * rs[bb], v[bb][2 * u + 1] * rs[bb]);
            v[bb][2 * u] = bf_lo(o[u]); v[bb][2 * u + 1] = bf_hi(o[u]);
        }
        *(LAS v4u*)(dst + bb * 1024 + lane * 16) = o;
    }
}
__device__ __forceinline__ float sel4(f32x4 v, int r) {
    const bool b0 = r & 1, b1 = r & 2;
    const float lo = b0 ? v[1] : v[0], hi = b0 ? v[3] : v[2];
    return b1 ? hi : lo;
}
__device__ __forceinline__ v4u clean4(f32x4 v) { return v4u{no_sent(v[0]), no_sent(v[1]), no_sent(v[2]), no_sent(v[3])}; }

__global__ void __launch_bounds__(NTHR, 2) fused_decode(Params p) {
    extern __shared__ __attribute__((aligned(16))) unsigned char lds_raw[];
    LAS unsigned char* L = (LAS unsigned char*)lds_raw;
    const u32 lb = (u32)(size_t)L;
    const int tid = threadIdx.x, lane = tid & 63, w = __builtin_amdgcn_readfirstlane(tid >> 6);
    const int bid = blockIdx.x;
    const int pair = bid >> 4, b = pair >> 3, kvh = pair & 7, s = bid & 15;
    const int jq = 4 * (s & 7) + 2 * b + (s >> 3);
    const int m1i = (s - kvh) & 7, jm = 4 * (7 * (kvh >> 1) + 4 * (kvh & 1) + m1i) + (jq & 3);
    const int x8 = bid & 7, hi = 4 * x8 + (bid >> 6), cg = (bid >> 3) & 7;
    const bool do_m1 = m1i < 4 - (kvh & 1), do_m2 = hi < 28;
    const int g = lane >> 4, n = lane & 15;
    const __amdgpu_buffer_rsrc_t rws = mk_rsrc(p.ws, WS_END);
    const u32 myslot = WS_SLOT + bid * 4096;
    STAMP(0);

    if (w < NSW) {
        const u16* kb = p.k + (size_t)pair * SK * HD;
        const u16* vb = p.v + (size_t)pair * SK * HD;
        const __amdgpu_buffer_rsrc_t rk = mk_rsrc(kb, SK * HD * 2), rv = mk_rsrc(vb, SK * HD * 2);
        constexpr u32 TSTR = NSW * 4096;
        const u32 kl = (n >> 2) * 256 + g * 64 + (n & 3) * 16, vl = lane * 16;
        const int reg = K_ADJ ? 2 * (s & 7) + (s >> 3) : s;
        u32 ko = (reg * NST * NSW + w) * 4096 + kl, vo = (reg * NST * NSW + w) * 4096 + vl;
        v4u Ka[4], Va[4], Kb[4], Vb[4], KX[RP][4], VX[RP][4];
#define LK(R, u) do { wait_vm<WK - 1>(); R[u] = __builtin_amdgcn_raw_buffer_load_b128(rk, ko + 1024 * (u), 0, AUX_NT); __builtin_amdgcn_sched_barrier(0); } while (0)
#define LV(R, u) do { wait_vm<WV - 1>(); R[u] = __builtin_amdgcn_raw_buffer_load_b128(rv, vo + 1024 * (u), 0, AUX_NT); __builtin_amdgcn_sched_barrier(0); } while (0)
#define NEXT() do { ko += TSTR; vo += TSTR; } while (0)
#define LOADT(KR, VR) do { LK(KR, 0); LK(KR, 1); LK(KR, 2); LK(KR, 3); LV(VR, 0); LV(VR, 1); LV(VR, 2); LV(VR, 3); NEXT(); } while (0)
        constexpr int WK = K_WK, WV = K_WV;
        if (K_B0) wg_barrier();
        const u32 park = lb + L_PARK + w * PARK * 8192;
#pragma unroll
        for (int t = 0; t < PARK; ++t) {
            const unsigned char* ks = (const unsigned char*)kb + ko;
            const unsigned char* vs = (const unsigned char*)vb + vo;
            wait_vm<K_W0 - 1>(); glds_nt_off<0>(ks, park + t * 8192);
            wait_vm<K_W0 - 1>(); glds_nt_off<1024>(ks, park + t * 8192);
            wait_vm<K_W0 - 1>(); glds_nt_off<2048>(ks, park + t * 8192);
            wait_vm<K_W0 - 1>(); glds_nt_off<3072>(ks, park + t * 8192);
            wait_vm<K_W0 - 1>(); glds_nt_off<0>(vs, park + t * 8192 + 4096);
            wait_vm<K_W0 - 1>(); glds_nt_off<1024>(vs, park + t * 8192 + 4096);
            wait_vm<K_W0 - 1>(); glds_nt_off<2048>(vs, park + t * 8192 + 4096);
            wait_vm<K_W0 - 1>(); glds_nt_off<3072>(vs, park + t * 8192 + 4096);
            NEXT();
        }
#pragma unroll
        for (int t = 0; t < RP; ++t) LOADT(KX[t], VX[t]);
        LOADT(Ka, Va);
        STAMP(1);
        wg_barrier();
        STAMP(2);
        const int jj = n & 3;
        const v4u Q = *(const LAS v4u*)(L + L_Q + (n >> 2) * 256 + 64 * g + 16 * jj);
        const u32 mask0 = jj == 0 ? 0x0000ffffu : jj == 1 ? 0xffff0000u : 0u, mask1 = jj == 2 ? 0x0000ffffu : jj == 3 ? 0xffff0000u : 0u;
        float m = -INFINITY, l = 0.f, sc[4], pr[4];
        f32x4 acc0 = {0.f, 0.f, 0.f, 0.f}, acc1 = {0.f, 0.f, 0.f, 0.f};
#define QK(KR)                                                                                                 \
    do {                                                                                                       \
        f32x4 dd[4];                                                                                           \
        _Pragma("unroll") for (int u = 0; u < 4; ++u) dd[u] = mfma32(KR[u], Q, f32x4{0.f, 0.f, 0.f, 0.f});     \
        asm volatile("" : "+v"(dd[0]), "+v"(dd[1]), "+v"(dd[2]), "+v"(dd[3]));     \
        _Pragma("unroll") for (int u = 0; u < 4; ++u) sc[u] = sel4(dd[u], jj);                                 \
        _Pragma("unroll") for (int u = 0; u < 4; ++u) sc[u] += dpp<XOR1>(sc[u]);                               \
        _Pragma("unroll") for (int u = 0; u < 4; ++u) sc[u] += dpp<XOR2>(sc[u]);                               \
        __builtin_amdgcn_sched_barrier(0);                                                                     \
    } while (0)
#define SOFTMAX()                                                                                              \
    do {                                                                                                       \
        const float tm = x32_max(x16_max(fmaxf(fmaxf(sc[0], sc[1]), fmaxf(sc[2], sc[3]))));                    \
        const float mn = fmaxf(m, tm), mc = mn * SC, alpha = ex2((m - mn) * SC);                               \
        _Pragma("unroll") for (int r = 0; r < 4; ++r) pr[r] = ex2(fmaf(sc[r], SC, -mc));                       \
        l = fmaf(l, alpha, (pr[0] + pr[1]) + (pr[2] + pr[3]));                                                 \
        m = mn;                                                                                                \
        acc0 *= alpha; acc1 *= alpha;                                                                          \
        __builtin_amdgcn_sched_barrier(0);                                                                     \
    } while (0)
#define PV(VR, r0, r1)                                                                                         \
    do {                                                                                                       \
        _Pragma("unroll") for (int r = r0; r < r1; ++r) {                                                      \
            const u32 pb = pk_bf16(pr[r], pr[r]), x0 = pb & mask0, x1 = pb & mask1;                            \
            acc0 = mfma16(VR[r].x, VR[r].y, x0, x1, acc0);                                                     \
            acc1 = mfma16(VR[r].z, VR[r].w, x0, x1, acc1);                                                     \
        }                                                                                                      \
        __builtin_amdgcn_sched_barrier(0);                                                                     \
    } while (0)
#define TILE(KR, VR) do { QK(KR); SOFTMAX(); PV(VR, 0, 4); } while (0)
#define UNPARK(t, KR, VR)                                                                                      \
    do {                                                                                                       \
        const LAS unsigned char* pk = L + L_PARK + (w * PARK + (t)) * 8192 + lane * 16;                        \
        _Pragma("unroll") for (int u = 0; u < 4; ++u) { KR[u] = *(const LAS v4u*)(pk + 1024 * u); VR[u] = *(const LAS v4u*)(pk + 4096 + 1024 * u); } \
    } while (0)
#define HOOK(k) do { } while (0)
#define STEP(KC, VC, KN, VN)                                                                                   \
    do {                                                                                                       \
        HOOK(5); LK(KN, 0); QK(KC); HOOK(4); LK(KN, 1); SOFTMAX(); LK(KN, 2); HOOK(3); PV(VC, 0, 2); LK(KN, 3); LV(VN, 0); HOOK(2); PV(VC, 2, 4);  \
        LV(VN, 1); LV(VN, 2); LV(VN, 3); NEXT();                                                               \
    } while (0)
        LK(Kb, 0); TILE(KX[0], VX[0]); LK(Kb, 1); LK(Kb, 2); UNPARK(0, KX[0], VX[0]); TILE(KX[1], VX[1]); LK(Kb, 3); LV(Vb, 0);
        TILE(KX[0], VX[0]); LV(Vb, 1); LV(Vb, 2); TILE(Ka, Va); LV(Vb, 3); NEXT();
        LK(Ka, 0); UNPARK(1, KX[1], VX[1]); TILE(KX[2], VX[2]); LK(Ka, 1); LK(Ka, 2); TILE(KX[3], VX[3]); LK(Ka, 3); LV(Va, 0);
        TILE(KX[1], VX[1]); LV(Va, 1); LV(Va, 2); TILE(Kb, Vb); LV(Va, 3); NEXT();
#pragma unroll 1
        for (int it = 1; it < NROUND; ++it) {
            STEP(Ka, Va, Kb, Vb);
            STEP(Kb, Vb, Ka, Va);
        }
        u32 tko = lane == 0 ? WS_TK + (pair * NSW + w) * 256 : OOR;
#define TICKET(o) __builtin_amdgcn_raw_ptr_buffer_atomic_add_i32(1, rws, o, 0, AUX_SC1)
#define CLAIM()                                                                                                \
    do {                                                                                                       \
        t = __builtin_amdgcn_readfirstlane(tk);                                                                \
        more = t < (u32)POOL;                                                                                  \
        const u32 to = more ? (16 * NST * NSW + NSW * t + w) * 4096 : 0x40000000u;                             \
        const u32 tm_ = more ? 0u : OOR;                                                                       \
        asm volatile("v_add_u32 %0, %3, %4\n\tv_add_u32 %1, %3, %5\n\tv_or_b32 %2, %6, %2" : "+v"(ko), "+v"(vo), "+v"(tko) : "s"(to), "v"(kl), "v"(vl), "s"(tm_)); \
        tk = TICKET(tko);                                                                         \
    } while (0)
        STEP(Ka, Va, Kb, Vb);
        int tk = TICKET(tko);
        STEP(Kb, Vb, Ka, Va);
        bool more;
        u32 t;
#define WAITC(N) asm volatile("s_cmpk_ge_u32 %0, %2\n\ts_cbranch_scc1 1f\n\ts_waitcnt vmcnt(%1)\n1:" ::"s"(t), "i"(N), "i"(POOL - (K_NB > 0 ? K_NB : 0)) : "memory", "scc")
#define LKC(R, u) do { WAITC(WK - 1); R[u] = __builtin_amdgcn_raw_buffer_load_b128(rk, ko + 1024 * (u), 0, AUX_NT); __builtin_amdgcn_sched_barrier(0); } while (0)
#define LVC(R, u) do { WAITC(WV - 1); R[u] = __builtin_amdgcn_raw_buffer_load_b128(rv, vo + 1024 * (u), 0, AUX_NT); __builtin_amdgcn_sched_barrier(0); } while (0)
#define STEPD(KC, VC, KN, VN)                                                                                  \
    do {                                                                                                       \
        HOOK(5); LKC(KN, 0); LKC(KN, 1); LKC(KN, 2); LKC(KN, 3); LVC(VN, 0); QK(KC); HOOK(4); LVC(VN, 1); SOFTMAX(); \
        LVC(VN, 2); HOOK(3); PV(VC, 0, 4); LVC(VN, 3);                                                         \
    } while (0)
#undef HOOK
        u32 cnt_a = lb + L_CNT, cnt_v = lane == 0;
        const int bb = w & 1, dh = w >> 1;
        u32 a_mw = L_MRG + ((w * 4 + (n >> 2)) * 132 + 32 * g + jj) * 4, a_lw = L_MML + (w * 4 + (n >> 2)) * 8;
        u32 a_mr = L_MRG + (w * 132 + 4 * (lane & 31)) * 4, a_lr = L_MML + w * 8;
        u32 o_pa = lane < 32 ? myslot + w * 512 + lane * 16 : OOR, o_pl = lane == 0 ? WS_ML + ((pair * 4 + w) * 16 + s) * 8 : OOR;
        u32 o_ga = WS_SLOT + ((bb * 8 + x8) * 16 + 4 * g) * 4096 + (hi & 3) * 512 + dh * 256 + n * 16;
        u32 o_gl = WS_ML + (((bb * 8 + x8) * 4 + (hi & 3)) * 16 + ((n + 4 * g) & 15)) * 8;
        u32 a_wo = L_WO + 32 * w + (8 * g + ((lane >> 2) & 3)) * 128 + 8 * jj, a_ow = L_O + bb * 256 + dh * 128 + n * 8 + g * 2;
        u32 a_or = L_O + (n & 1) * 256 + 16 * g, o_ps = n < 2 ? myslot + SL_PS + n * 256 + w * 64 + g * 16 : OOR;
        asm volatile("" : "+v"(cnt_a), "+v"(cnt_v), "+v"(a_mw), "+v"(a_lw), "+v"(a_mr), "+v"(a_lr), "+v"(o_pa), "+v"(o_pl));
        asm volatile("" : "+v"(o_ga), "+v"(o_gl), "+v"(a_wo), "+v"(a_ow), "+v"(a_or), "+v"(o_ps));
#define NEARLY_DONE() asm volatile("ds_add_u32 %0, %1" ::"v"(cnt_a), "v"(cnt_v) : "memory")
#define HOOK(k) do { if (K_ANN == (k) && !more) NEARLY_DONE(); } while (0)
#pragma unroll 1
        for (;;) {
            CLAIM();
            if (K_NB >= 0) STEPD(Ka, Va, Kb, Vb); else STEP(Ka, Va, Kb, Vb);
            if (!more) break;
            CLAIM();
            if (K_NB >= 0) STEPD(Kb, Vb, Ka, Va); else STEP(Kb, Vb, Ka, Va);
            if (!more) break;
        }
        if (K_ANN == 1) NEARLY_DONE();
#undef NEARLY_DONE
#undef STEPD
#undef LVC
#undef LKC
#undef WAITC
#undef HOOK
#undef CLAIM
#undef TICKET
#undef STEP
#undef UNPARK
#undef TILE
#undef PV
#undef SOFTMAX
#undef QK
#undef LOADT
#undef NEXT
#undef LV
#undef LK
        STAMP(3);
        {
            LAS float* mr = (LAS float*)(L + a_mw);
#pragma unroll
            for (int r = 0; r < 4; ++r) { mr[8 * r] = acc0[r]; mr[8 * r + 4] = acc1[r]; }
            *(LAS f32x2*)(L + a_lw) = f32x2{m * SC, x32_sum(x16_sum(l))};
        }
        wg_barrier();
        {
            f32x2 ml[NSW]; f32x4 ov[NSW];
#pragma unroll
            for (int u = 0; u < NSW; ++u) ml[u] = *(const LAS f32x2*)(L + a_lr + u * 32);
#pragma unroll
            for (int u = 0; u < NSW; ++u) ov[u] = *(const LAS f32x4*)(L + a_mr + u * (4 * 132 * 4));
            const float M = fmaxf(fmaxf(ml[0].x, ml[1].x), fmaxf(ml[2].x, ml[3].x));
            float f[NSW];
#pragma unroll
            for (int u = 0; u < NSW; ++u) f[u] = ex2(ml[u].x - M);
            const f32x4 o = (f[0] * ov[0] + f[1] * ov[1]) + (f[2] * ov[2] + f[3] * ov[3]);
            __builtin_amdgcn_raw_buffer_store_b128(clean4(o), rws, o_pa, 0, AUX_SC1);
            const float Ls = fmaf(f[0], ml[0].y, f[1] * ml[1].y) + fmaf(f[2], ml[2].y, f[3] * ml[3].y);
            __builtin_amdgcn_raw_buffer_store_b64(v2u{no_sent(M), no_sent(Ls)}, rws, o_pl, 0, AUX_SC1);
        }
        STAMP(4);
        v4u WO[4];
        {
            v4u a[4]; v2u q2;
#define GATHER()                                                                                               \
    do {                                                                                                       \
        q2 = __builtin_amdgcn_raw_buffer_load_b64(rws, o_gl, 0, AUX_SC1);                                      \
        _Pragma("unroll") for (int k = 0; k < 4; ++k) a[k] = __builtin_amdgcn_raw_buffer_load_b128(rws, o_ga, k * 4096, AUX_SC1); \
    } while (0)
#define ARRIVED() __all(max(max(max(umax4(a[0]), umax4(a[1])), max(umax4(a[2]), umax4(a[3]))), max(q2.x, q2.y)) != SENT)
            wg_barrier();
            GATHER();
            __builtin_amdgcn_sched_barrier(0);
            STAMP(8);
            COUNT0(7);
#pragma unroll
            for (int u = 0; u < 4; ++u) {
                const v2u lo = tr_read(L + a_wo + 4096 * u), hi2 = tr_read(L + a_wo + 4096 * u + 512);
                WO[u] = v4u{lo.x, lo.y, hi2.x, hi2.y};
            }
            __builtin_amdgcn_sched_barrier(0);
            if (!ARRIVED()) {
#pragma unroll 1
                for (u32 spin = 0; spin < SPIN_CAP; ++spin) {
                    asm volatile("" ::: "memory");
                    GATHER();
                    COUNT(7);
                    if (ARRIVED()) break;
                }
            }
#undef ARRIVED
#undef GATHER
            STAMP(5);
            const float mp = __uint_as_float(q2.x);
            const float M = row_max(mp);
            const float wt = ex2(mp - M);
            const float inv = __builtin_amdgcn_rcpf(row_sum(wt * __uint_as_float(q2.y)));
            const f32x4 o = (dpp<0x150>(wt) * __builtin_bit_cast(f32x4, a[0]) + dpp<0x151>(wt) * __builtin_bit_cast(f32x4, a[1])) +
                            (dpp<0x152>(wt) * __builtin_bit_cast(f32x4, a[2]) + dpp<0x153>(wt) * __builtin_bit_cast(f32x4, a[3]));
            const auto s02 = __builtin_amdgcn_permlane32_swap(__float_as_uint(o[0]), __float_as_uint(o[2]), false, false);
            const auto s13 = __builtin_amdgcn_permlane32_swap(__float_as_uint(o[1]), __float_as_uint(o[3]), false, false);
            const float xs = __uint_as_float(s02[0]) + __uint_as_float(s02[1]), ys = __uint_as_float(s13[0]) + __uint_as_float(s13[1]);
            const auto sz = __builtin_amdgcn_permlane16_swap(__float_as_uint(xs), __float_as_uint(ys), false, false);
            const float z = (__uint_as_float(sz[0]) + __uint_as_float(sz[1])) * inv;
            *(LAS u16*)(L + a_ow) = (u16)pk_bf16(z, z);
        }
        wg_barrier();
        {
            v4u ob[4];
#pragma unroll
            for (int u = 0; u < 4; ++u) ob[u] = *(const LAS v4u*)(L + a_or + 64 * u);
            __builtin_amdgcn_sched_barrier(0);
            const f32x4 z = {0.f, 0.f, 0.f, 0.f};
            const f32x4 sp = mfma32(WO[2], ob[2], mfma32(WO[0], ob[0], z)) + mfma32(WO[3], ob[3], mfma32(WO[1], ob[1], z));
            __builtin_amdgcn_raw_buffer_store_b128(clean4(sp), rws, o_ps, 0, AUX_SC1);
        }
        STAMP(6);
        STAMPS_OUT(K_NSTAMP_S);
    } else {
        __builtin_amdgcn_s_setprio(3);
        v4u xr0, xr1; v2u ep;
        {
            const void* xa = p.x + 8 * lane;
            const void* ea = p.ws + WS_EPOCH;
            asm volatile("global_load_dwordx4 %0, %3, off\n\tglobal_load_dwordx4 %1, %3, off offset:1024\n\tglobal_load_dwordx2 %2, %4, off sc1"
                         : "=&v"(xr0), "=&v"(xr1), "=&v"(ep) : "v"(xa), "v"(ea) : "memory");
        }
        {
            const v4u sv = {SENT, SENT, SENT, SENT};
#pragma unroll
            for (int u = 0; u < 3; ++u) __builtin_amdgcn_raw_buffer_store_b128(sv, rws, myslot + u * 1024 + lane * 16, 0, AUX_SC1);
            const bool zt = s == 0 && (lane >> 2) == 2;
            const u32 o4 = lane < 4 ? WS_ML + ((pair * 4 + lane) * 16 + s) * 8 : lane < 6 ? myslot + SL_FA + (lane - 4) * 128 : zt ? WS_TK + (pair * NSW + (lane & 3)) * 256 : OOR;
            __builtin_amdgcn_raw_buffer_store_b64(zt ? v2u{0u, 0u} : v2u{SENT, SENT}, rws, o4, 0, AUX_SC1);
        }
        const u16* wqs = p.wq + (size_t)(lane >> 1) * 4096 + kvh * 512 + 16 * jq + 8 * (lane & 1);
#pragma unroll
        for (int u = 0; u < K_WQ; ++u) glds_sh(wqs + (size_t)u * 32 * 4096, lb + L_WQ + u * 1024);
        if (K_B0) wg_barrier();
        asm volatile("s_waitcnt vmcnt(%3)" : "+v"(xr0), "+v"(xr1), "+v"(ep) : "i"(4 + K_WQ) : "memory");
#pragma unroll
        for (int u = K_WQ; u < (K_NOWQ ? 0 : 16); ++u) { wait_vm<K_WQ - 1>(); glds_sh(wqs + (size_t)u * 32 * 4096, lb + L_WQ + u * 1024); }
        STAMP(1);
        const u64 tag = ((((u64)ep.y << 32) | ep.x) + 1) & 0xFFFFFFFFFFFFull;
        float xv[2][8];
#pragma unroll
        for (int u = 0; u < 4; ++u) { xv[0][2 * u] = bf_lo(xr0[u]); xv[0][2 * u + 1] = bf_hi(xr0[u]); xv[1][2 * u] = bf_lo(xr1[u]); xv[1][2 * u + 1] = bf_hi(xr1[u]); }
        layer_norm(xv, L + L_XN, lane);
        {
            v4u B[16];
            load_act(B, L + L_XN, lane);
            __builtin_amdgcn_sched_barrier(0);
            wait_vm<0>();
            STAMP(2);
            const f32x4 qv = gemv16(L + L_WQ, B, lane);
            const u32 qb = pk_bf16(sel4(qv, n >> 1), 0.f) & 0xffffu;
            GAS u64* dst = (GAS u64*)(p.ws + WS_QG) + (kvh * 2 + (n & 1)) * 512 + 16 * jq + 4 * g + (n >> 1);
            if (n < 8) __hip_atomic_store(dst, (tag << 16) | qb, RLX_AGENT);
        }
        STAMP(3);
        {
            GAS u64* src = (GAS u64*)(p.ws + WS_QG) + (kvh * 2 + b) * 512 + lane;
            u64 A[8];
            __builtin_amdgcn_s_sleep(K_SQ0);
#pragma unroll 1
            for (u32 spin = 0; spin < SPIN_CAP; ++spin) {
                bool ok = true;
#pragma unroll
                for (int k = 0; k < 8; ++k) A[k] = __hip_atomic_load(src + 64 * k, RLX_AGENT);
#pragma unroll
                for (int k = 0; k < 8; ++k) ok &= (A[k] >> 16) == tag;
                if (__all(ok)) break;
                __builtin_amdgcn_s_sleep(K_SQ1);
            }
#pragma unroll
            for (int k = 0; k < 8; ++k) *(LAS u16*)(L + L_Q + (64 * k + lane) * 2) = (u16)A[k];
        }
        *(LAS u32*)(L + L_CNT) = 0u;
        STAMP(4);
        wg_barrier();
        __builtin_amdgcn_s_setprio(1);
        layer_norm(xv, L + L_Y, lane);
        const int fb = bid >> 7, fq = (bid >> 3) & 15, hf = lane >> 5, fc = 64 * x8 + 4 * fq + 2 * hf;
        const u32 xo = *(const GAS u32*)(p.x + fb * DM + fc), b2o = *(const GAS u32*)(p.b2 + fc);
        const int col1 = do_m1 ? 16 * jm + 4 * g + ((n >> 1) & 3) : 0;
        const float bias1 = bf_lo(p.b1[col1]);
        if (do_m1 && !(X_SKIP & 8)) {
            const u16* s1 = p.w1 + (size_t)(lane >> 1) * FF + 16 * jm + 8 * (lane & 1);
#pragma unroll
            for (int u = 0; u < 16; ++u) { wait_vm<K_WW1 - 1>(); glds_sh(s1 + (size_t)u * 32 * FF, lb + L_W1 + u * 1024); }
        }
        const u16* so = p.wo + (size_t)(hi * 128 + (lane >> 3)) * DM + 64 * cg + 8 * (lane & 7);
        if (!K_WOLATE) {
#pragma unroll
            for (int u = 0; u < 16; ++u) { wait_vm<K_WWO - 1>(); glds_nt(so + (size_t)u * 8 * DM, lb + L_WO + u * 1024); }
        }
        if (do_m2 && !(X_SKIP & 16)) {
            const u16* s2 = p.w2 + (size_t)(hi * 64 + (lane >> 3)) * DM + 64 * cg + 8 * (lane & 7);
#pragma unroll
            for (int u = 0; u < 8; ++u) { wait_vm<K_WWO - 1>(); glds_nt(s2 + (size_t)u * 8 * DM, lb + L_W2 + u * 1024); }
        }
        wait_vm<0>();
        STAMP(10);
        if (do_m1) {
            v4u B[16];
            load_act(B, L + L_Y, lane);
            const float u = rbf(rbf(sel4(gemv16(L + L_W1, B, lane), n >> 1)) + bias1);
            const float z = 1.5957691216057308f * fmaf(0.044715f * u * u, u, u);
            const float h = u / (1.f + __expf(-z));
            if (n < 8) __hip_atomic_store((GAS u64*)(p.ws + WS_HG) + (n & 1) * FF + col1, (tag << 16) | (pk_bf16(h, 0.f) & 0xffffu), RLX_AGENT);
        }
        const int pi = lane & 31, pw = ((pi & 3) * 8 + x8) * 8 + (pi >> 2);
        const u32 pslot = WS_SLOT + pw * 4096 + fb * 256 + fq * 16 + hf * 8;
        {
            const int ek = hf ? (pw >> 4) & 7 : x8, ej = hf ? 4 * (pw & 7) + 2 * (pw >> 7) + ((pw >> 3) & 1) : pi;
            GAS u64* ev = (GAS u64*)(p.ws + WS_QG) + ((X_SKIP & 1) ? kvh * 1024 + 16 * jq : ek * 1024 + 16 * ej);
            GAS u64* hs = do_m2 && !(X_SKIP & 4) ? (GAS u64*)(p.ws + WS_HG) + 64 * hi + lane : ev;
            const int hstr = do_m2 && !(X_SKIP & 4) ? FF : 0;
            u64 e, h0, h1;
            if (!do_m1) { __builtin_amdgcn_s_sleep(K_SH0); __builtin_amdgcn_s_sleep(K_SH0); }
            __builtin_amdgcn_s_sleep(K_SH0);
#pragma unroll 1
            for (u32 spin = 0; spin < SPIN_CAP; ++spin) {
                e = __hip_atomic_load(ev, RLX_AGENT); h0 = __hip_atomic_load(hs, RLX_AGENT); h1 = __hip_atomic_load(hs + hstr, RLX_AGENT);
                if (__all(((e >> 16) == tag) & ((h0 >> 16) == tag) & ((h1 >> 16) == tag))) break;
                __builtin_amdgcn_s_sleep(K_SH1);
            }
            *(LAS u16*)(L + L_H + lane * 2) = (u16)h0;
            *(LAS u16*)(L + L_H + 128 + lane * 2) = (u16)h1;
        }
        if (do_m2) {
#pragma unroll
            for (int cb = 0; cb < 4; ++cb) {
                f32x4 mp = {0.f, 0.f, 0.f, 0.f};
#pragma unroll
                for (int u = 0; u < 2; ++u) mp = mfma32(tr_frag(L + L_W2 + 32 * cb, 128, 32 * u, lane), *(const LAS v4u*)(L + L_H + (n & 1) * 128 + 64 * u + 16 * g), mp);
                __builtin_amdgcn_raw_buffer_store_b128(clean4(mp), rws, n < 2 ? myslot + SL_PM + n * 256 + cb * 64 + g * 16 : OOR, 0, AUX_SC1);
            }
        }
        STAMP(5);
        float gate[2];
        {
            v2u a;
            __builtin_amdgcn_s_sleep(K_SH0);
#pragma unroll 1
            for (u32 spin = 0; spin < SPIN_CAP; ++spin) {
                asm volatile("" ::: "memory");
                a = __builtin_amdgcn_raw_buffer_load_b64(rws, pi < 28 && !(X_SKIP & 2) ? pslot + SL_PM : OOR, 0, AUX_SC1);
                if (__all(max(a.x, a.y) != SENT)) break;
                __builtin_amdgcn_s_sleep(K_SH1);
            }
#pragma unroll
            for (int k = 0; k < 2; ++k) {
                const float mm = rbf(rbf(x16_sum(row_sum(__uint_as_float(a[k])))) + (k ? bf_hi(b2o) : bf_lo(b2o)));
                gate[k] = rbf(mm / (1.f + __expf(-mm)));
            }
        }
        STAMP(11);
        __builtin_amdgcn_s_setprio(3);
        const u32 fme = lane == 0 ? myslot + SL_FA : OOR;
        const u32 fao = lane < 32 ? WS_SLOT + (((lane >> 4) * 8 + x8) * 16 + (lane & 15)) * 4096 + SL_FA : OOR;
        const u32 fbo = lane < 32 ? WS_SLOT + pw * 4096 + SL_FB : OOR;
#define WAIT_WORDS(o)                                                                                          \
    _Pragma("unroll 1") for (u32 spin = 0; spin < SPIN_CAP; ++spin) {                                          \
        asm volatile("" ::: "memory");                                                                         \
        if (__all(__builtin_amdgcn_raw_buffer_load_b32(rws, o, 0, AUX_SC1) != SENT)) break;                    \
        __builtin_amdgcn_s_sleep(K_SP);                                                                        \
    }
#define WAIT_CNT(c)                                                                                            \
    _Pragma("unroll 1") for (u32 spin = 0; spin < 16 * SPIN_CAP; ++spin) {                                     \
        if (__hip_atomic_load((LAS u32*)(L + L_CNT), __ATOMIC_RELAXED, __HIP_MEMORY_SCOPE_WORKGROUP) >= (c)) break; \
        __builtin_amdgcn_s_sleep(1);                                                                           \
    }
        if (K_WOLATE) {
            WAIT_CNT(K_WOLATE);
#pragma unroll
            for (int u = 0; u < 16; ++u) glds_nt(so + (size_t)u * 8 * DM, lb + L_WO + u * 1024);
        }
        WAIT_CNT(NSW);
#undef WAIT_CNT
        __builtin_amdgcn_raw_buffer_store_b32(1u, rws, fme, 0, AUX_SC1);
        wg_barrier();
        WAIT_WORDS(fao);
        wait_vm<0>();
        if (K_DA) __builtin_amdgcn_s_sleep(K_DA);
        if (K_ANNB == 1) __builtin_amdgcn_raw_buffer_store_b32(1u, rws, fme + (SL_FB - SL_FA), 0, AUX_SC1);
        wg_barrier();
        if (K_ANNB == 2) { __builtin_amdgcn_s_sleep(K_DC); __builtin_amdgcn_raw_buffer_store_b32(1u, rws, fme + (SL_FB - SL_FA), 0, AUX_SC1); }
        wg_barrier();
        STAMP(6);
        {
            v2u a;
            WAIT_WORDS(fbo);
            if (K_DB) __builtin_amdgcn_s_sleep(K_DB);
#undef WAIT_WORDS
            COUNT0(9);
#pragma unroll 1
            for (u32 spin = 0; spin < SPIN_CAP; ++spin) {
                asm volatile("" ::: "memory");
                a = __builtin_amdgcn_raw_buffer_load_b64(rws, pslot + SL_PS, 0, AUX_SC1);
                if (__all(max(a.x, a.y) != SENT)) break;
                COUNT(9);
                if (K_SC) __builtin_amdgcn_s_sleep(K_SC);
            }
            STAMP(7);
            const float s0 = rbf(x16_sum(row_sum(__uint_as_float(a.x)))), s1 = rbf(x16_sum(row_sum(__uint_as_float(a.y))));
            const u32 r = pk_bf16(bf_lo(xo) + rbf(s0 * gate[0]), bf_hi(xo) + rbf(s1 * gate[1]));
            if (pi == 0) *(GAS u32*)(p.out + fb * DM + fc) = r;
            if (bid == 0 && lane == 0) __hip_atomic_store((GAS u64*)(p.ws + WS_EPOCH), (((u64)ep.y << 32) | ep.x) + 1, RLX_AGENT);
        }
        STAMP(8);
        STAMPS_OUT(K_NSTAMP_H);
    }
}

extern "C" void kernel_launch(void* const* d_in, const int* in_sizes, int n_in, void* d_out, int out_size, void* d_ws, size_t ws_size,
                              hipStream_t stream) {
    Params p;
    p.x = (const u16*)d_in[0]; p.k = (const u16*)d_in[1]; p.v = (const u16*)d_in[2]; p.wq = (const u16*)d_in[3]; p.wo = (const u16*)d_in[4];
    p.w1 = (const u16*)d_in[6]; p.b1 = (const u16*)d_in[7]; p.w2 = (const u16*)d_in[8]; p.b2 = (const u16*)d_in[9];
    p.out = (u16*)d_out; p.ws = (unsigned char*)d_ws;
    constexpr int LDS_BYTES = L_END > 96 * 1024 ? L_END : 96 * 1024;
    static_assert(LDS_BYTES <= 160 * 1024, "");
    (void)hipFuncSetAttribute((const void*)fused_decode, hipFuncAttributeMaxDynamicSharedMemorySize, LDS_BYTES);
    hipLaunchKernelGGL(fused_decode, dim3(NWG), dim3(NTHR), LDS_BYTES, stream, p);
}
```

```cpp
#include <hip/hip_runtime.h>
#include <hip/hip_bf16.h>
#include <stdint.h>

#define GAS __attribute__((address_space(1)))
#define LAS __attribute__((address_space(3)))
typedef unsigned u32;
typedef unsigned long long u64;
typedef unsigned short u16;
typedef unsigned v4u __attribute__((ext_vector_type(4)));
typedef unsigned v2u __attribute__((ext_vector_type(2)));
typedef float f32x4 __attribute__((ext_vector_type(4)));
typedef float f32x2 __attribute__((ext_vector_type(2)));
typedef short s16x4 __attribute__((ext_vector_type(4)));
typedef __bf16 bf16x8 __attribute__((ext_vector_type(8)));
typedef __bf16 bf16x2 __attribute__((ext_vector_type(2)));

#define K_WK 5
#define K_WV 6
#define K_WQ 8
#define K_W0 6
#define K_S 29
#define K_SQ0 100
#define K_SQ1 24
#define K_SH0 64
#define K_SH1 64
#define K_WW1 3
#define K_WWO 4
#define K_OUTSC1 0
#define K_SB 0
#define K_SC 0
#define K_ANN 5
#define K_DA 0
#define K_DB 0
#define K_ANNB 2
#define K_SP 1
#define K_DC 8
#define K_WOLATE 2
#define K_NB 16
#define X_SKIP 0
#define K_WKD 6
#define K_WVD 7
#define K_TP 5
#define K_NW 0
#define K_WOW 0
#define K_ROT 3
#define K_SHNT 0
#define K_NOWQ 0
#define K_B0 0
#define K_TS 0

constexpr int DM = 512, HD = 128, SK = 32768, FF = 1792;
constexpr int NWG = 256, NSW = 4, NTHR = (NSW + 1) * 64;
constexpr int NS = K_S, NST = NS + 1;
constexpr int LP = 4, RP = 8, NPRO = LP + RP + 2;
constexpr int NKU = NS - NPRO;
constexpr int POOL = SK / 16 / NSW - 16 * NST;
static_assert(NKU >= 3 && NS <= 32 && POOL >= 0, "");
constexpr float SC = 0.08838834764831845f * 1.4426950408889634f;
constexpr u32 SENT = 0xFFFFFFFFu;
constexpr u32 SPIN_CAP = 1u << 18;
constexpr u32 OOR = 0xFFFFFF00u;

constexpr u32 WS_EPOCH = 0;
constexpr u32 WS_QG = 4096;
constexpr u32 WS_HG = WS_QG + 8 * 2 * 512 * 8;
constexpr u32 WS_TK = 102400;
constexpr u32 WS_ML = 118784;
constexpr u32 WS_SLOT = 131072;
constexpr u32 SL_PS = 2048, SL_PM = 2560, SL_FA = 3072, SL_FB = 3200;
constexpr u32 WS_END = WS_SLOT + NWG * 4096;
constexpr u32 WS_TS = WS_END;
static_assert(WS_HG + 2 * FF * 8 <= WS_TK && WS_TK + 64 * 256 <= WS_ML && WS_ML + 8192 <= WS_SLOT, "");

constexpr u32 L_WQ = 0, L_W1 = 0, L_WO = 16384, L_W2 = 32768;
constexpr u32 L_XN = 40960, L_Y = L_XN + 2048, L_Q = L_Y + 2048, L_H = L_Q + 1024, L_O = L_H + 256;
constexpr u32 L_MRG = L_O + 512;
constexpr u32 L_MML = L_MRG + NSW * 4 * 132 * 4;
constexpr u32 L_CNT = L_MML + NSW * 4 * 2 * 4;
constexpr u32 L_SC = 57344;
constexpr u32 L_PARK = L_SC + NSW * 8192;
constexpr u32 L_END = L_PARK + NSW * LP * 4096;
static_assert(L_CNT + 16 <= L_SC, "");

struct Params {
    const u16 *x, *k, *v, *wq, *wo, *w1, *b1, *w2, *b2;
    u16* out;
    unsigned char* ws;
};

__device__ __forceinline__ f32x4 mfma32(v4u a, v4u b, f32x4 c) {
    return __builtin_amdgcn_mfma_f32_16x16x32_bf16(__builtin_bit_cast(bf16x8, a), __builtin_bit_cast(bf16x8, b), c, 0, 0, 0);
}
__device__ __forceinline__ f32x4 mfma16(u32 a0, u32 a1, u32 b0, u32 b1, f32x4 c) {
    return __builtin_amdgcn_mfma_f32_16x16x16bf16_1k(__builtin_bit_cast(s16x4, v2u{a0, a1}), __builtin_bit_cast(s16x4, v2u{b0, b1}), c, 0, 0, 0);
}
__device__ __forceinline__ u32 pk_bf16(float lo, float hi) {
    return __builtin_bit_cast(u32, __builtin_convertvector(f32x2{lo, hi}, bf16x2));
}
__device__ __forceinline__ float bf_lo(u32 w) { return __builtin_bit_cast(float, w << 16); }
__device__ __forceinline__ float bf_hi(u32 w) { return __builtin_bit_cast(float, w & 0xffff0000u); }
__device__ __forceinline__ float rbf(float f) { return bf_lo(pk_bf16(f, f)); }
__device__ __forceinline__ float ex2(float x) { return __builtin_amdgcn_exp2f(x); }
template <int CTRL> __device__ __forceinline__ float dpp(float x) {
    return __builtin_bit_cast(float, __builtin_amdgcn_mov_dpp(__builtin_bit_cast(int, x), CTRL, 0xf, 0xf, true));
}
constexpr int XOR1 = 0xB1, XOR2 = 0x4E, XOR7 = 0x141, ROR8 = 0x128;
__device__ __forceinline__ float x16_max(float x) {
    auto s = __builtin_amdgcn_permlane16_swap(__float_as_uint(x), __float_as_uint(x), false, false);
    return fmaxf(__uint_as_float(s[0]), __uint_as_float(s[1]));
}
__device__ __forceinline__ float x32_max(float x) {
    auto s = __builtin_amdgcn_permlane32_swap(__float_as_uint(x), __float_as_uint(x), false, false);
    return fmaxf(__uint_as_float(s[0]), __uint_as_float(s[1]));
}
__device__ __forceinline__ float x16_sum(float x) {
    auto s = __builtin_amdgcn_permlane16_swap(__float_as_uint(x), __float_as_uint(x), false, false);
    return __uint_as_float(s[0]) + __uint_as_float(s[1]);
}
__device__ __forceinline__ float x32_sum(float x) {
    auto s = __builtin_amdgcn_permlane32_swap(__float_as_uint(x), __float_as_uint(x), false, false);
    return __uint_as_float(s[0]) + __uint_as_float(s[1]);
}
__device__ __forceinline__ float row_sum(float x) {
    x += dpp<XOR1>(x); x += dpp<XOR2>(x); x += dpp<XOR7>(x); x += dpp<ROR8>(x); return x;
}
__device__ __forceinline__ float row_max(float x) {
    float r;
    asm("s_nop 1\n\tv_max_f32_dpp %0, %1, %1 quad_perm:[1,0,3,2] row_mask:0xf bank_mask:0xf\n\ts_nop 1\n\t"
        "v_max_f32_dpp %0, %0, %0 quad_perm:[2,3,0,1] row_mask:0xf bank_mask:0xf\n\ts_nop 1\n\t"
        "v_max_f32_dpp %0, %0, %0 row_half_mirror row_mask:0xf bank_mask:0xf\n\ts_nop 1\n\t"
        "v_max_f32_dpp %0, %0, %0 row_ror:8 row_mask:0xf bank_mask:0xf" : "=&v"(r) : "v"(x));
    return r;
}
__device__ __forceinline__ float wave_sum(float x) { return x32_sum(x16_sum(row_sum(x))); }
__device__ __forceinline__ u32 umax4(v4u a) { return max(max(a.x, a.y), max(a.z, a.w)); }
__device__ __forceinline__ u32 no_sent(float f) { return min(__float_as_uint(f), 0xFFFFFFFEu); }

#define RLX_AGENT __ATOMIC_RELAXED, __HIP_MEMORY_SCOPE_AGENT
constexpr int AUX_NT = 2, AUX_SC1 = 16;
__device__ __forceinline__ __amdgpu_buffer_rsrc_t mk_rsrc(const void* p, u32 bytes) {
    return __builtin_amdgcn_make_buffer_rsrc((void*)p, 0, bytes, 0x00020000);
}
__device__ __forceinline__ void glds_nt(const void* src, u32 dst) {
    asm volatile("s_mov_b32 m0, %1\n\ts_nop 0\n\tglobal_load_lds_dwordx4 %0, off nt" ::"v"(src), "s"(dst) : "memory");
}
__device__ __forceinline__ void glds_sh(const void* src, u32 dst) {
    asm volatile("s_mov_b32 m0, %1\n\ts_nop 0\n\tglobal_load_lds_dwordx4 %0, off" ::"v"(src), "s"(dst) : "memory");
}
template <int OFF> __device__ __forceinline__ void glds_nt_off(const void* src, u32 dst) {
    asm volatile("s_mov_b32 m0, %1\n\ts_nop 0\n\tglobal_load_lds_dwordx4 %0, off offset:%2 nt" ::"v"(src), "s"(dst), "i"(OFF) : "memory");
}
template <int N> __device__ __forceinline__ void wait_vm() { asm volatile("s_waitcnt vmcnt(%0)" ::"i"(N) : "memory"); }
__device__ __forceinline__ void wg_barrier() { asm volatile("s_waitcnt lgkmcnt(0)\n\ts_barrier" ::: "memory"); }
__device__ __forceinline__ v2u tr_read(const LAS unsigned char* p) {
    return __builtin_bit_cast(v2u, __builtin_amdgcn_ds_read_tr16_b64_v4i16((LAS s16x4*)p));
}
__device__ __forceinline__ v4u tr_frag(const LAS unsigned char* img, int pitch, int row0, int lane) {
    const int g = lane >> 4, q = (lane >> 2) & 3, p = lane & 3;
    const LAS unsigned char* a = img + (row0 + 8 * g + q) * pitch + 8 * p;
    const v2u lo = tr_read(a), hi = tr_read(a + 4 * pitch);
    return v4u{lo.x, lo.y, hi.x, hi.y};
}
#define STAMP(i) do { } while (0)
#define STAMPS_OUT(N) do { } while (0)
#define COUNT0(i) do { } while (0)
#define COUNT(i) do { } while (0)

__device__ __forceinline__ void load_act(v4u (&B)[16], const LAS unsigned char* act, int lane) {
    const LAS unsigned char* ap = act + (lane & 1) * 1024 + (lane >> 4) * 16;
#pragma unroll
    for (int s = 0; s < 16; ++s) B[s] = *(const LAS v4u*)(ap + 64 * s);
}
__device__ __forceinline__ f32x4 gemv16(const LAS unsigned char* img, const v4u (&B)[16], int lane) {
    v4u A[16];
#pragma unroll
    for (int s = 0; s < 16; ++s) A[s] = tr_frag(img, 32, 32 * s, lane);
    __builtin_amdgcn_sched_barrier(0);
    f32x4 a[4];
#pragma unroll
    for (int s = 0; s < 16; ++s) a[s & 3] = mfma32(A[s], B[s], s < 4 ? f32x4{0.f, 0.f, 0.f, 0.f} : a[s & 3]);
    return (a[0] + a[1]) + (a[2] + a[3]);
}
__device__ __forceinline__ void layer_norm(float (&v)[2][8], LAS unsigned char* dst, int lane) {
    float mean[2], rs[2];
#pragma unroll
    for (int bb = 0; bb < 2; ++bb) mean[bb] = wave_sum(((v[bb][0] + v[bb][1]) + (v[bb][2] + v[bb][3])) + ((v[bb][4] + v[bb][5]) + (v[bb][6] + v[bb][7]))) * (1.f / DM);
#pragma unroll
    for (int bb = 0; bb < 2; ++bb) {
        float sq = 0.f;
#pragma unroll
        for (int u = 0; u < 8; ++u) { v[bb][u] -= mean[bb]; sq = fmaf(v[bb][u], v[bb][u], sq); }
        rs[bb] = __builtin_amdgcn_rsqf(wave_sum(sq) * (1.f / DM) + 1e-5f);
    }
#pragma unroll
    for (int bb = 0; bb < 2; ++bb) {
        v4u o;
#pragma unroll
        for (int u = 0; u < 4; ++u) {
            o[u] = pk_bf16(v[bb][2 * u] * rs[bb], v[bb][2 * u + 1] * rs[bb]);
            v[bb][2 * u] = bf_lo(o[u]); v[bb][2 * u + 1] = bf_hi(o[u]);
        }
        *(LAS v4u*)(dst + bb * 1024 + lane * 16) = o;
    }
}
__device__ __forceinline__ float sel4(f32x4 v, int r) {
    const bool b0 = r & 1, b1 = r & 2;
    const float lo = b0 ? v[1] : v[0], hi = b0 ? v[3] : v[2];
    return b1 ? hi : lo;
}
__device__ __forceinline__ v4u clean4(f32x4 v) { return v4u{no_sent(v[0]), no_sent(v[1]), no_sent(v[2]), no_sent(v[3])}; }

__global__ void __launch_bounds__(NTHR, 2) fused_decode(Params p) {
    extern __shared__ __attribute__((aligned(16))) unsigned char lds_raw[];
    LAS unsigned char* L = (LAS unsigned char*)lds_raw;
    const u32 lb = (u32)(size_t)L;
    const int tid = threadIdx.x, lane = tid & 63, w = __builtin_amdgcn_readfirstlane(tid >> 6);
    const int bid = blockIdx.x;
    const int pair = bid >> 4, b = pair >> 3, kvh = pair & 7, s = bid & 15;
    const int jq = 4 * (s & 7) + 2 * b + (s >> 3);
    const int m1i = (s - kvh) & 7, jm = 4 * (7 * (kvh >> 1) + 4 * (kvh & 1) + m1i) + (jq & 3);
    const int x8 = bid & 7, hi = 4 * x8 + (bid >> 6), cg = (bid >> 3) & 7;
    const bool do_m1 = m1i < 4 - (kvh & 1), do_m2 = hi < 28;
    const int g = lane >> 4, n = lane & 15;
    const __amdgpu_buffer_rsrc_t rws = mk_rsrc(p.ws, WS_END);
    const u32 myslot = WS_SLOT + bid * 4096;
    STAMP(0);

    if (w < NSW) {
        const u16* kb = p.k + (size_t)pair * SK * HD;
        const u16* vb = p.v + (size_t)pair * SK * HD;
        const __amdgpu_buffer_rsrc_t rk = mk_rsrc(kb, SK * HD * 2), rv = mk_rsrc(vb, SK * HD * 2);
        constexpr u32 TSTR = NSW * 4096;
        const u32 kl = (n >> 2) * 256 + g * 64 + (n & 3) * 16, vl = lane * 16;
        u32 ko = (s * NST * NSW + w) * 4096 + kl, vo = (s * NST * NSW + w) * 4096 + vl;
        v4u R[3][4], KX[RP][4], Ka[4], Va[4], Kb[4], Vb[4];
        constexpr int WK = K_WK, WV = K_WV;
#define LK(X, u) do { wait_vm<WK - 1>(); X[u] = __builtin_amdgcn_raw_buffer_load_b128(rk, ko + 1024 * (u), 0, AUX_NT); __builtin_amdgcn_sched_barrier(0); } while (0)
#define LV(X, u) do { wait_vm<WV - 1>(); X[u] = __builtin_amdgcn_raw_buffer_load_b128(rv, vo + 1024 * (u), 0, AUX_NT); __builtin_amdgcn_sched_barrier(0); } while (0)
        if (K_B0) wg_barrier();
        const u32 park = lb + L_PARK + w * LP * 4096;
#pragma unroll
        for (int t = 0; t < LP; ++t) {
            const unsigned char* ks = (const unsigned char*)kb + ko;
            wait_vm<K_W0 - 1>(); glds_nt_off<0>(ks, park + t * 4096);
            wait_vm<K_W0 - 1>(); glds_nt_off<1024>(ks, park + t * 4096);
            wait_vm<K_W0 - 1>(); glds_nt_off<2048>(ks, park + t * 4096);
            wait_vm<K_W0 - 1>(); glds_nt_off<3072>(ks, park + t * 4096);
            ko += TSTR;
        }
#pragma unroll
        for (int t = 0; t < RP; ++t) { LK(KX[t], 0); LK(KX[t], 1); LK(KX[t], 2); LK(KX[t], 3); ko += TSTR; }
#pragma unroll
        for (int t = 0; t < 2; ++t) { LK(R[t], 0); LK(R[t], 1); LK(R[t], 2); LK(R[t], 3); ko += TSTR; }
        STAMP(1);
        wg_barrier();
        STAMP(2);
        const int jj = n & 3;
        const bool j0 = jj & 1, j1 = jj & 2;
        const v4u Q = *(const LAS v4u*)(L + L_Q + (n >> 2) * 256 + 64 * g + 16 * jj);
        const u32 mask0 = jj == 0 ? 0x0000ffffu : jj == 1 ? 0xffff0000u : 0u, mask1 = jj == 2 ? 0x0000ffffu : jj == 3 ? 0xffff0000u : 0u;
        float m = -INFINITY, l = 0.f, sc[4], pr[4];
        f32x4 acc0 = {0.f, 0.f, 0.f, 0.f}, acc1 = {0.f, 0.f, 0.f, 0.f};
#define QKD(KR)                                                                                                \
        f32x4 dd[4];                                                                                           \
        _Pragma("unroll") for (int u = 0; u < 4; ++u) dd[u] = mfma32(KR[u], Q, f32x4{0.f, 0.f, 0.f, 0.f});     \
        asm volatile("" : "+v"(dd[0]), "+v"(dd[1]), "+v"(dd[2]), "+v"(dd[3]));     \
        _Pragma("unroll") for (int u = 0; u < 4; ++u) sc[u] = sel4(dd[u], jj);
        LAS float* scp = (LAS float*)(L + L_SC + w * 8192) + lane;
#define KP(KR, idx)                                                                                            \
    do {                                                                                                       \
        QKD(KR)                                                                                                \
        const float y01 = (j0 ? sc[1] : sc[0]) + dpp<XOR1>(j0 ? sc[0] : sc[1]), y23 = (j0 ? sc[3] : sc[2]) + dpp<XOR1>(j0 ? sc[2] : sc[3]); \
        const float y = (j1 ? y23 : y01) + dpp<XOR2>(j1 ? y01 : y23);                                          \
        m = fmaxf(m, y);                                                                                       \
        scp[(idx) * 64] = y;                                                                                   \
        __builtin_amdgcn_sched_barrier(0);                                                                     \
    } while (0)
#define UNPARK(t, KR)                                                                                          \
    do {                                                                                                       \
        const LAS unsigned char* pk = L + L_PARK + (w * LP + (t)) * 4096 + lane * 16;                          \
        _Pragma("unroll") for (int u = 0; u < 4; ++u) KR[u] = *(const LAS v4u*)(pk + 1024 * u);                \
    } while (0)
#define VP(VR, sv)                                                                                             \
    do {                                                                                                       \
        const float p1 = ex2(fmaf(sv, SC, -mc));                                                               \
        l += p1;                                                                                               \
        const int pb = pk_bf16(p1, p1);                                                                        \
        _Pragma("unroll") for (int r = 0; r < 4; ++r) {                                                        \
            const u32 t_ = r == 0 ? __builtin_amdgcn_mov_dpp(pb, 0x00, 0xf, 0xf, true) : r == 1 ? __builtin_amdgcn_mov_dpp(pb, 0x55, 0xf, 0xf, true) \
                         : r == 2 ? __builtin_amdgcn_mov_dpp(pb, 0xAA, 0xf, 0xf, true) : __builtin_amdgcn_mov_dpp(pb, 0xFF, 0xf, 0xf, true); \
            acc0 = mfma16(VR[r].x, VR[r].y, t_ & mask0, t_ & mask1, acc0);                                     \
            acc1 = mfma16(VR[r].z, VR[r].w, t_ & mask0, t_ & mask1, acc1);                                     \
        }                                                                                                      \
        __builtin_amdgcn_sched_barrier(0);                                                                     \
    } while (0)
#define UKK(u, idx) do { LK(R[((u) + 2) % 3], 0); LK(R[((u) + 2) % 3], 1); KP(R[(u) % 3], idx); LK(R[((u) + 2) % 3], 2); LK(R[((u) + 2) % 3], 3); ko += TSTR; } while (0)
#define UVK(u, idx) do { LV(R[((u) + 2) % 3], 0); LV(R[((u) + 2) % 3], 1); KP(R[(u) % 3], idx); LV(R[((u) + 2) % 3], 2); LV(R[((u) + 2) % 3], 3); vo += TSTR; } while (0)
#define UVV(u, idx) do { const float sv_ = scp[(idx) * 64]; LV(R[((u) + 2) % 3], 0); LV(R[((u) + 2) % 3], 1); VP(R[(u) % 3], sv_); LV(R[((u) + 2) % 3], 2); LV(R[((u) + 2) % 3], 3); vo += TSTR; } while (0)
        static_assert(LP == 4 && RP == 8, "");
        LK(R[2], 0); KP(KX[0], 4); UNPARK(0, KX[0]); LK(R[2], 1); KP(R[0], 12); KP(KX[1], 5); UNPARK(1, KX[1]); LK(R[2], 2); KP(KX[2], 6); LK(R[2], 3); KP(KX[3], 7); ko += TSTR;
        LK(R[0], 0); KP(KX[0], 0); UNPARK(2, KX[2]); LK(R[0], 1); KP(R[1], 13); KP(KX[1], 1); UNPARK(3, KX[3]); LK(R[0], 2); KP(KX[4], 8); LK(R[0], 3); KP(KX[5], 9); ko += TSTR;
        LK(R[1], 0); KP(KX[2], 2); LK(R[1], 1); KP(R[2], 14); KP(KX[3], 3); LK(R[1], 2); KP(KX[6], 10); LK(R[1], 3); KP(KX[7], 11); ko += TSTR;
        constexpr int KA = (NKU - 3) / 3, KR_ = (NKU - 3) % 3;
        scp += 15 * 64;
#pragma unroll 1
        for (int it = 0; it < KA; ++it) { UKK(0, 0); UKK(1, 1); UKK(2, 2); scp += 3 * 64; }
        if (KR_ > 0) UKK(0, 0);
        if (KR_ > 1) UKK(1, 1);
        STAMP(9);
        UVK(KR_, KR_); UVK(KR_ + 1, KR_ + 1);
        m = fmaxf(m, dpp<XOR1>(m)); m = fmaxf(m, dpp<XOR2>(m)); m = x32_max(x16_max(m));
        const float mc = m * SC;
        constexpr int U0 = KR_ + 2, VA = (NS - 2) / 3, VR_ = (NS - 2) % 3;
        scp -= (15 + 3 * KA) * 64;
#pragma unroll 1
        for (int it = 0; it < VA; ++it) { UVV(U0, 0); UVV(U0 + 1, 1); UVV(U0 + 2, 2); scp += 3 * 64; }
        if (VR_ > 0) UVV(U0, 0);
        if (VR_ > 1) UVV(U0 + 1, 1);
#define QK(KR)                                                                                                 \
    do {                                                                                                       \
        QKD(KR)                                                                                                \
        _Pragma("unroll") for (int u = 0; u < 4; ++u) sc[u] += dpp<XOR1>(sc[u]);                               \
        _Pragma("unroll") for (int u = 0; u < 4; ++u) sc[u] += dpp<XOR2>(sc[u]);                               \
        __builtin_amdgcn_sched_barrier(0);                                                                     \
    } while (0)
#define SOFTMAX()                                                                                              \
    do {                                                                                                       \
        const float tm = x32_max(x16_max(fmaxf(fmaxf(sc[0], sc[1]), fmaxf(sc[2], sc[3]))));                    \
        const float mn = fmaxf(m, tm), mc2 = mn * SC, alpha = ex2((m - mn) * SC);                              \
        _Pragma("unroll") for (int r = 0; r < 4; ++r) pr[r] = ex2(fmaf(sc[r], SC, -mc2));                      \
        l = fmaf(l, alpha, (pr[0] + pr[1]) + (pr[2] + pr[3]));                                                 \
        m = mn;                                                                                                \
        acc0 *= alpha; acc1 *= alpha;                                                                          \
        __builtin_amdgcn_sched_barrier(0);                                                                     \
    } while (0)
#define PV(VR, r0, r1)                                                                                         \
    do {                                                                                                       \
        _Pragma("unroll") for (int r = r0; r < r1; ++r) {                                                      \
            const u32 pb = pk_bf16(pr[r], pr[r]), x0 = pb & mask0, x1 = pb & mask1;                            \
            acc0 = mfma16(VR[r].x, VR[r].y, x0, x1, acc0);                                                     \
            acc1 = mfma16(VR[r].z, VR[r].w, x0, x1, acc1);                                                     \
        }                                                                                                      \
        __builtin_amdgcn_sched_barrier(0);                                                                     \
    } while (0)
        u32 tko = lane == 0 ? WS_TK + (pair * NSW + w) * 256 : OOR;
#define TICKET(o) __builtin_amdgcn_raw_ptr_buffer_atomic_add_i32(1, rws, o, 0, AUX_SC1)
#define CLAIM()                                                                                                \
    do {                                                                                                       \
        t = __builtin_amdgcn_readfirstlane(tk);                                                                \
        more = t < (u32)POOL;                                                                                  \
        const u32 to = more ? (16 * NST * NSW + NSW * t + w) * 4096 : 0x40000000u;                             \
        const u32 tm_ = more ? 0u : OOR;                                                                       \
        asm volatile("v_add_u32 %0, %3, %4\n\tv_add_u32 %1, %3, %5\n\tv_or_b32 %2, %6, %2" : "+v"(ko), "+v"(vo), "+v"(tko) : "s"(to), "v"(kl), "v"(vl), "s"(tm_)); \
        if (K_TP == 0) tk = TICKET(tko);                                                                       \
    } while (0)
        STAMP(10);
        int tk;
        {
            constexpr int U1 = U0 + VR_;
            const float s0 = scp[VR_ * 64], s1 = scp[(VR_ + 1) * 64];
            if (K_TP < 2) tk = TICKET(tko);
            LK(Ka, 0); LK(Ka, 1);
            if (K_TP >= 2 && K_TP < 4) tk = TICKET(tko);
            VP(R[U1 % 3], s0); LK(Ka, 2); LK(Ka, 3);
            if (K_TP >= 4 && K_TP < 6) tk = TICKET(tko);
            LV(Va, 0); LV(Va, 1);
            if (K_TP >= 6) tk = TICKET(tko);
            VP(R[(U1 + 1) % 3], s1); LV(Va, 2); LV(Va, 3);
            l += dpp<XOR1>(l); l += dpp<XOR2>(l);
        }
        bool more;
        u32 t;
#define WAITC(N) asm volatile("s_cmpk_ge_u32 %0, %2\n\ts_cbranch_scc1 1f\n\ts_waitcnt vmcnt(%1)\n1:" ::"s"(t), "i"(N), "i"(POOL > K_NB ? POOL - K_NB : 0) : "memory", "scc")
#define LKC(X, u) do { WAITC(K_WKD - 1); X[u] = __builtin_amdgcn_raw_buffer_load_b128(rk, ko + 1024 * (u), 0, AUX_NT); __builtin_amdgcn_sched_barrier(0); } while (0)
#define LVC(X, u) do { WAITC(K_WVD - 1); X[u] = __builtin_amdgcn_raw_buffer_load_b128(rv, vo + 1024 * (u), 0, AUX_NT); __builtin_amdgcn_sched_barrier(0); } while (0)
#define TKT(k) do { if (K_TP == (k)) { tk = TICKET(tko); __builtin_amdgcn_sched_barrier(0); } } while (0)
#define STEPD(KC, VC, KN, VN)                                                                                  \
    do {                                                                                                       \
        HOOK(5); LKC(KN, 0); TKT(1); LKC(KN, 1); TKT(2); LKC(KN, 2); TKT(3); LKC(KN, 3); TKT(4); LVC(VN, 0); TKT(5); QK(KC); HOOK(4); LVC(VN, 1); TKT(6); SOFTMAX(); \
        LVC(VN, 2); TKT(7); HOOK(3); PV(VC, 0, 4); LVC(VN, 3);                                                         \
    } while (0)
        u32 cnt_a = lb + L_CNT, cnt_v = lane == 0;
        const int bb = w & 1, dh = w >> 1;
        u32 a_mw = L_MRG + ((w * 4 + (n >> 2)) * 132 + 32 * g + jj) * 4, a_lw = L_MML + (w * 4 + (n >> 2)) * 8;
        u32 a_mr = L_MRG + (w * 132 + 4 * (lane & 31)) * 4, a_lr = L_MML + w * 8;
        u32 o_pa = lane < 32 ? myslot + w * 512 + lane * 16 : OOR, o_pl = lane == 0 ? WS_ML + ((pair * 4 + w) * 16 + s) * 8 : OOR;
        u32 o_ga = WS_SLOT + ((bb * 8 + x8) * 16 + 4 * g) * 4096 + (hi & 3) * 512 + dh * 256 + n * 16;
        u32 o_gl = WS_ML + (((bb * 8 + x8) * 4 + (hi & 3)) * 16 + ((n + 4 * g) & 15)) * 8;
        u32 a_wo = L_WO + 32 * w + (8 * g + ((lane >> 2) & 3)) * 128 + 8 * jj, a_ow = L_O + bb * 256 + dh * 128 + n * 8 + g * 2;
        u32 a_or = L_O + (n & 1) * 256 + 16 * g, o_ps = n < 2 ? myslot + SL_PS + n * 256 + w * 64 + g * 16 : OOR;
        asm volatile("" : "+v"(cnt_a), "+v"(cnt_v), "+v"(a_mw), "+v"(a_lw), "+v"(a_mr), "+v"(a_lr), "+v"(o_pa), "+v"(o_pl));
        asm volatile("" : "+v"(o_ga), "+v"(o_gl), "+v"(a_wo), "+v"(a_ow), "+v"(a_or), "+v"(o_ps));
        u32 ord = 0;
#define NEARLY_DONE() asm volatile("ds_add_rtn_u32 %0, %1, %2" : "+v"(ord) : "v"(cnt_a), "v"(cnt_v) : "memory")
#define HOOK(k)                                                                                                \
    do {                                                                                                       \
        if (K_NW && (k) == 5 && t >= (u32)(POOL - K_NW)) asm volatile("ds_add_u32 %0, %1 offset:4" ::"v"(cnt_a), "v"(cnt_v) : "memory"); \
        if (K_ANN == (k) && !more) NEARLY_DONE();                                                              \
    } while (0)
#pragma unroll 1
        for (;;) {
            CLAIM();
            STAMP(11);
            STEPD(Ka, Va, Kb, Vb);
            if (!more) break;
            CLAIM();
            STAMP(11);
            STEPD(Kb, Vb, Ka, Va);
            if (!more) break;
        }
        if (K_ANN == 1) NEARLY_DONE();
#undef NEARLY_DONE
#undef STEPD
#undef TKT
#undef LVC
#undef LKC
#undef WAITC
#undef HOOK
#undef CLAIM
#undef TICKET
        STAMP(3);
        asm volatile("s_waitcnt lgkmcnt(0)" : "+v"(ord)::"memory");
        const u32 ord_s = __builtin_amdgcn_readfirstlane(ord);
        {
            LAS float* mr = (LAS float*)(L + a_mw);
#pragma unroll
            for (int r = 0; r < 4; ++r) { mr[8 * r] = acc0[r]; mr[8 * r + 4] = acc1[r]; }
            *(LAS f32x2*)(L + a_lw) = f32x2{m * SC, x32_sum(x16_sum(l))};
        }
        if (K_WOW >= 0) {
            if (ord_s == K_WOW) {
                const u16* so = p.wo + (size_t)(hi * 128 + (lane >> 3)) * DM + 64 * cg + 8 * (lane & 7);
#pragma unroll
                for (int u = 0; u < 16; ++u) glds_nt(so + (size_t)u * 8 * DM, lb + L_WO + u * 1024);
            }
        }
        wg_barrier();
        {
            f32x2 ml[NSW]; f32x4 ov[NSW];
#pragma unroll
            for (int u = 0; u < NSW; ++u) ml[u] = *(const LAS f32x2*)(L + a_lr + u * 32);
#pragma unroll
            for (int u = 0; u < NSW; ++u) ov[u] = *(const LAS f32x4*)(L + a_mr + u * (4 * 132 * 4));
            const float M = fmaxf(fmaxf(ml[0].x, ml[1].x), fmaxf(ml[2].x, ml[3].x));
            float f[NSW];
#pragma unroll
            for (int u = 0; u < NSW; ++u) f[u] = ex2(ml[u].x - M);
            const f32x4 o = (f[0] * ov[0] + f[1] * ov[1]) + (f[2] * ov[2] + f[3] * ov[3]);
            __builtin_amdgcn_raw_buffer_store_b128(clean4(o), rws, o_pa, 0, AUX_SC1);
            const float Ls = fmaf(f[0], ml[0].y, f[1] * ml[1].y) + fmaf(f[2], ml[2].y, f[3] * ml[3].y);
            __builtin_amdgcn_raw_buffer_store_b64(v2u{no_sent(M), no_sent(Ls)}, rws, o_pl, 0, AUX_SC1);
        }
        STAMP(4);
        v4u WO[4];
        {
            v4u a[4]; v2u q2;
#define GATHER()                                                                                               \
    do {                                                                                                       \
        q2 = __builtin_amdgcn_raw_buffer_load_b64(rws, o_gl, 0, AUX_SC1);                                      \
        _Pragma("unroll") for (int k = 0; k < 4; ++k) a[k] = __builtin_amdgcn_raw_buffer_load_b128(rws, o_ga, k * 4096, AUX_SC1); \
    } while (0)
#define ARRIVED() __all(max(max(max(umax4(a[0]), umax4(a[1])), max(umax4(a[2]), umax4(a[3]))), max(q2.x, q2.y)) != SENT)
            if (K_WOW >= 0) wait_vm<2>();
            wg_barrier();
            GATHER();
            __builtin_amdgcn_sched_barrier(0);
            STAMP(8);
            COUNT0(7);
#pragma unroll
            for (int u = 0; u < 4; ++u) {
                const v2u lo = tr_read(L + a_wo + 4096 * u), hi2 = tr_read(L + a_wo + 4096 * u + 512);
                WO[u] = v4u{lo.x, lo.y, hi2.x, hi2.y};
            }
            __builtin_amdgcn_sched_barrier(0);
            if (!ARRIVED()) {
#pragma unroll 1
                for (u32 spin = 0; spin < SPIN_CAP; ++spin) {
                    asm volatile("" ::: "memory");
                    GATHER();
                    COUNT(7);
                    if (ARRIVED()) break;
                }
            }
#undef ARRIVED
#undef GATHER
            STAMP(5);
            const float mp = __uint_as_float(q2.x);
            const float M = row_max(mp);
            const float wt = ex2(mp - M);
            const float inv = __builtin_amdgcn_rcpf(row_sum(wt * __uint_as_float(q2.y)));
            const f32x4 o = (dpp<0x150>(wt) * __builtin_bit_cast(f32x4, a[0]) + dpp<0x151>(wt) * __builtin_bit_cast(f32x4, a[1])) +
                            (dpp<0x152>(wt) * __builtin_bit_cast(f32x4, a[2]) + dpp<0x153>(wt) * __builtin_bit_cast(f32x4, a[3]));
            const auto s02 = __builtin_amdgcn_permlane32_swap(__float_as_uint(o[0]), __float_as_uint(o[2]), false, false);
            const auto s13 = __builtin_amdgcn_permlane32_swap(__float_as_uint(o[1]), __float_as_uint(o[3]), false, false);
            const float xs = __uint_as_float(s02[0]) + __uint_as_float(s02[1]), ys = __uint_as_float(s13[0]) + __uint_as_float(s13[1]);
            const auto sz = __builtin_amdgcn_permlane16_swap(__float_as_uint(xs), __float_as_uint(ys), false, false);
            const float z = (__uint_as_float(sz[0]) + __uint_as_float(sz[1])) * inv;
            *(LAS u16*)(L + a_ow) = (u16)pk_bf16(z, z);
        }
        wg_barrier();
        {
            v4u ob[4];
#pragma unroll
            for (int u = 0; u < 4; ++u) ob[u] = *(const LAS v4u*)(L + a_or + 64 * u);
            __builtin_amdgcn_sched_barrier(0);
            const f32x4 z = {0.f, 0.f, 0.f, 0.f};
            const f32x4 sp = mfma32(WO[2], ob[2], mfma32(WO[0], ob[0], z)) + mfma32(WO[3], ob[3], mfma32(WO[1], ob[1], z));
            __builtin_amdgcn_raw_buffer_store_b128(clean4(sp), rws, o_ps, 0, AUX_SC1);
        }
        STAMP(6);
        asm volatile("" ::"v"(Ka[0]), "v"(Ka[1]), "v"(Ka[2]), "v"(Ka[3]), "v"(Va[0]), "v"(Va[1]), "v"(Va[2]), "v"(Va[3]), "v"(tk));
        asm volatile("" ::"v"(Kb[0]), "v"(Kb[1]), "v"(Kb[2]), "v"(Kb[3]), "v"(Vb[0]), "v"(Vb[1]), "v"(Vb[2]), "v"(Vb[3]));
        STAMPS_OUT(K_NSTAMP_S);
    } else {
        __builtin_amdgcn_s_setprio(3);
        v4u xr0, xr1; v2u ep;
        {
            const void* xa = p.x + 8 * lane;
            const void* ea = p.ws + WS_EPOCH;
            asm volatile("global_load_dwordx4 %0, %3, off\n\tglobal_load_dwordx4 %1, %3, off offset:1024\n\tglobal_load_dwordx2 %2, %4, off sc1"
                         : "=&v"(xr0), "=&v"(xr1), "=&v"(ep) : "v"(xa), "v"(ea) : "memory");
        }
        {
            const v4u sv = {SENT, SENT, SENT, SENT};
#pragma unroll
            for (int u = 0; u < 3; ++u) __builtin_amdgcn_raw_buffer_store_b128(sv, rws, myslot + u * 1024 + lane * 16, 0, AUX_SC1);
            const bool zt = s == 0 && (lane >> 2) == 2;
            const u32 o4 = lane < 4 ? WS_ML + ((pair * 4 + lane) * 16 + s) * 8 : lane < 6 ? myslot + SL_FA + (lane - 4) * 128 : zt ? WS_TK + (pair * NSW + (lane & 3)) * 256 : OOR;
            __builtin_amdgcn_raw_buffer_store_b64(zt ? v2u{0u, 0u} : v2u{SENT, SENT}, rws, o4, 0, AUX_SC1);
        }
        const int rot = (K_ROT & 1) ? 4 * (jq & 3) : 0, rot1 = (K_ROT & 2) ? 4 * (jq & 3) : 0;
        const u16* wqs = p.wq + (size_t)(lane >> 1) * 4096 + kvh * 512 + 16 * jq + 8 * (lane & 1);
#pragma unroll
        for (int u = 0; u < K_WQ; ++u) { const int ur = (u + rot) & 15; glds_sh(wqs + (size_t)ur * 32 * 4096, lb + L_WQ + ur * 1024); }
        if (K_B0) wg_barrier();
        asm volatile("s_waitcnt vmcnt(%3)" : "+v"(xr0), "+v"(xr1), "+v"(ep) : "i"(4 + K_WQ) : "memory");
#pragma unroll
        for (int u = K_WQ; u < (K_NOWQ ? 0 : 16); ++u) { const int ur = (u + rot) & 15; wait_vm<K_WQ - 1>(); glds_sh(wqs + (size_t)ur * 32 * 4096, lb + L_WQ + ur * 1024); }
        STAMP(1);
        const u64 tag = ((((u64)ep.y << 32) | ep.x) + 1) & 0xFFFFFFFFFFFFull;
        float xv[2][8];
#pragma unroll
        for (int u = 0; u < 4; ++u) { xv[0][2 * u] = bf_lo(xr0[u]); xv[0][2 * u + 1] = bf_hi(xr0[u]); xv[1][2 * u] = bf_lo(xr1[u]); xv[1][2 * u + 1] = bf_hi(xr1[u]); }
        layer_norm(xv, L + L_XN, lane);
        {
            v4u B[16];
            load_act(B, L + L_XN, lane);
            __builtin_amdgcn_sched_barrier(0);
            wait_vm<0>();
            STAMP(2);
            const f32x4 qv = gemv16(L + L_WQ, B, lane);
            const u32 qb = pk_bf16(sel4(qv, n >> 1), 0.f) & 0xffffu;
            GAS u64* dst = (GAS u64*)(p.ws + WS_QG) + (kvh * 2 + (n & 1)) * 512 + 16 * jq + 4 * g + (n >> 1);
            if (n < 8) __hip_atomic_store(dst, (tag << 16) | qb, RLX_AGENT);
        }
        STAMP(3);
        {
            GAS u64* src = (GAS u64*)(p.ws + WS_QG) + (kvh * 2 + b) * 512 + lane;
            u64 A[8];
            __builtin_amdgcn_s_sleep(K_SQ0);
#pragma unroll 1
            for (u32 spin = 0; spin < SPIN_CAP; ++spin) {
                bool ok = true;
#pragma unroll
                for (int k = 0; k < 8; ++k) A[k] = __hip_atomic_load(src + 64 * k, RLX_AGENT);
#pragma unroll
                for (int k = 0; k < 8; ++k) ok &= (A[k] >> 16) == tag;
                if (__all(ok)) break;
                __builtin_amdgcn_s_sleep(K_SQ1);
            }
#pragma unroll
            for (int k = 0; k < 8; ++k) *(LAS u16*)(L + L_Q + (64 * k + lane) * 2) = (u16)A[k];
        }
        *(LAS v2u*)(L + L_CNT) = v2u{0u, 0u};
        STAMP(4);
        wg_barrier();
        __builtin_amdgcn_s_setprio(1);
        layer_norm(xv, L + L_Y, lane);
        const int fb = bid >> 7, fq = (bid >> 3) & 15, hf = lane >> 5, fc = 64 * x8 + 4 * fq + 2 * hf;
        const u32 xo = *(const GAS u32*)(p.x + fb * DM + fc), b2o = *(const GAS u32*)(p.b2 + fc);
        const int col1 = do_m1 ? 16 * jm + 4 * g + ((n >> 1) & 3) : 0;
        const float bias1 = bf_lo(p.b1[col1]);
        if (do_m1 && !(X_SKIP & 8)) {
            const u16* s1 = p.w1 + (size_t)(lane >> 1) * FF + 16 * jm + 8 * (lane & 1);
#pragma unroll
            for (int u = 0; u < 16; ++u) { const int ur = (u + rot1) & 15; wait_vm<K_WW1 - 1>(); glds_sh(s1 + (size_t)ur * 32 * FF, lb + L_W1 + ur * 1024); }
        }
        const u16* so = p.wo + (size_t)(hi * 128 + (lane >> 3)) * DM + 64 * cg + 8 * (lane & 7);
        if (!K_WOLATE && K_WOW < 0) {
#pragma unroll
            for (int u = 0; u < 16; ++u) { wait_vm<K_WWO - 1>(); glds_nt(so + (size_t)u * 8 * DM, lb + L_WO + u * 1024); }
        }
        if (do_m2 && !(X_SKIP & 16)) {
            const u16* s2 = p.w2 + (size_t)(hi * 64 + (lane >> 3)) * DM + 64 * cg + 8 * (lane & 7);
#pragma unroll
            for (int u = 0; u < 8; ++u) { wait_vm<K_WWO - 1>(); glds_nt(s2 + (size_t)u * 8 * DM, lb + L_W2 + u * 1024); }
        }
        wait_vm<0>();
        STAMP(10);
        if (do_m1) {
            v4u B[16];
            load_act(B, L + L_Y, lane);
            const float u = rbf(rbf(sel4(gemv16(L + L_W1, B, lane), n >> 1)) + bias1);
            const float z = 1.5957691216057308f * fmaf(0.044715f * u * u, u, u);
            const float h = u / (1.f + __expf(-z));
            if (n < 8) __hip_atomic_store((GAS u64*)(p.ws + WS_HG) + (n & 1) * FF + col1, (tag << 16) | (pk_bf16(h, 0.f) & 0xffffu), RLX_AGENT);
        }
        const int pi = lane & 31, pw = ((pi & 3) * 8 + x8) * 8 + (pi >> 2);
        const u32 pslot = WS_SLOT + pw * 4096 + fb * 256 + fq * 16 + hf * 8;
        {
            const int ek = hf ? (pw >> 4) & 7 : x8, ej = hf ? 4 * (pw & 7) + 2 * (pw >> 7) + ((pw >> 3) & 1) : pi;
            GAS u64* ev = (GAS u64*)(p.ws + WS_QG) + ((X_SKIP & 1) ? kvh * 1024 + 16 * jq : ek * 1024 + 16 * ej);
            GAS u64* hs = do_m2 && !(X_SKIP & 4) ? (GAS u64*)(p.ws + WS_HG) + 64 * hi + lane : ev;
            const int hstr = do_m2 && !(X_SKIP & 4) ? FF : 0;
            u64 e, h0, h1;
            if (!do_m1) { __builtin_amdgcn_s_sleep(K_SH0); __builtin_amdgcn_s_sleep(K_SH0); }
            __builtin_amdgcn_s_sleep(K_SH0);
#pragma unroll 1
            for (u32 spin = 0; spin < SPIN_CAP; ++spin) {
                e = __hip_atomic_load(ev, RLX_AGENT); h0 = __hip_atomic_load(hs, RLX_AGENT); h1 = __hip_atomic_load(hs + hstr, RLX_AGENT);
                if (__all(((e >> 16) == tag) & ((h0 >> 16) == tag) & ((h1 >> 16) == tag))) break;
                __builtin_amdgcn_s_sleep(K_SH1);
            }
            *(LAS u16*)(L + L_H + lane * 2) = (u16)h0;
            *(LAS u16*)(L + L_H + 128 + lane * 2) = (u16)h1;
        }
        if (do_m2) {
#pragma unroll
            for (int cb = 0; cb < 4; ++cb) {
                f32x4 mp = {0.f, 0.f, 0.f, 0.f};
#pragma unroll
                for (int u = 0; u < 2; ++u) mp = mfma32(tr_frag(L + L_W2 + 32 * cb, 128, 32 * u, lane), *(const LAS v4u*)(L + L_H + (n & 1) * 128 + 64 * u + 16 * g), mp);
                __builtin_amdgcn_raw_buffer_store_b128(clean4(mp), rws, n < 2 ? myslot + SL_PM + n * 256 + cb * 64 + g * 16 : OOR, 0, AUX_SC1);
            }
        }
        STAMP(5);
        float gate[2];
        {
            v2u a;
            __builtin_amdgcn_s_sleep(K_SH0);
#pragma unroll 1
            for (u32 spin = 0; spin < SPIN_CAP; ++spin) {
                asm volatile("" ::: "memory");
                a = __builtin_amdgcn_raw_buffer_load_b64(rws, pi < 28 && !(X_SKIP & 2) ? pslot + SL_PM : OOR, 0, AUX_SC1);
                if (__all(max(a.x, a.y) != SENT)) break;
                __builtin_amdgcn_s_sleep(K_SH1);
            }
#pragma unroll
            for (int k = 0; k < 2; ++k) {
                const float mm = rbf(rbf(x16_sum(row_sum(__uint_as_float(a[k])))) + (k ? bf_hi(b2o) : bf_lo(b2o)));
                gate[k] = rbf(mm / (1.f + __expf(-mm)));
            }
        }
        STAMP(11);
        __builtin_amdgcn_s_setprio(3);
        const u32 fme = lane == 0 ? myslot + SL_FA : OOR;
        const u32 fao = lane < 32 ? WS_SLOT + (((lane >> 4) * 8 + x8) * 16 + (lane & 15)) * 4096 + SL_FA : OOR;
        const u32 fbo = lane < 32 ? WS_SLOT + pw * 4096 + SL_FB : OOR;
#define WAIT_WORDS(o)                                                                                          \
    _Pragma("unroll 1") for (u32 spin = 0; spin < SPIN_CAP; ++spin) {                                          \
        asm volatile("" ::: "memory");                                                                         \
        if (__all(__builtin_amdgcn_raw_buffer_load_b32(rws, o, 0, AUX_SC1) != SENT)) break;                    \
        __builtin_amdgcn_s_sleep(K_SP);                                                                        \
    }
#define WAIT_CNT(o, c)                                                                                         \
    _Pragma("unroll 1") for (u32 spin = 0; spin < 16 * SPIN_CAP; ++spin) {                                     \
        if (__hip_atomic_load((LAS u32*)(L + L_CNT + (o)), __ATOMIC_RELAXED, __HIP_MEMORY_SCOPE_WORKGROUP) >= (c)) break; \
        __builtin_amdgcn_s_sleep(1);                                                                           \
    }
        if (K_WOLATE && K_WOW < 0) {
            WAIT_CNT(K_NW ? 4 : 0, K_WOLATE);
#pragma unroll
            for (int u = 0; u < 16; ++u) glds_nt(so + (size_t)u * 8 * DM, lb + L_WO + u * 1024);
        }
        WAIT_CNT(0, NSW);
#undef WAIT_CNT
        __builtin_amdgcn_raw_buffer_store_b32(1u, rws, fme, 0, AUX_SC1);
        wg_barrier();
        WAIT_WORDS(fao);
        wait_vm<0>();
        if (K_DA) __builtin_amdgcn_s_sleep(K_DA);
        if (K_ANNB == 1) __builtin_amdgcn_raw_buffer_store_b32(1u, rws, fme + (SL_FB - SL_FA), 0, AUX_SC1);
        wg_barrier();
        if (K_ANNB == 2) { __builtin_amdgcn_s_sleep(K_DC); __builtin_amdgcn_raw_buffer_store_b32(1u, rws, fme + (SL_FB - SL_FA), 0, AUX_SC1); }
        wg_barrier();
        STAMP(6);
        {
            v2u a;
            WAIT_WORDS(fbo);
            if (K_DB) __builtin_amdgcn_s_sleep(K_DB);
#undef WAIT_WORDS
            COUNT0(9);
#pragma unroll 1
            for (u32 spin = 0; spin < SPIN_CAP; ++spin) {
                asm volatile("" ::: "memory");
                a = __builtin_amdgcn_raw_buffer_load_b64(rws, pslot + SL_PS, 0, AUX_SC1);
                if (__all(max(a.x, a.y) != SENT)) break;
                COUNT(9);
                if (K_SC) __builtin_amdgcn_s_sleep(K_SC);
            }
            STAMP(7);
            const float s0 = rbf(x16_sum(row_sum(__uint_as_float(a.x)))), s1 = rbf(x16_sum(row_sum(__uint_as_float(a.y))));
            const u32 r = pk_bf16(bf_lo(xo) + rbf(s0 * gate[0]), bf_hi(xo) + rbf(s1 * gate[1]));
            if (pi == 0) *(GAS u32*)(p.out + fb * DM + fc) = r;
            if (bid == 0 && lane == 0) __hip_atomic_store((GAS u64*)(p.ws + WS_EPOCH), (((u64)ep.y << 32) | ep.x) + 1, RLX_AGENT);
        }
        STAMP(8);
        STAMPS_OUT(K_NSTAMP_H);
    }
}

extern "C" void kernel_launch(void* const* d_in, const int* in_sizes, int n_in, void* d_out, int out_size, void* d_ws, size_t ws_size,
                              hipStream_t stream) {
    Params p;
    p.x = (const u16*)d_in[0]; p.k = (const u16*)d_in[1]; p.v = (const u16*)d_in[2]; p.wq = (const u16*)d_in[3]; p.wo = (const u16*)d_in[4];
    p.w1 = (const u16*)d_in[6]; p.b1 = (const u16*)d_in[7]; p.w2 = (const u16*)d_in[8]; p.b2 = (const u16*)d_in[9];
    p.out = (u16*)d_out; p.ws = (unsigned char*)d_ws;
    constexpr int LDS_BYTES = L_END > 96 * 1024 ? L_END : 96 * 1024;
    static_assert(LDS_BYTES <= 160 * 1024, "");
    (void)hipFuncSetAttribute((const void*)fused_decode, hipFuncAttributeMaxDynamicSharedMemorySize, LDS_BYTES);
    hipLaunchKernelGGL(fused_decode, dim3(NWG), dim3(NTHR), LDS_BYTES, stream, p);
}
```

```cpp
#include <hip/hip_runtime.h>
#include <hip/hip_bf16.h>
#include <stdint.h>

#define GAS __attribute__((address_space(1)))
#define LAS __attribute__((address_space(3)))
typedef unsigned u32;
typedef unsigned long long u64;
typedef unsigned short u16;
typedef unsigned v4u __attribute__((ext_vector_type(4)));
typedef unsigned v2u __attribute__((ext_vector_type(2)));
typedef float f32x4 __attribute__((ext_vector_type(4)));
typedef float f32x2 __attribute__((ext_vector_type(2)));
typedef short s16x4 __attribute__((ext_vector_type(4)));
typedef __bf16 bf16x8 __attribute__((ext_vector_type(8)));
typedef __bf16 bf16x2 __attribute__((ext_vector_type(2)));

#define K_DEPTH 6
#define K_PACE 2
#define K_INTER 0
#define K_AUXK 2
#define K_AUXV 2
#define K_SPINB SPIN_CAP
#define K_TS 0

constexpr int DM = 512, HD = 128, SK = 32768, FF = 1792;
constexpr int NWG = 256, NSW = 4, NTHR = (NSW + 1) * 64;
constexpr int DEPTH = K_DEPTH, PACE = K_PACE;
constexpr int NTILE = SK / 16 / 16 / NSW;
constexpr float SC = 0.08838834764831845f * 1.4426950408889634f;
constexpr u32 SENT = 0xFFFFFFFFu;
constexpr u32 SPIN_CAP = 1u << 18;

constexpr u32 WS_EPOCH = 0;
constexpr u32 WS_QG = 4096;
constexpr u32 WS_HG = WS_QG + 8 * 2 * 512 * 8;
constexpr u32 WS_SLOT = 131072;
constexpr u32 SL_PS = 2048, SL_PM = 2560, SL_ML = 3072;
constexpr u32 WS_END = WS_SLOT + NWG * 4096;
constexpr u32 WS_TS = WS_END;
static_assert(WS_HG + 2 * FF * 8 <= WS_SLOT, "");

constexpr u32 L_WQ = 0, L_W1 = 0, L_WO = 16384, L_W2 = 32768;
constexpr u32 L_XN = 40960, L_Y = L_XN + 2048, L_Q = L_Y + 2048, L_H = L_Q + 1024, L_O = L_H + 256;
constexpr u32 L_MRG = L_O + 512;
constexpr u32 L_MML = L_MRG + NSW * 4 * 132 * 4;
constexpr u32 L_END = L_MML + NSW * 4 * 2 * 4;

struct Params {
    const u16 *x, *k, *v, *wq, *wo, *w1, *b1, *w2, *b2;
    u16* out;
    unsigned char* ws;
};

__device__ __forceinline__ f32x4 mfma32(v4u a, v4u b, f32x4 c) {
    return __builtin_amdgcn_mfma_f32_16x16x32_bf16(__builtin_bit_cast(bf16x8, a), __builtin_bit_cast(bf16x8, b), c, 0, 0, 0);
}
__device__ __forceinline__ f32x4 mfma16(u32 a0, u32 a1, u32 b0, u32 b1, f32x4 c) {
    return __builtin_amdgcn_mfma_f32_16x16x16bf16_1k(__builtin_bit_cast(s16x4, v2u{a0, a1}), __builtin_bit_cast(s16x4, v2u{b0, b1}), c, 0, 0, 0);
}
__device__ __forceinline__ u32 pk_bf16(float lo, float hi) {
    return __builtin_bit_cast(u32, __builtin_convertvector(f32x2{lo, hi}, bf16x2));
}
__device__ __forceinline__ float bf_lo(u32 w) { return __builtin_bit_cast(float, w << 16); }
__device__ __forceinline__ float bf_hi(u32 w) { return __builtin_bit_cast(float, w & 0xffff0000u); }
__device__ __forceinline__ float rbf(float f) { return bf_lo(pk_bf16(f, f)); }
__device__ __forceinline__ float ex2(float x) { return __builtin_amdgcn_exp2f(x); }
template <int CTRL> __device__ __forceinline__ float dpp(float x) {
    return __builtin_bit_cast(float, __builtin_amdgcn_mov_dpp(__builtin_bit_cast(int, x), CTRL, 0xf, 0xf, true));
}
constexpr int XOR1 = 0xB1, XOR2 = 0x4E, XOR7 = 0x141, ROR8 = 0x128;
__device__ __forceinline__ float x16_max(float x) {
    auto s = __builtin_amdgcn_permlane16_swap(__float_as_uint(x), __float_as_uint(x), false, false);
    return fmaxf(__uint_as_float(s[0]), __uint_as_float(s[1]));
}
__device__ __forceinline__ float x32_max(float x) {
    auto s = __builtin_amdgcn_permlane32_swap(__float_as_uint(x), __float_as_uint(x), false, false);
    return fmaxf(__uint_as_float(s[0]), __uint_as_float(s[1]));
}
__device__ __forceinline__ float x16_sum(float x) {
    auto s = __builtin_amdgcn_permlane16_swap(__float_as_uint(x), __float_as_uint(x), false, false);
    return __uint_as_float(s[0]) + __uint_as_float(s[1]);
}
__device__ __forceinline__ float x32_sum(float x) {
    auto s = __builtin_amdgcn_permlane32_swap(__float_as_uint(x), __float_as_uint(x), false, false);
    return __uint_as_float(s[0]) + __uint_as_float(s[1]);
}
__device__ __forceinline__ float row_sum(float x) {
    x += dpp<XOR1>(x); x += dpp<XOR2>(x); x += dpp<XOR7>(x); x += dpp<ROR8>(x); return x;
}
__device__ __forceinline__ float wave_sum(float x) { return x32_sum(x16_sum(row_sum(x))); }
__device__ __forceinline__ u32 umax4(v4u a) { return max(max(a.x, a.y), max(a.z, a.w)); }
__device__ __forceinline__ u32 no_sent(float f) { const u32 u = __float_as_uint(f); return u == SENT ? 0x7FC00000u : u; }

#define RLX_AGENT __ATOMIC_RELAXED, __HIP_MEMORY_SCOPE_AGENT
constexpr int AUX_NT = 2, AUX_SC1 = 16;
__device__ __forceinline__ __amdgpu_buffer_rsrc_t mk_rsrc(const void* p, u32 bytes) {
    return __builtin_amdgcn_make_buffer_rsrc((void*)p, 0, bytes, 0x00020000);
}
__device__ __forceinline__ void glds_nt(const void* src, u32 dst) {
    asm volatile("s_mov_b32 m0, %1\n\ts_nop 0\n\tglobal_load_lds_dwordx4 %0, off nt" ::"v"(src), "s"(dst) : "memory");
}
template <int N> __device__ __forceinline__ void wait_vm() { asm volatile("s_waitcnt vmcnt(%0)" ::"i"(N) : "memory"); }
__device__ __forceinline__ void wg_barrier() { asm volatile("s_waitcnt lgkmcnt(0)\n\ts_barrier" ::: "memory"); }
__device__ __forceinline__ v2u tr_read(const LAS unsigned char* p) {
    return __builtin_bit_cast(v2u, __builtin_amdgcn_ds_read_tr16_b64_v4i16((LAS s16x4*)p));
}
__device__ __forceinline__ v4u tr_frag(const LAS unsigned char* img, int pitch, int row0, int lane) {
    const int g = lane >> 4, q = (lane >> 2) & 3, p = lane & 3;
    const LAS unsigned char* a = img + (row0 + 8 * g + q) * pitch + 8 * p;
    const v2u lo = tr_read(a), hi = tr_read(a + 4 * pitch);
    return v4u{lo.x, lo.y, hi.x, hi.y};
}
#define STAMP(i) do { } while (0)
#define STAMPS_OUT(N) do { } while (0)

__device__ __forceinline__ f32x4 gemv16(const LAS unsigned char* img, const LAS unsigned char* act, int lane) {
    const int g = lane >> 4, n = lane & 15;
    f32x4 a0 = {0.f, 0.f, 0.f, 0.f}, a1 = {0.f, 0.f, 0.f, 0.f};
    const LAS unsigned char* ap = act + (n & 1) * 1024 + g * 16;
#pragma unroll
    for (int s = 0; s < 16; s += 2) {
        a0 = mfma32(tr_frag(img, 32, 32 * s, lane), *(const LAS v4u*)(ap + 64 * s), a0);
        a1 = mfma32(tr_frag(img, 32, 32 * s + 32, lane), *(const LAS v4u*)(ap + 64 * s + 64), a1);
    }
    return a0 + a1;
}
__device__ __forceinline__ float sel4(f32x4 v, int r) { return r == 0 ? v[0] : r == 1 ? v[1] : r == 2 ? v[2] : v[3]; }

__global__ void __launch_bounds__(NTHR, 2) fused_decode(Params p) {
    u64 t_begin; asm volatile("s_memrealtime %0" : "=s"(t_begin));
    __builtin_amdgcn_sched_barrier(0);
    extern __shared__ __attribute__((aligned(16))) unsigned char lds_raw[];
    LAS unsigned char* L = (LAS unsigned char*)lds_raw;
    const int tid = threadIdx.x, lane = tid & 63, w = __builtin_amdgcn_readfirstlane(tid >> 6);
    const int bid = blockIdx.x;
    const int pair = bid >> 4, b = pair >> 3, kvh = pair & 7, s = bid & 15;
    const int hh = 2 * b + (s >> 3), cg = s & 7, hi = 4 * kvh + hh;
    const int g = lane >> 4, n = lane & 15;
    const __amdgpu_buffer_rsrc_t rws = mk_rsrc(p.ws, WS_END);
    const u32 myslot = WS_SLOT + bid * 4096;
    STAMP(0);

    if (w < NSW) {
        const __amdgpu_buffer_rsrc_t rk = K_INTER ? mk_rsrc(p.k + (size_t)pair * SK * HD, SK * HD * 2) : mk_rsrc(p.k + ((size_t)pair * SK + s * 2048) * HD, 2048 * HD * 2);
        const __amdgpu_buffer_rsrc_t rv = K_INTER ? mk_rsrc(p.v + (size_t)pair * SK * HD, SK * HD * 2) : mk_rsrc(p.v + ((size_t)pair * SK + s * 2048) * HD, 2048 * HD * 2);
        const u32 t0 = (K_INTER ? s * NSW + w : w) * 4096;
        constexpr u32 TSTR = (K_INTER ? 16 * NSW : NSW) * 4096;
        u32 ko = t0 + (4 * (n & 3) + (n >> 2)) * 256 + g * 16, vo = t0 + lane * 16;
        v4u Kr[DEPTH][4], Vr[DEPTH][4];
#define ISSUE(d)                                                                                               \
    do {                                                                                                       \
        _Pragma("unroll") for (int j = 0; j < 4; ++j) Kr[d][j] = __builtin_amdgcn_raw_buffer_load_b128(rk, ko + 64 * j, 0, K_AUXK);   \
        _Pragma("unroll") for (int j = 0; j < 4; ++j) Vr[d][j] = __builtin_amdgcn_raw_buffer_load_b128(rv, vo + 1024 * j, 0, K_AUXV); \
        ko += TSTR; vo += TSTR;                                                                                \
        __builtin_amdgcn_sched_barrier(0);                                                                     \
    } while (0)
        wg_barrier();
#pragma unroll
        for (int d = 0; d < DEPTH; ++d) {
            if (d >= PACE) { asm volatile("" ::"v"(Vr[d - PACE][3])); __builtin_amdgcn_sched_barrier(0); }
            ISSUE(d);
        }
        STAMP(1);
        wg_barrier();
        STAMP(2);
        v4u Q[4];
#pragma unroll
        for (int j = 0; j < 4; ++j) Q[j] = *(const LAS v4u*)(L + L_Q + (n >> 2) * 256 + 64 * j + 16 * g);
        const int jj = n & 3;
        const u32 mask0 = jj == 0 ? 0x0000ffffu : jj == 1 ? 0xffff0000u : 0u, mask1 = jj == 2 ? 0x0000ffffu : jj == 3 ? 0xffff0000u : 0u;
        float m = -INFINITY, l = 0.f;
        f32x4 acc0 = {0.f, 0.f, 0.f, 0.f}, acc1 = {0.f, 0.f, 0.f, 0.f};
#define TILE(d)                                                                                                \
    do {                                                                                                       \
        f32x4 sc = {0.f, 0.f, 0.f, 0.f};                                                                       \
        _Pragma("unroll") for (int j = 0; j < 4; ++j) sc = mfma32(Kr[d][j], Q[j], sc);                         \
        const float tm = x32_max(x16_max(fmaxf(fmaxf(sc[0], sc[1]), fmaxf(sc[2], sc[3]))));                    \
        const float mn = fmaxf(m, tm), mc = mn * SC, alpha = ex2((m - mn) * SC);                               \
        float pr[4];                                                                                           \
        _Pragma("unroll") for (int r = 0; r < 4; ++r) pr[r] = ex2(fmaf(sc[r], SC, -mc));                       \
        l = fmaf(l, alpha, (pr[0] + pr[1]) + (pr[2] + pr[3]));                                                 \
        m = mn;                                                                                                \
        acc0 *= alpha; acc1 *= alpha;                                                                          \
        _Pragma("unroll") for (int r = 0; r < 4; ++r) {                                                        \
            const u32 pb = pk_bf16(pr[r], pr[r]), x0 = pb & mask0, x1 = pb & mask1;                            \
            acc0 = mfma16(Vr[d][r].x, Vr[d][r].y, x0, x1, acc0);                                               \
            acc1 = mfma16(Vr[d][r].z, Vr[d][r].w, x0, x1, acc1);                                               \
        }                                                                                                      \
        __builtin_amdgcn_sched_barrier(0);                                                                     \
    } while (0)
#pragma unroll 1
        for (int it = 0; it < NTILE / DEPTH; ++it) {
#pragma unroll
            for (int d = 0; d < DEPTH; ++d) { TILE(d); ISSUE(d); }
        }
#pragma unroll
        for (int d = 0; d < NTILE % DEPTH; ++d) TILE(d);
#undef TILE
#undef ISSUE
        STAMP(3);
        l = x32_sum(x16_sum(l));
        {
            LAS float* mr = (LAS float*)(L + L_MRG) + (w * 4 + (n >> 2)) * 132 + 32 * g + jj;
#pragma unroll
            for (int r = 0; r < 4; ++r) { mr[8 * r] = acc0[r]; mr[8 * r + 4] = acc1[r]; }
            if (g == 0 && jj == 0) *(LAS f32x2*)(L + L_MML + (w * 4 + (n >> 2)) * 8) = f32x2{m * SC, l};
        }
        wg_barrier();
        {
            f32x2 ml[NSW];
#pragma unroll
            for (int u = 0; u < NSW; ++u) ml[u] = *(const LAS f32x2*)(L + L_MML + (u * 4 + w) * 8);
            float M = ml[0].x;
#pragma unroll
            for (int u = 1; u < NSW; ++u) M = fmaxf(M, ml[u].x);
            float Ls = 0.f; f32x4 o = {0.f, 0.f, 0.f, 0.f};
#pragma unroll
            for (int u = 0; u < NSW; ++u) {
                const float f = ex2(ml[u].x - M);
                Ls = fmaf(f, ml[u].y, Ls);
                o += f * *(const LAS f32x4*)(L + L_MRG + ((u * 4 + w) * 132 + 4 * (lane & 31)) * 4);
            }
            if (lane < 32) __builtin_amdgcn_raw_buffer_store_b128(v4u{no_sent(o[0]), no_sent(o[1]), no_sent(o[2]), no_sent(o[3])}, rws, myslot + w * 512 + lane * 16, 0, AUX_SC1);
            else if (lane == 32) __builtin_amdgcn_raw_buffer_store_b64(v2u{no_sent(M), no_sent(Ls)}, rws, myslot + SL_ML + w * 8, 0, AUX_SC1);
        }
        STAMP(4);
        v4u WO[4];
#pragma unroll
        for (int j = 0; j < 4; ++j) WO[j] = tr_frag(L + L_WO + 32 * w, 128, 32 * j, lane);
        {
            const int bb = w & 1, dh = w >> 1;
            const u32 src = WS_SLOT + (((bb * 8 + kvh) * 16) + 4 * g) * 4096;
            const u32 ao = src + hh * 512 + dh * 256 + n * 16, mo = src + SL_ML + hh * 8;
            v4u a[4]; v2u q2[4];
#pragma unroll 1
            for (u32 spin = 0; spin < K_SPINB; ++spin) {
                u32 mx = 0;
#pragma unroll
                for (int k = 0; k < 4; ++k) { a[k] = __builtin_amdgcn_raw_buffer_load_b128(rws, ao + k * 4096, 0, AUX_SC1); q2[k] = __builtin_amdgcn_raw_buffer_load_b64(rws, mo + k * 4096, 0, AUX_SC1); }
#pragma unroll
                for (int k = 0; k < 4; ++k) mx = max(mx, max(umax4(a[k]), max(q2[k].x, q2[k].y)));
                if (__all(mx != SENT)) break;
                __builtin_amdgcn_s_sleep(2);
            }
            STAMP(5);
            float M = fmaxf(fmaxf(__uint_as_float(q2[0].x), __uint_as_float(q2[1].x)), fmaxf(__uint_as_float(q2[2].x), __uint_as_float(q2[3].x)));
            M = x32_max(x16_max(M));
            float Ls = 0.f; f32x4 o = {0.f, 0.f, 0.f, 0.f};
#pragma unroll
            for (int k = 0; k < 4; ++k) {
                const float f = ex2(__uint_as_float(q2[k].x) - M);
                Ls = fmaf(f, __uint_as_float(q2[k].y), Ls);
                o += f * __builtin_bit_cast(f32x4, a[k]);
            }
            Ls = x32_sum(x16_sum(Ls));
            const float inv = 1.f / Ls;
#pragma unroll
            for (int r = 0; r < 4; ++r) o[r] = x32_sum(x16_sum(o[r])) * inv;
            if (g == 0) *(LAS v2u*)(L + L_O + bb * 256 + dh * 128 + n * 8) = v2u{pk_bf16(o[0], o[1]), pk_bf16(o[2], o[3])};
        }
        wg_barrier();
        {
            f32x4 sp = {0.f, 0.f, 0.f, 0.f};
#pragma unroll
            for (int j = 0; j < 4; ++j) sp = mfma32(WO[j], *(const LAS v4u*)(L + L_O + (n & 1) * 256 + 64 * j + 16 * g), sp);
            if (n < 2) __builtin_amdgcn_raw_buffer_store_b128(v4u{no_sent(sp[0]), no_sent(sp[1]), no_sent(sp[2]), no_sent(sp[3])}, rws, myslot + SL_PS + n * 256 + w * 64 + g * 16, 0, AUX_SC1);
        }
        STAMP(6);
        STAMPS_OUT(K_NSTAMP_S);
    } else {
        __builtin_amdgcn_s_setprio(3);
        const u32 lb = (u32)(size_t)L;
        v4u xr0, xr1; v2u ep;
        {
            const void* xa = p.x + 8 * lane;
            const void* ea = p.ws + WS_EPOCH;
            asm volatile("global_load_dwordx4 %0, %3, off\n\tglobal_load_dwordx4 %1, %3, off offset:1024\n\tglobal_load_dwordx2 %2, %4, off sc1"
                         : "=&v"(xr0), "=&v"(xr1), "=&v"(ep) : "v"(xa), "v"(ea) : "memory");
        }
        {
            const int jq = 4 * cg + hh;
            const u16* src = p.wq + (size_t)(lane >> 1) * 4096 + kvh * 512 + 16 * jq + 8 * (lane & 1);
#pragma unroll
            for (int u = 0; u < 16; ++u) glds_nt(src + (size_t)u * 32 * 4096, lb + L_WQ + u * 1024);
        }
        {
            const v4u sv = {SENT, SENT, SENT, SENT};
#pragma unroll
            for (int u = 0; u < 3; ++u) __builtin_amdgcn_raw_buffer_store_b128(sv, rws, myslot + u * 1024 + lane * 16, 0, AUX_SC1);
            __builtin_amdgcn_raw_buffer_store_b128(sv, rws, lane < 2 ? myslot + SL_ML + lane * 16 : 0xFFFFFF00u, 0, AUX_SC1);
        }
        asm volatile("s_barrier" ::: "memory");
        const bool do_m1 = hi >= 16 && cg < 7, do_m2 = hi < 28;
        const int jm = (cg * 4 + ((hi - 16) >> 2)) * 4 + (hi & 3);
        asm volatile("s_waitcnt vmcnt(20)" : "+v"(xr0), "+v"(xr1), "+v"(ep)::"memory");
        STAMP(1);
        const u64 tag = ((((u64)ep.y << 32) | ep.x) + 1) & 0xFFFFFFFFFFFFull;
        {
            float v[2][8];
#pragma unroll
            for (int j = 0; j < 4; ++j) { v[0][2 * j] = bf_lo(xr0[j]); v[0][2 * j + 1] = bf_hi(xr0[j]); v[1][2 * j] = bf_lo(xr1[j]); v[1][2 * j + 1] = bf_hi(xr1[j]); }
#pragma unroll
            for (int pass = 0; pass < 2; ++pass) {
#pragma unroll
                for (int bb = 0; bb < 2; ++bb) {
                    float su = 0.f;
#pragma unroll
                    for (int j = 0; j < 8; ++j) su += v[bb][j];
                    const float mean = wave_sum(su) * (1.f / DM);
                    float sq = 0.f;
#pragma unroll
                    for (int j = 0; j < 8; ++j) { const float d = v[bb][j] - mean; sq = fmaf(d, d, sq); }
                    const float var = wave_sum(sq) * (1.f / DM);
                    const float rs = 1.f / sqrtf(var + 1e-5f);
                    v4u o;
#pragma unroll
                    for (int j = 0; j < 4; ++j) {
                        o[j] = pk_bf16((v[bb][2 * j] - mean) * rs, (v[bb][2 * j + 1] - mean) * rs);
                        v[bb][2 * j] = bf_lo(o[j]); v[bb][2 * j + 1] = bf_hi(o[j]);
                    }
                    *(LAS v4u*)(L + (pass ? L_Y : L_XN) + bb * 1024 + lane * 16) = o;
                }
            }
        }
        wait_vm<4>();
        STAMP(2);
        {
            const f32x4 qv = gemv16(L + L_WQ, L + L_XN, lane);
            const u32 qb = pk_bf16(sel4(qv, n >> 1), 0.f) & 0xffffu;
            GAS u64* dst = (GAS u64*)(p.ws + WS_QG) + (kvh * 2 + (n & 1)) * 512 + 16 * (4 * cg + hh) + 4 * g + (n >> 1);
            STAMP(9);
            wait_vm<0>();
            STAMP(10);
            if (n < 8) __hip_atomic_store(dst, (tag << 16) | qb, RLX_AGENT);
        }
        STAMP(3);
        {
            GAS u64* src = (GAS u64*)(p.ws + WS_QG) + (kvh * 2 + b) * 512 + lane;
            u64 A[8], B[8], C[8];
#define POLL(X) do { _Pragma("unroll") for (int k = 0; k < 8; ++k) X[k] = __hip_atomic_load(src + 64 * k, RLX_AGENT); __builtin_amdgcn_sched_barrier(0); } while (0)
#define GOOD(X) ({ bool ok_ = true; _Pragma("unroll") for (int k = 0; k < 8; ++k) ok_ &= (X[k] >> 16) == tag; __all(ok_); })
#define TAKE(X) do { _Pragma("unroll") for (int k = 0; k < 8; ++k) *(LAS u16*)(L + L_Q + (64 * k + lane) * 2) = (u16)X[k]; } while (0)
            POLL(A); __builtin_amdgcn_s_sleep(12); POLL(B); __builtin_amdgcn_s_sleep(12); POLL(C);
#pragma unroll 1
            for (u32 spin = 0; spin < SPIN_CAP; ++spin) {
                if (GOOD(A)) { TAKE(A); break; }
                POLL(A);
                if (GOOD(B)) { TAKE(B); break; }
                POLL(B);
                if (GOOD(C)) { TAKE(C); break; }
                POLL(C);
            }
#undef POLL
#undef GOOD
#undef TAKE
        }
        STAMP(4);
        wg_barrier();
        if (do_m1) {
            const u16* src = p.w1 + (size_t)(lane >> 1) * FF + 16 * jm + 8 * (lane & 1);
#pragma unroll
            for (int u = 0; u < 16; ++u) glds_nt(src + (size_t)u * 32 * FF, lb + L_W1 + u * 1024);
        }
        {
            const u16* src = p.wo + (size_t)(hi * 128 + (lane >> 3)) * DM + 64 * cg + 8 * (lane & 7);
#pragma unroll
            for (int u = 0; u < 16; ++u) glds_nt(src + (size_t)u * 8 * DM, lb + L_WO + u * 1024);
        }
        if (do_m2) {
            const u16* src = p.w2 + (size_t)(hi * 64 + (lane >> 3)) * DM + 64 * cg + 8 * (lane & 7);
#pragma unroll
            for (int u = 0; u < 8; ++u) glds_nt(src + (size_t)u * 8 * DM, lb + L_W2 + u * 1024);
        }
        if (do_m1) {
            const int col = 16 * jm + 4 * g + ((n >> 1) & 3);
            const float bias = bf_lo(p.b1[col]);
            wait_vm<0>();
            const float u = rbf(rbf(sel4(gemv16(L + L_W1, L + L_Y, lane), n >> 1)) + bias);
            const float z = 1.5957691216057308f * fmaf(0.044715f * u * u, u, u);
            const float h = u / (1.f + __expf(-z));
            if (n < 8) __hip_atomic_store((GAS u64*)(p.ws + WS_HG) + (n & 1) * FF + col, (tag << 16) | (pk_bf16(h, 0.f) & 0xffffu), RLX_AGENT);
        }
        const int fb = hi >> 4, fc = 64 * cg + 4 * (hi & 15);
        const v2u xo = *(const GAS v2u*)(p.x + fb * DM + fc), b2o = *(const GAS v2u*)(p.b2 + fc);
        {
            const int i = lane & 31;
            GAS u64* ev = (GAS u64*)(p.ws + WS_QG) + ((i >> 2) * 2) * 512 + 16 * (4 * cg + (i & 3));
            GAS u64* hs = do_m2 ? (GAS u64*)(p.ws + WS_HG) + 64 * hi + lane : ev;
            const int hstr = do_m2 ? FF : 0;
            u64 e, h0, h1;
#pragma unroll 1
            for (u32 spin = 0; spin < SPIN_CAP; ++spin) {
                e = __hip_atomic_load(ev, RLX_AGENT); h0 = __hip_atomic_load(hs, RLX_AGENT); h1 = __hip_atomic_load(hs + hstr, RLX_AGENT);
                if (__all(((e >> 16) == tag) & ((h0 >> 16) == tag) & ((h1 >> 16) == tag))) break;
                __builtin_amdgcn_s_sleep(8);
            }
            *(LAS u16*)(L + L_H + lane * 2) = (u16)h0;
            *(LAS u16*)(L + L_H + 128 + lane * 2) = (u16)h1;
        }
        wait_vm<0>();
        if (do_m2) {
#pragma unroll
            for (int cb = 0; cb < 4; ++cb) {
                f32x4 mp = {0.f, 0.f, 0.f, 0.f};
#pragma unroll
                for (int j = 0; j < 2; ++j) mp = mfma32(tr_frag(L + L_W2 + 32 * cb, 128, 32 * j, lane), *(const LAS v4u*)(L + L_H + (n & 1) * 128 + 64 * j + 16 * g), mp);
                if (n < 2) __builtin_amdgcn_raw_buffer_store_b128(v4u{no_sent(mp[0]), no_sent(mp[1]), no_sent(mp[2]), no_sent(mp[3])}, rws, myslot + SL_PM + n * 256 + cb * 64 + g * 16, 0, AUX_SC1);
            }
        }
        STAMP(5);
        wg_barrier();
        wg_barrier();
        STAMP(6);
        {
            const int i = lane & 31;
            const bool live = lane < 60;
            const int pb = ((i >> 1) & 1) * 8 + (i >> 2), ps = cg + 8 * (i & 1);
            const u32 off = live ? WS_SLOT + (pb * 16 + ps) * 4096 + (lane < 32 ? SL_PS : SL_PM) + fb * 256 + (hi & 15) * 16 : 0xFFFFFF00u;
            v4u a;
#pragma unroll 1
            for (u32 spin = 0; spin < SPIN_CAP; ++spin) {
                a = __builtin_amdgcn_raw_buffer_load_b128(rws, off, 0, AUX_SC1);
                if (__all(umax4(a) != SENT)) break;
                __builtin_amdgcn_s_sleep(1);
            }
            STAMP(7);
            float r[4];
#pragma unroll
            for (int k = 0; k < 4; ++k) {
                const float t = x16_sum(row_sum(__uint_as_float(a[k])));
                auto sw = __builtin_amdgcn_permlane32_swap(__float_as_uint(t), __float_as_uint(t), false, false);
                const float sv = rbf(__uint_as_float(sw[0])), mv = __uint_as_float(sw[1]);
                const u32 xw = k < 2 ? xo.x : xo.y, bw = k < 2 ? b2o.x : b2o.y;
                const float xv = (k & 1) ? bf_hi(xw) : bf_lo(xw), bv = (k & 1) ? bf_hi(bw) : bf_lo(bw);
                const float mm = rbf(rbf(mv) + bv);
                const float gt = rbf(mm / (1.f + __expf(-mm)));
                r[k] = xv + rbf(sv * gt);
            }
            if (lane == 0) {
                *(GAS v2u*)(p.out + fb * DM + fc) = v2u{pk_bf16(r[0], r[1]), pk_bf16(r[2], r[3])};
                if (bid == 0) __hip_atomic_store((GAS u64*)(p.ws + WS_EPOCH), (((u64)ep.y << 32) | ep.x) + 1, RLX_AGENT);
            }
        }
        while (__builtin_amdgcn_s_memrealtime() - t_begin < 7000) __builtin_amdgcn_s_sleep(1);
        STAMPS_OUT(K_NSTAMP_H);
    }
}

extern "C" void kernel_launch(void* const* d_in, const int* in_sizes, int n_in, void* d_out, int out_size, void* d_ws, size_t ws_size,
                              hipStream_t stream) {
    Params p;
    p.x = (const u16*)d_in[0]; p.k = (const u16*)d_in[1]; p.v = (const u16*)d_in[2]; p.wq = (const u16*)d_in[3]; p.wo = (const u16*)d_in[4];
    p.w1 = (const u16*)d_in[6]; p.b1 = (const u16*)d_in[7]; p.w2 = (const u16*)d_in[8]; p.b2 = (const u16*)d_in[9];
    p.out = (u16*)d_out; p.ws = (unsigned char*)d_ws;
    constexpr int LDS_BYTES = 163840;
    static_assert(L_END <= LDS_BYTES, "");
    (void)hipFuncSetAttribute((const void*)fused_decode, hipFuncAttributeMaxDynamicSharedMemorySize, LDS_BYTES);
    hipLaunchKernelGGL(fused_decode, dim3(NWG), dim3(NTHR), LDS_BYTES, stream, p);
}
```

```cpp
#include <hip/hip_runtime.h>
#include <hip/hip_bf16.h>
#include <stdint.h>

#define GAS __attribute__((address_space(1)))
#define LAS __attribute__((address_space(3)))
typedef unsigned u32;
typedef unsigned long long u64;
typedef unsigned short u16;
typedef unsigned v4u __attribute__((ext_vector_type(4)));
typedef unsigned v2u __attribute__((ext_vector_type(2)));
typedef float f32x4 __attribute__((ext_vector_type(4)));
typedef float f32x2 __attribute__((ext_vector_type(2)));
typedef short s16x4 __attribute__((ext_vector_type(4)));
typedef __bf16 bf16x8 __attribute__((ext_vector_type(8)));
typedef __bf16 bf16x2 __attribute__((ext_vector_type(2)));

constexpr int DM = 512, HD = 128, SK = 32768, FF = 1792;
constexpr int NWG = 256, NSW = 4, NTHR = (NSW + 1) * 64;
constexpr int NS = 29, NST = NS + 1;
constexpr int LP = 4, RP = 8, NPRO = LP + RP + 2;
constexpr int NKU = NS - NPRO;
constexpr int POOL = SK / 16 / NSW - 16 * NST;
constexpr int NBURST = 16;
static_assert(NKU >= 3 && NS <= 32 && POOL >= NBURST, "");
constexpr int W0 = 6, WK = 5, WV = 6, WKD = 6, WVD = 7;
constexpr int WQ = 8, WW1 = 3, WW2 = 4;
constexpr float SC = 0.08838834764831845f * 1.4426950408889634f;
constexpr u32 SENT = 0xFFFFFFFFu;
constexpr u32 SPIN_CAP = 1u << 21;
constexpr u32 OOR = 0xFFFFFF00u;

constexpr u32 WS_EPOCH = 0;
constexpr u32 WS_QG = 4096;
constexpr u32 WS_HG = WS_QG + 8 * 2 * 512 * 8;
constexpr u32 WS_TK = 102400;
constexpr u32 WS_ML = 118784;
constexpr u32 WS_SLOT = 131072, SLOT = 4352;
constexpr u32 SL_PS = 2048, SL_PM = 2560, SL_FA = 3072, SL_FB = 3200;
constexpr u32 WS_END = WS_SLOT + NWG * SLOT;
static_assert(WS_HG + 2 * FF * 8 <= WS_TK && WS_TK + 64 * 256 <= WS_ML && WS_ML + 8192 <= WS_SLOT, "");

constexpr u32 L_WQ = 0, L_W1 = 0, L_WO = 16384, L_W2 = 32768;
constexpr u32 L_XN = 40960, L_Y = L_XN + 2048, L_Q = L_Y + 2048, L_H = L_Q + 1024, L_O = L_H + 256;
constexpr u32 L_MRG = L_O + 512;
constexpr u32 L_MML = L_MRG + NSW * 4 * 132 * 4;
constexpr u32 L_CNT = L_MML + NSW * 4 * 2 * 4;
constexpr u32 L_SC = 57344;
constexpr u32 L_PARK = L_SC + NSW * 8192;
constexpr u32 L_END = L_PARK + NSW * LP * 4096;
static_assert(L_CNT + 16 <= L_SC && L_END > 80 * 1024 && L_END <= 160 * 1024, "more than half of a CU's LDS: one workgroup per CU");

struct Params {
    const u16 *x, *k, *v, *wq, *wo, *w1, *b1, *w2, *b2;
    u16* out;
    unsigned char* ws;
};

__device__ __forceinline__ f32x4 mfma32(v4u a, v4u b, f32x4 c) {
    return __builtin_amdgcn_mfma_f32_16x16x32_bf16(__builtin_bit_cast(bf16x8, a), __builtin_bit_cast(bf16x8, b), c, 0, 0, 0);
}
__device__ __forceinline__ f32x4 mfma16(u32 a0, u32 a1, u32 b0, u32 b1, f32x4 c) {
    return __builtin_amdgcn_mfma_f32_16x16x16bf16_1k(__builtin_bit_cast(s16x4, v2u{a0, a1}), __builtin_bit_cast(s16x4, v2u{b0, b1}), c, 0, 0, 0);
}
__device__ __forceinline__ u32 pk_bf16(float lo, float hi) {
    return __builtin_bit_cast(u32, __builtin_convertvector(f32x2{lo, hi}, bf16x2));
}
__device__ __forceinline__ float bf_lo(u32 w) { return __builtin_bit_cast(float, w << 16); }
__device__ __forceinline__ float bf_hi(u32 w) { return __builtin_bit_cast(float, w & 0xffff0000u); }
__device__ __forceinline__ float rbf(float f) { return bf_lo(pk_bf16(f, f)); }
__device__ __forceinline__ float ex2(float x) { return __builtin_amdgcn_exp2f(x); }
template <int CTRL> __device__ __forceinline__ float dpp(float x) {
    return __builtin_bit_cast(float, __builtin_amdgcn_mov_dpp(__builtin_bit_cast(int, x), CTRL, 0xf, 0xf, true));
}
constexpr int XOR1 = 0xB1, XOR2 = 0x4E, XOR7 = 0x141, ROR8 = 0x128;
__device__ __forceinline__ float x16_max(float x) {
    auto s = __builtin_amdgcn_permlane16_swap(__float_as_uint(x), __float_as_uint(x), false, false);
    return fmaxf(__uint_as_float(s[0]), __uint_as_float(s[1]));
}
__device__ __forceinline__ float x32_max(float x) {
    auto s = __builtin_amdgcn_permlane32_swap(__float_as_uint(x), __float_as_uint(x), false, false);
    return fmaxf(__uint_as_float(s[0]), __uint_as_float(s[1]));
}
__device__ __forceinline__ float x16_sum(float x) {
    auto s = __builtin_amdgcn_permlane16_swap(__float_as_uint(x), __float_as_uint(x), false, false);
    return __uint_as_float(s[0]) + __uint_as_float(s[1]);
}
__device__ __forceinline__ float x32_sum(float x) {
    auto s = __builtin_amdgcn_permlane32_swap(__float_as_uint(x), __float_as_uint(x), false, false);
    return __uint_as_float(s[0]) + __uint_as_float(s[1]);
}
__device__ __forceinline__ float row_sum(float x) {
    x += dpp<XOR1>(x); x += dpp<XOR2>(x); x += dpp<XOR7>(x); x += dpp<ROR8>(x); return x;
}
__device__ __forceinline__ float row_max(float x) {
    float r;
    asm("s_nop 1\n\tv_max_f32_dpp %0, %1, %1 quad_perm:[1,0,3,2] row_mask:0xf bank_mask:0xf\n\ts_nop 1\n\t"
        "v_max_f32_dpp %0, %0, %0 quad_perm:[2,3,0,1] row_mask:0xf bank_mask:0xf\n\ts_nop 1\n\t"
        "v_max_f32_dpp %0, %0, %0 row_half_mirror row_mask:0xf bank_mask:0xf\n\ts_nop 1\n\t"
        "v_max_f32_dpp %0, %0, %0 row_ror:8 row_mask:0xf bank_mask:0xf" : "=&v"(r) : "v"(x));
    return r;
}
__device__ __forceinline__ float wave_sum(float x) { return x32_sum(x16_sum(row_sum(x))); }
__device__ __forceinline__ u32 umax4(v4u a) { return max(max(a.x, a.y), max(a.z, a.w)); }
__device__ __forceinline__ u32 no_sent(float f) { return min(__float_as_uint(f), 0xFFFFFFFEu); }

#define RLX_AGENT __ATOMIC_RELAXED, __HIP_MEMORY_SCOPE_AGENT
constexpr int AUX_NT = 2, AUX_SC1 = 16;
__device__ __forceinline__ __amdgpu_buffer_rsrc_t mk_rsrc(const void* p, u32 bytes) {
    return __builtin_amdgcn_make_buffer_rsrc((void*)p, 0, bytes, 0x00020000);
}
__device__ __forceinline__ void glds_nt(const void* src, u32 dst) {
    asm volatile("s_mov_b32 m0, %1\n\ts_nop 0\n\tglobal_load_lds_dwordx4 %0, off nt" ::"v"(src), "s"(dst) : "memory");
}
__device__ __forceinline__ void glds_sh(const void* src, u32 dst) {
    asm volatile("s_mov_b32 m0, %1\n\ts_nop 0\n\tglobal_load_lds_dwordx4 %0, off" ::"v"(src), "s"(dst) : "memory");
}
template <int OFF> __device__ __forceinline__ void glds_nt_off(const void* src, u32 dst) {
    asm volatile("s_mov_b32 m0, %1\n\ts_nop 0\n\tglobal_load_lds_dwordx4 %0, off offset:%2 nt" ::"v"(src), "s"(dst), "i"(OFF) : "memory");
}
template <int N> __device__ __forceinline__ void wait_vm() { asm volatile("s_waitcnt vmcnt(%0)" ::"i"(N) : "memory"); }
__device__ __forceinline__ void wg_barrier() { asm volatile("s_waitcnt lgkmcnt(0)\n\ts_barrier" ::: "memory"); }
__device__ __forceinline__ v2u tr_read(const LAS unsigned char* p) {
    return __builtin_bit_cast(v2u, __builtin_amdgcn_ds_read_tr16_b64_v4i16((LAS s16x4*)p));
}
__device__ __forceinline__ v4u tr_frag(const LAS unsigned char* img, int pitch, int row0, int lane) {
    const int g = lane >> 4, q = (lane >> 2) & 3, p = lane & 3;
    const LAS unsigned char* a = img + (row0 + 8 * g + q) * pitch + 8 * p;
    const v2u lo = tr_read(a), hi = tr_read(a + 4 * pitch);
    return v4u{lo.x, lo.y, hi.x, hi.y};
}

__device__ __forceinline__ void load_act(v4u (&B)[16], const LAS unsigned char* act, int lane) {
    const LAS unsigned char* ap = act + (lane & 1) * 1024 + (lane >> 4) * 16;
#pragma unroll
    for (int s = 0; s < 16; ++s) B[s] = *(const LAS v4u*)(ap + 64 * s);
}
__device__ __forceinline__ f32x4 gemv16(const LAS unsigned char* img, const v4u (&B)[16], int lane) {
    v4u A[16];
#pragma unroll
    for (int s = 0; s < 16; ++s) A[s] = tr_frag(img, 32, 32 * s, lane);
    __builtin_amdgcn_sched_barrier(0);
    f32x4 a[4];
#pragma unroll
    for (int s = 0; s < 16; ++s) a[s & 3] = mfma32(A[s], B[s], s < 4 ? f32x4{0.f, 0.f, 0.f, 0.f} : a[s & 3]);
    return (a[0] + a[1]) + (a[2] + a[3]);
}
__device__ __forceinline__ void layer_norm(float (&v)[2][8], LAS unsigned char* dst, int lane) {
    float mean[2], rs[2];
#pragma unroll
    for (int bb = 0; bb < 2; ++bb) mean[bb] = wave_sum(((v[bb][0] + v[bb][1]) + (v[bb][2] + v[bb][3])) + ((v[bb][4] + v[bb][5]) + (v[bb][6] + v[bb][7]))) * (1.f / DM);
#pragma unroll
    for (int bb = 0; bb < 2; ++bb) {
        float sq = 0.f;
#pragma unroll
        for (int u = 0; u < 8; ++u) { v[bb][u] -= mean[bb]; sq = fmaf(v[bb][u], v[bb][u], sq); }
        rs[bb] = __builtin_amdgcn_rsqf(wave_sum(sq) * (1.f / DM) + 1e-5f);
    }
#pragma unroll
    for (int bb = 0; bb < 2; ++bb) {
        v4u o;
#pragma unroll
        for (int u = 0; u < 4; ++u) {
            o[u] = pk_bf16(v[bb][2 * u] * rs[bb], v[bb][2 * u + 1] * rs[bb]);
            v[bb][2 * u] = bf_lo(o[u]); v[bb][2 * u + 1] = bf_hi(o[u]);
        }
        *(LAS v4u*)(dst + bb * 1024 + lane * 16) = o;
    }
}
__device__ __forceinline__ float sel4(f32x4 v, int r) {
    const bool b0 = r & 1, b1 = r & 2;
    const float lo = b0 ? v[1] : v[0], hi = b0 ? v[3] : v[2];
    return b1 ? hi : lo;
}
__device__ __forceinline__ v4u clean4(f32x4 v) { return v4u{no_sent(v[0]), no_sent(v[1]), no_sent(v[2]), no_sent(v[3])}; }

__global__ void __launch_bounds__(NTHR, 2) fused_decode(Params p) {
    extern __shared__ __attribute__((aligned(16))) unsigned char lds_raw[];
    LAS unsigned char* L = (LAS unsigned char*)lds_raw;
    const u32 lb = (u32)(size_t)L;
    const int tid = threadIdx.x, lane = tid & 63, w = __builtin_amdgcn_readfirstlane(tid >> 6);
    const int bid = blockIdx.x;
    const int pair = bid >> 4, b = pair >> 3, kvh = pair & 7, s = bid & 15;
    const int jq = 4 * (s & 7) + 2 * b + (s >> 3);
    const int m1i = (s - kvh) & 7, jm = 4 * (7 * (kvh >> 1) + 4 * (kvh & 1) + m1i) + (jq & 3);
    const int x8 = bid & 7, hi = 4 * x8 + (bid >> 6), cg = (bid >> 3) & 7;
    const bool do_m1 = m1i < 4 - (kvh & 1), do_m2 = hi < 28;
    const int g = lane >> 4, n = lane & 15;
    const __amdgpu_buffer_rsrc_t rws = mk_rsrc(p.ws, WS_END);
    const u32 myslot = WS_SLOT + bid * SLOT;

    asm volatile("" ::"s"(p.k), "s"(p.v), "s"(p.x), "s"(p.wq), "s"(p.ws));
    if (w >= NSW) {
        __builtin_amdgcn_s_setprio(3);
        v4u xr0, xr1; v2u ep;
        {
            const void* xa = p.x + 8 * lane;
            const void* ea = p.ws + WS_EPOCH;
            asm volatile("global_load_dwordx4 %0, %3, off\n\tglobal_load_dwordx4 %1, %3, off offset:1024\n\tglobal_load_dwordx2 %2, %4, off sc1"
                         : "=&v"(xr0), "=&v"(xr1), "=&v"(ep) : "v"(xa), "v"(ea) : "memory");
        }
        {
            const v4u sv = {SENT, SENT, SENT, SENT};
#pragma unroll
            for (int u = 0; u < 3; ++u) __builtin_amdgcn_raw_buffer_store_b128(sv, rws, myslot + u * 1024 + lane * 16, 0, AUX_SC1);
            const bool zt = s == 0 && (lane >> 2) == 2;
            const u32 o4 = lane < 4 ? WS_ML + ((pair * 4 + lane) * 16 + s) * 8 : lane < 6 ? myslot + SL_FA + (lane - 4) * 128 : zt ? WS_TK + (pair * NSW + (lane & 3)) * 256 : OOR;
            __builtin_amdgcn_raw_buffer_store_b64(zt ? v2u{0u, 0u} : v2u{SENT, SENT}, rws, o4, 0, AUX_SC1);
        }
        const int rot = 4 * (jq & 3);
        const u16* wqs = p.wq + (size_t)(lane >> 1) * 4096 + kvh * 512 + 16 * jq + 8 * (lane & 1);
#pragma unroll
        for (int u = 0; u < WQ; ++u) { const int ur = (u + rot) & 15; glds_sh(wqs + (size_t)ur * 32 * 4096, lb + L_WQ + ur * 1024); }
        asm volatile("s_waitcnt vmcnt(%3)" : "+v"(xr0), "+v"(xr1), "+v"(ep) : "i"(4 + WQ) : "memory");
#pragma unroll
        for (int u = WQ; u < 16; ++u) { const int ur = (u + rot) & 15; wait_vm<WQ - 1>(); glds_sh(wqs + (size_t)ur * 32 * 4096, lb + L_WQ + ur * 1024); }
        const u64 tag = ((((u64)ep.y << 32) | ep.x) + 1) & 0xFFFFFFFFFFFFull;
        float xv[2][8];
#pragma unroll
        for (int u = 0; u < 4; ++u) { xv[0][2 * u] = bf_lo(xr0[u]); xv[0][2 * u + 1] = bf_hi(xr0[u]); xv[1][2 * u] = bf_lo(xr1[u]); xv[1][2 * u + 1] = bf_hi(xr1[u]); }
        layer_norm(xv, L + L_XN, lane);
        {
            v4u B[16];
            load_act(B, L + L_XN, lane);
            __builtin_amdgcn_sched_barrier(0);
            wait_vm<0>();
            const f32x4 qv = gemv16(L + L_WQ, B, lane);
            const u32 qb = pk_bf16(sel4(qv, n >> 1), 0.f) & 0xffffu;
            GAS u64* dst = (GAS u64*)(p.ws + WS_QG) + (kvh * 2 + (n & 1)) * 512 + 16 * jq + 4 * g + (n >> 1);
            if (n < 8) __hip_atomic_store(dst, (tag << 16) | qb, RLX_AGENT);
        }
        {
            GAS u64* src = (GAS u64*)(p.ws + WS_QG) + (kvh * 2 + b) * 512 + lane;
            u64 A[8];
            __builtin_amdgcn_s_sleep(80);
#pragma unroll 1
            for (u32 spin = 0; spin < SPIN_CAP; ++spin) {
                bool ok = true;
#pragma unroll
                for (int k = 0; k < 8; ++k) A[k] = __hip_atomic_load(src + 64 * k, RLX_AGENT);
#pragma unroll
                for (int k = 0; k < 8; ++k) ok &= (A[k] >> 16) == tag;
                if (__all(ok)) break;
                __builtin_amdgcn_s_sleep(24);
            }
#pragma unroll
            for (int k = 0; k < 8; ++k) *(LAS u16*)(L + L_Q + (64 * k + lane) * 2) = (u16)A[k];
        }
        *(LAS u32*)(L + L_CNT) = 0u;
        wg_barrier();
        __builtin_amdgcn_s_setprio(1);
        layer_norm(xv, L + L_Y, lane);
        const int fb = bid >> 7, fq = (bid >> 3) & 15, hf = lane >> 5, fc = 64 * x8 + 4 * fq + 2 * hf;
        const u32 xo = *(const GAS u32*)(p.x + fb * DM + fc), b2o = *(const GAS u32*)(p.b2 + fc);
        const int col1 = do_m1 ? 16 * jm + 4 * g + ((n >> 1) & 3) : 0;
        const float bias1 = bf_lo(p.b1[col1]);
        if (do_m1) {
            const u16* s1 = p.w1 + (size_t)(lane >> 1) * FF + 16 * jm + 8 * (lane & 1);
#pragma unroll
            for (int u = 0; u < 16; ++u) { const int ur = (u + rot) & 15; wait_vm<WW1 - 1>(); glds_sh(s1 + (size_t)ur * 32 * FF, lb + L_W1 + ur * 1024); }
        }
        if (do_m2) {
            const u16* s2 = p.w2 + (size_t)(hi * 64 + (lane >> 3)) * DM + 64 * cg + 8 * (lane & 7);
#pragma unroll
            for (int u = 0; u < 8; ++u) { wait_vm<WW2 - 1>(); glds_nt(s2 + (size_t)u * 8 * DM, lb + L_W2 + u * 1024); }
        }
        wait_vm<0>();
        if (do_m1) {
            v4u B[16];
            load_act(B, L + L_Y, lane);
            const float u = rbf(rbf(sel4(gemv16(L + L_W1, B, lane), n >> 1)) + bias1);
            const float z = 1.5957691216057308f * fmaf(0.044715f * u * u, u, u);
            const float h = u / (1.f + __expf(-z));
            if (n < 8) __hip_atomic_store((GAS u64*)(p.ws + WS_HG) + (n & 1) * FF + col1, (tag << 16) | (pk_bf16(h, 0.f) & 0xffffu), RLX_AGENT);
        }
        const int pi = lane & 31, pw = ((pi & 3) * 8 + x8) * 8 + (pi >> 2);
        const u32 pslot = WS_SLOT + pw * SLOT + fb * 256 + fq * 16 + hf * 8;
        {
            const int ek = hf ? (pw >> 4) & 7 : x8, ej = hf ? 4 * (pw & 7) + 2 * (pw >> 7) + ((pw >> 3) & 1) : pi;
            GAS u64* ev = (GAS u64*)(p.ws + WS_QG) + ek * 1024 + 16 * ej;
            GAS u64* hs = do_m2 ? (GAS u64*)(p.ws + WS_HG) + 64 * hi + lane : ev;
            const int hstr = do_m2 ? FF : 0;
            u64 e, h0, h1;
            if (!do_m1) { __builtin_amdgcn_s_sleep(64); __builtin_amdgcn_s_sleep(64); }
            __builtin_amdgcn_s_sleep(64);
#pragma unroll 1
            for (u32 spin = 0; spin < SPIN_CAP; ++spin) {
                e = __hip_atomic_load(ev, RLX_AGENT); h0 = __hip_atomic_load(hs, RLX_AGENT); h1 = __hip_atomic_load(hs + hstr, RLX_AGENT);
                if (__all(((e >> 16) == tag) & ((h0 >> 16) == tag) & ((h1 >> 16) == tag))) break;
                __builtin_amdgcn_s_sleep(64);
            }
            *(LAS u16*)(L + L_H + lane * 2) = (u16)h0;
            *(LAS u16*)(L + L_H + 128 + lane * 2) = (u16)h1;
        }
        if (do_m2) {
#pragma unroll
            for (int cb = 0; cb < 4; ++cb) {
                f32x4 mp = {0.f, 0.f, 0.f, 0.f};
#pragma unroll
                for (int u = 0; u < 2; ++u) mp = mfma32(tr_frag(L + L_W2 + 32 * cb, 128, 32 * u, lane), *(const LAS v4u*)(L + L_H + (n & 1) * 128 + 64 * u + 16 * g), mp);
                __builtin_amdgcn_raw_buffer_store_b128(clean4(mp), rws, n < 2 ? myslot + SL_PM + n * 256 + cb * 64 + g * 16 : OOR, 0, AUX_SC1);
            }
        }
        float gate[2];
        {
            v2u a;
            __builtin_amdgcn_s_sleep(64);
#pragma unroll 1
            for (u32 spin = 0; spin < SPIN_CAP; ++spin) {
                asm volatile("" ::: "memory");
                a = __builtin_amdgcn_raw_buffer_load_b64(rws, pi < 28 ? pslot + SL_PM : OOR, 0, AUX_SC1);
                if (__all(max(a.x, a.y) != SENT)) break;
                __builtin_amdgcn_s_sleep(64);
            }
#pragma unroll
            for (int k = 0; k < 2; ++k) {
                const float mm = rbf(rbf(x16_sum(row_sum(__uint_as_float(a[k])))) + (k ? bf_hi(b2o) : bf_lo(b2o)));
                gate[k] = rbf(mm / (1.f + __expf(-mm)));
            }
        }
        __builtin_amdgcn_s_setprio(3);
        const u32 fme = lane == 0 ? myslot + SL_FA : OOR;
        const u32 fao = lane < 32 ? WS_SLOT + (((lane >> 4) * 8 + x8) * 16 + (lane & 15)) * SLOT + SL_FA : OOR;
        const u32 fbo = lane < 32 ? WS_SLOT + pw * SLOT + SL_FB : OOR;
#define WAIT_WORDS(o)                                                                                          \
    _Pragma("unroll 1") for (u32 spin = 0; spin < SPIN_CAP; ++spin) {                                          \
        asm volatile("" ::: "memory");                                                                         \
        if (__all(__builtin_amdgcn_raw_buffer_load_b32(rws, o, 0, AUX_SC1) != SENT)) break;                    \
        __builtin_amdgcn_s_sleep(1);                                                                           \
    }
#pragma unroll 1
        for (u32 spin = 0; spin < 16 * SPIN_CAP; ++spin) {
            if (__hip_atomic_load((LAS u32*)(L + L_CNT), __ATOMIC_RELAXED, __HIP_MEMORY_SCOPE_WORKGROUP) >= NSW) break;
            __builtin_amdgcn_s_sleep(1);
        }
        __builtin_amdgcn_raw_buffer_store_b32(1u, rws, fme, 0, AUX_SC1);
        wg_barrier();
        WAIT_WORDS(fao);
        wg_barrier();
        __builtin_amdgcn_s_sleep(8);
        __builtin_amdgcn_raw_buffer_store_b32(1u, rws, fme + (SL_FB - SL_FA), 0, AUX_SC1);
        wg_barrier();
        WAIT_WORDS(fbo);
#undef WAIT_WORDS
        {
            v2u a;
#pragma unroll 1
            for (u32 spin = 0; spin < SPIN_CAP; ++spin) {
                asm volatile("" ::: "memory");
                a = __builtin_amdgcn_raw_buffer_load_b64(rws, pslot + SL_PS, 0, AUX_SC1);
                if (__all(max(a.x, a.y) != SENT)) break;
            }
            const float s0 = rbf(x16_sum(row_sum(__uint_as_float(a.x)))), s1 = rbf(x16_sum(row_sum(__uint_as_float(a.y))));
            const u32 r = pk_bf16(bf_lo(xo) + rbf(s0 * gate[0]), bf_hi(xo) + rbf(s1 * gate[1]));
            if (pi == 0) *(GAS u32*)(p.out + fb * DM + fc) = r;
            if (bid == 0 && lane == 0) __hip_atomic_store((GAS u64*)(p.ws + WS_EPOCH), (((u64)ep.y << 32) | ep.x) + 1, RLX_AGENT);
        }
    } else {
        const u16* kb = p.k + (size_t)pair * SK * HD;
        const u16* vb = p.v + (size_t)pair * SK * HD;
        const __amdgpu_buffer_rsrc_t rk = mk_rsrc(kb, SK * HD * 2), rv = mk_rsrc(vb, SK * HD * 2);
        constexpr u32 TSTR = NSW * 4096;
        const u32 kl = (n >> 2) * 256 + g * 64 + (n & 3) * 16, vl = lane * 16;
        u32 ko = (s * NST * NSW + w) * 4096 + kl, vo = (s * NST * NSW + w) * 4096 + vl;
        v4u R[3][4], KX[RP][4], Ka[4], Va[4], Kb[4], Vb[4];
#define LK(X, u) do { wait_vm<WK - 1>(); X[u] = __builtin_amdgcn_raw_buffer_load_b128(rk, ko + 1024 * (u), 0, AUX_NT); __builtin_amdgcn_sched_barrier(0); } while (0)
#define LV(X, u) do { wait_vm<WV - 1>(); X[u] = __builtin_amdgcn_raw_buffer_load_b128(rv, vo + 1024 * (u), 0, AUX_NT); __builtin_amdgcn_sched_barrier(0); } while (0)
        const u32 park = lb + L_PARK + w * LP * 4096;
#pragma unroll
        for (int t = 0; t < LP; ++t) {
            const unsigned char* ks = (const unsigned char*)kb + ko;
            wait_vm<W0 - 1>(); glds_nt_off<0>(ks, park + t * 4096);
            wait_vm<W0 - 1>(); glds_nt_off<1024>(ks, park + t * 4096);
            wait_vm<W0 - 1>(); glds_nt_off<2048>(ks, park + t * 4096);
            wait_vm<W0 - 1>(); glds_nt_off<3072>(ks, park + t * 4096);
            ko += TSTR;
        }
#pragma unroll
        for (int t = 0; t < RP; ++t) { LK(KX[t], 0); LK(KX[t], 1); LK(KX[t], 2); LK(KX[t], 3); ko += TSTR; }
#pragma unroll
        for (int t = 0; t < 2; ++t) { LK(R[t], 0); LK(R[t], 1); LK(R[t], 2); LK(R[t], 3); ko += TSTR; }
        wg_barrier();
        const int jj = n & 3;
        const bool j0 = jj & 1, j1 = jj & 2;
        const v4u Q = *(const LAS v4u*)(L + L_Q + (n >> 2) * 256 + 64 * g + 16 * jj);
        const u32 mask0 = jj == 0 ? 0x0000ffffu : jj == 1 ? 0xffff0000u : 0u, mask1 = jj == 2 ? 0x0000ffffu : jj == 3 ? 0xffff0000u : 0u;
        float m = -INFINITY, l = 0.f, sc[4], pr[4];
        f32x4 acc0 = {0.f, 0.f, 0.f, 0.f}, acc1 = {0.f, 0.f, 0.f, 0.f};
#define QKD(KR)                                                                                                \
        f32x4 dd[4];                                                                                           \
        _Pragma("unroll") for (int u = 0; u < 4; ++u) dd[u] = mfma32(KR[u], Q, f32x4{0.f, 0.f, 0.f, 0.f});     \
        asm volatile("" : "+v"(dd[0]), "+v"(dd[1]), "+v"(dd[2]), "+v"(dd[3]));     \
        _Pragma("unroll") for (int u = 0; u < 4; ++u) sc[u] = sel4(dd[u], jj);
        LAS float* scp = (LAS float*)(L + L_SC + w * 8192) + lane;
#define KP(KR, idx)                                                                                            \
    do {                                                                                                       \
        QKD(KR)                                                                                                \
        const float y01 = (j0 ? sc[1] : sc[0]) + dpp<XOR1>(j0 ? sc[0] : sc[1]), y23 = (j0 ? sc[3] : sc[2]) + dpp<XOR1>(j0 ? sc[2] : sc[3]); \
        const float y = (j1 ? y23 : y01) + dpp<XOR2>(j1 ? y01 : y23);                                          \
        m = fmaxf(m, y);                                                                                       \
        scp[(idx) * 64] = y;                                                                                   \
        __builtin_amdgcn_sched_barrier(0);                                                                     \
    } while (0)
#define UNPARK(t, KR)                                                                                          \
    do {                                                                                                       \
        const LAS unsigned char* pk = L + L_PARK + (w * LP + (t)) * 4096 + lane * 16;                          \
        _Pragma("unroll") for (int u = 0; u < 4; ++u) KR[u] = *(const LAS v4u*)(pk + 1024 * u);                \
    } while (0)
#define VP(VR, sv)                                                                                             \
    do {                                                                                                       \
        const float p1 = ex2(fmaf(sv, SC, -mc));                                                               \
        l += p1;                                                                                               \
        const int pb = pk_bf16(p1, p1);                                                                        \
        _Pragma("unroll") for (int r = 0; r < 4; ++r) {                                                        \
            const u32 t_ = r == 0 ? __builtin_amdgcn_mov_dpp(pb, 0x00, 0xf, 0xf, true) : r == 1 ? __builtin_amdgcn_mov_dpp(pb, 0x55, 0xf, 0xf, true) \
                         : r == 2 ? __builtin_amdgcn_mov_dpp(pb, 0xAA, 0xf, 0xf, true) : __builtin_amdgcn_mov_dpp(pb, 0xFF, 0xf, 0xf, true); \
            acc0 = mfma16(VR[r].x, VR[r].y, t_ & mask0, t_ & mask1, acc0);                                     \
            acc1 = mfma16(VR[r].z, VR[r].w, t_ & mask0, t_ & mask1, acc1);                                     \
        }                                                                                                      \
        __builtin_amdgcn_sched_barrier(0);                                                                     \
    } while (0)
#define UKK(u, idx) do { LK(R[((u) + 2) % 3], 0); LK(R[((u) + 2) % 3], 1); KP(R[(u) % 3], idx); LK(R[((u) + 2) % 3], 2); LK(R[((u) + 2) % 3], 3); ko += TSTR; } while (0)
#define UVK(u, idx) do { LV(R[((u) + 2) % 3], 0); LV(R[((u) + 2) % 3], 1); KP(R[(u) % 3], idx); LV(R[((u) + 2) % 3], 2); LV(R[((u) + 2) % 3], 3); vo += TSTR; } while (0)
#define UVV(u, idx) do { const float sv_ = scp[(idx) * 64]; LV(R[((u) + 2) % 3], 0); LV(R[((u) + 2) % 3], 1); VP(R[(u) % 3], sv_); LV(R[((u) + 2) % 3], 2); LV(R[((u) + 2) % 3], 3); vo += TSTR; } while (0)
        static_assert(LP == 4 && RP == 8, "");
        LK(R[2], 0); KP(KX[0], 4); UNPARK(0, KX[0]); LK(R[2], 1); KP(R[0], 12); KP(KX[1], 5); UNPARK(1, KX[1]); LK(R[2], 2); KP(KX[2], 6); LK(R[2], 3); KP(KX[3], 7); ko += TSTR;
        LK(R[0], 0); KP(KX[0], 0); UNPARK(2, KX[2]); LK(R[0], 1); KP(R[1], 13); KP(KX[1], 1); UNPARK(3, KX[3]); LK(R[0], 2); KP(KX[4], 8); LK(R[0], 3); KP(KX[5], 9); ko += TSTR;
        LK(R[1], 0); KP(KX[2], 2); LK(R[1], 1); KP(R[2], 14); KP(KX[3], 3); LK(R[1], 2); KP(KX[6], 10); LK(R[1], 3); KP(KX[7], 11); ko += TSTR;
        constexpr int KA = (NKU - 3) / 3, KR_ = (NKU - 3) % 3;
        scp += 15 * 64;
#pragma unroll 1
        for (int it = 0; it < KA; ++it) { UKK(0, 0); UKK(1, 1); UKK(2, 2); scp += 3 * 64; }
        if (KR_ > 0) UKK(0, 0);
        if (KR_ > 1) UKK(1, 1);
        UVK(KR_, KR_); UVK(KR_ + 1, KR_ + 1);
        m = fmaxf(m, dpp<XOR1>(m)); m = fmaxf(m, dpp<XOR2>(m)); m = x32_max(x16_max(m));
        const float mc = m * SC;
        constexpr int U0 = KR_ + 2, VA = (NS - 2) / 3, VR_ = (NS - 2) % 3;
        scp -= (15 + 3 * KA) * 64;
#pragma unroll 1
        for (int it = 0; it < VA; ++it) { UVV(U0, 0); UVV(U0 + 1, 1); UVV(U0 + 2, 2); scp += 3 * 64; }
        if (VR_ > 0) UVV(U0, 0);
        if (VR_ > 1) UVV(U0 + 1, 1);
        u32 tko = lane == 0 ? WS_TK + (pair * NSW + w) * 256 : OOR;
#define TICKET() __builtin_amdgcn_raw_ptr_buffer_atomic_add_i32(1, rws, tko, 0, AUX_SC1)
        int tk;
        {
            constexpr int U1 = U0 + VR_;
            const float s0 = scp[VR_ * 64], s1 = scp[(VR_ + 1) * 64];
            LK(Ka, 0); LK(Ka, 1); VP(R[U1 % 3], s0); LK(Ka, 2); LK(Ka, 3);
            tk = TICKET();
            LV(Va, 0); LV(Va, 1); VP(R[(U1 + 1) % 3], s1); LV(Va, 2); LV(Va, 3);
            l += dpp<XOR1>(l); l += dpp<XOR2>(l);
        }
        const int bb = w & 1, dh = w >> 1;
        u32 cnt_a = lb + L_CNT, cnt_v = lane == 0;
        u32 a_mw = L_MRG + ((w * 4 + (n >> 2)) * 132 + 32 * g + jj) * 4, a_lw = L_MML + (w * 4 + (n >> 2)) * 8;
        u32 a_mr = L_MRG + (w * 132 + 4 * (lane & 31)) * 4, a_lr = L_MML + w * 8;
        u32 o_pa = lane < 32 ? myslot + w * 512 + lane * 16 : OOR, o_pl = lane == 0 ? WS_ML + ((pair * 4 + w) * 16 + s) * 8 : OOR;
        u32 o_ga = WS_SLOT + ((bb * 8 + x8) * 16 + 4 * g) * SLOT + (hi & 3) * 512 + dh * 256 + n * 16;
        u32 o_gl = WS_ML + (((bb * 8 + x8) * 4 + (hi & 3)) * 16 + ((n + 4 * g) & 15)) * 8;
        u32 a_wo = L_WO + 32 * w + (8 * g + ((lane >> 2) & 3)) * 128 + 8 * jj, a_ow = L_O + bb * 256 + dh * 128 + n * 8 + g * 2;
        u32 a_or = L_O + (n & 1) * 256 + 16 * g, o_ps = n < 2 ? myslot + SL_PS + n * 256 + w * 64 + g * 16 : OOR;
        asm volatile("" : "+v"(cnt_a), "+v"(cnt_v), "+v"(a_mw), "+v"(a_lw), "+v"(a_mr), "+v"(a_lr), "+v"(o_pa), "+v"(o_pl));
        asm volatile("" : "+v"(o_ga), "+v"(o_gl), "+v"(a_wo), "+v"(a_ow), "+v"(a_or), "+v"(o_ps));
        bool more;
        u32 t, ord = 0;
#define WAITC(N) asm volatile("s_cmpk_ge_u32 %0, %2\n\ts_cbranch_scc1 1f\n\ts_waitcnt vmcnt(%1)\n1:" ::"s"(t), "i"(N), "i"(POOL - NBURST) : "memory", "scc")
#define LKC(X, u) do { WAITC(WKD - 1); X[u] = __builtin_amdgcn_raw_buffer_load_b128(rk, ko + 1024 * (u), 0, AUX_NT); __builtin_amdgcn_sched_barrier(0); } while (0)
#define LVC(X, u) do { WAITC(WVD - 1); X[u] = __builtin_amdgcn_raw_buffer_load_b128(rv, vo + 1024 * (u), 0, AUX_NT); __builtin_amdgcn_sched_barrier(0); } while (0)
#define STEP(KC, VC, KN, VN)                                                                                   \
    do {                                                                                                       \
        t = __builtin_amdgcn_readfirstlane(tk);                                                                \
        more = t < (u32)POOL;                                                                                  \
        const u32 to = more ? (16 * NST * NSW + NSW * t + w) * 4096 : 0x40000000u, tm_ = more ? 0u : OOR;      \
        asm volatile("v_add_u32 %0, %3, %4\n\tv_add_u32 %1, %3, %5\n\tv_or_b32 %2, %6, %2" : "+v"(ko), "+v"(vo), "+v"(tko) : "s"(to), "v"(kl), "v"(vl), "s"(tm_)); \
        if (!more) asm volatile("ds_add_rtn_u32 %0, %1, %2" : "+v"(ord) : "v"(cnt_a), "v"(cnt_v) : "memory");  \
        LKC(KN, 0); LKC(KN, 1); LKC(KN, 2); LKC(KN, 3); LVC(VN, 0);                                            \
        tk = TICKET(); __builtin_amdgcn_sched_barrier(0);                                                      \
        LVC(VN, 1); LVC(VN, 2); LVC(VN, 3);                                                                    \
        {                                                                                                      \
            QKD(KC)                                                                                            \
            _Pragma("unroll") for (int u = 0; u < 4; ++u) sc[u] += dpp<XOR1>(sc[u]);                           \
            _Pragma("unroll") for (int u = 0; u < 4; ++u) sc[u] += dpp<XOR2>(sc[u]);                           \
            __builtin_amdgcn_sched_barrier(0);                                                                 \
        }                                                                                                      \
        {                                                                                                      \
            const float tm = x32_max(x16_max(fmaxf(fmaxf(sc[0], sc[1]), fmaxf(sc[2], sc[3]))));                \
            const float mn = fmaxf(m, tm), mc2 = mn * SC, alpha = ex2((m - mn) * SC);                          \
            _Pragma("unroll") for (int r = 0; r < 4; ++r) pr[r] = ex2(fmaf(sc[r], SC, -mc2));                  \
            l = fmaf(l, alpha, (pr[0] + pr[1]) + (pr[2] + pr[3]));                                             \
            m = mn;                                                                                            \
            acc0 *= alpha; acc1 *= alpha;                                                                      \
            __builtin_amdgcn_sched_barrier(0);                                                                 \
        }                                                                                                      \
        _Pragma("unroll") for (int r = 0; r < 4; ++r) {                                                        \
            const u32 pb = pk_bf16(pr[r], pr[r]), x0 = pb & mask0, x1 = pb & mask1;                            \
            acc0 = mfma16(VC[r].x, VC[r].y, x0, x1, acc0);                                                     \
            acc1 = mfma16(VC[r].z, VC[r].w, x0, x1, acc1);                                                     \
        }                                                                                                      \
        __builtin_amdgcn_sched_barrier(0);                                                                     \
    } while (0)
#pragma unroll 1
        for (;;) {
            STEP(Ka, Va, Kb, Vb);
            if (!more) break;
            STEP(Kb, Vb, Ka, Va);
            if (!more) break;
        }
        asm volatile("s_waitcnt lgkmcnt(0)" : "+v"(ord)::"memory");
        const u32 ord_s = __builtin_amdgcn_readfirstlane(ord);
        {
            LAS float* mr = (LAS float*)(L + a_mw);
#pragma unroll
            for (int r = 0; r < 4; ++r) { mr[8 * r] = acc0[r]; mr[8 * r + 4] = acc1[r]; }
            *(LAS f32x2*)(L + a_lw) = f32x2{m * SC, x32_sum(x16_sum(l))};
        }
        if (ord_s == 0) {
            const u16* so = p.wo + (size_t)(hi * 128 + (lane >> 3)) * DM + 64 * cg + 8 * (lane & 7);
#pragma unroll
            for (int u = 0; u < 16; ++u) glds_nt(so + (size_t)u * 8 * DM, lb + L_WO + u * 1024);
        }
        wg_barrier();
        {
            f32x2 ml[NSW]; f32x4 ov[NSW];
#pragma unroll
            for (int u = 0; u < NSW; ++u) ml[u] = *(const LAS f32x2*)(L + a_lr + u * 32);
#pragma unroll
            for (int u = 0; u < NSW; ++u) ov[u] = *(const LAS f32x4*)(L + a_mr + u * (4 * 132 * 4));
            const float M = fmaxf(fmaxf(ml[0].x, ml[1].x), fmaxf(ml[2].x, ml[3].x));
            float f[NSW];
#pragma unroll
            for (int u = 0; u < NSW; ++u) f[u] = ex2(ml[u].x - M);
            const f32x4 o = (f[0] * ov[0] + f[1] * ov[1]) + (f[2] * ov[2] + f[3] * ov[3]);
            __builtin_amdgcn_raw_buffer_store_b128(clean4(o), rws, o_pa, 0, AUX_SC1);
            const float Ls = fmaf(f[0], ml[0].y, f[1] * ml[1].y) + fmaf(f[2], ml[2].y, f[3] * ml[3].y);
            __builtin_amdgcn_raw_buffer_store_b64(v2u{no_sent(M), no_sent(Ls)}, rws, o_pl, 0, AUX_SC1);
        }
        v4u WO[4];
        {
            v4u a[4]; v2u q2;
#define GATHER()                                                                                               \
    do {                                                                                                       \
        q2 = __builtin_amdgcn_raw_buffer_load_b64(rws, o_gl, 0, AUX_SC1);                                      \
        _Pragma("unroll") for (int k = 0; k < 4; ++k) a[k] = __builtin_amdgcn_raw_buffer_load_b128(rws, o_ga, k * SLOT, AUX_SC1); \
    } while (0)
#define ARRIVED() __all(max(max(max(umax4(a[0]), umax4(a[1])), max(umax4(a[2]), umax4(a[3]))), max(q2.x, q2.y)) != SENT)
            wait_vm<2>();
            wg_barrier();
            GATHER();
            __builtin_amdgcn_sched_barrier(0);
#pragma unroll
            for (int u = 0; u < 4; ++u) {
                const v2u lo = tr_read(L + a_wo + 4096 * u), hi2 = tr_read(L + a_wo + 4096 * u + 512);
                WO[u] = v4u{lo.x, lo.y, hi2.x, hi2.y};
            }
            __builtin_amdgcn_sched_barrier(0);
            if (!ARRIVED()) {
#pragma unroll 1
                for (u32 spin = 0; spin < SPIN_CAP; ++spin) {
                    asm volatile("" ::: "memory");
                    GATHER();
                    if (ARRIVED()) break;
                }
            }
            const float mp = __uint_as_float(q2.x);
            const float M = row_max(mp);
            const float wt = ex2(mp - M);
            const float inv = __builtin_amdgcn_rcpf(row_sum(wt * __uint_as_float(q2.y)));
            const f32x4 o = (dpp<0x150>(wt) * __builtin_bit_cast(f32x4, a[0]) + dpp<0x151>(wt) * __builtin_bit_cast(f32x4, a[1])) +
                            (dpp<0x152>(wt) * __builtin_bit_cast(f32x4, a[2]) + dpp<0x153>(wt) * __builtin_bit_cast(f32x4, a[3]));
            const auto s02 = __builtin_amdgcn_permlane32_swap(__float_as_uint(o[0]), __float_as_uint(o[2]), false, false);
            const auto s13 = __builtin_amdgcn_permlane32_swap(__float_as_uint(o[1]), __float_as_uint(o[3]), false, false);
            const float xs = __uint_as_float(s02[0]) + __uint_as_float(s02[1]), ys = __uint_as_float(s13[0]) + __uint_as_float(s13[1]);
            const auto sz = __builtin_amdgcn_permlane16_swap(__float_as_uint(xs), __float_as_uint(ys), false, false);
            const float z = (__uint_as_float(sz[0]) + __uint_as_float(sz[1])) * inv;
            *(LAS u16*)(L + a_ow) = (u16)pk_bf16(z, z);
        }
        wg_barrier();
        {
            v4u ob[4];
#pragma unroll
            for (int u = 0; u < 4; ++u) ob[u] = *(const LAS v4u*)(L + a_or + 64 * u);
            __builtin_amdgcn_sched_barrier(0);
            const f32x4 z = {0.f, 0.f, 0.f, 0.f};
            const f32x4 sp = mfma32(WO[2], ob[2], mfma32(WO[0], ob[0], z)) + mfma32(WO[3], ob[3], mfma32(WO[1], ob[1], z));
            __builtin_amdgcn_raw_buffer_store_b128(clean4(sp), rws, o_ps, 0, AUX_SC1);
        }
        asm volatile("" ::"v"(Ka[0]), "v"(Ka[1]), "v"(Ka[2]), "v"(Ka[3]), "v"(Va[0]), "v"(Va[1]), "v"(Va[2]), "v"(Va[3]), "v"(tk));
        asm volatile("" ::"v"(Kb[0]), "v"(Kb[1]), "v"(Kb[2]), "v"(Kb[3]), "v"(Vb[0]), "v"(Vb[1]), "v"(Vb[2]), "v"(Vb[3]));
    }
}

extern "C" void kernel_launch(void* const* d_in, const int* in_sizes, int n_in, void* d_out, int out_size, void* d_ws, size_t ws_size,
                              hipStream_t stream) {
    Params p;
    p.x = (const u16*)d_in[0]; p.k = (const u16*)d_in[1]; p.v = (const u16*)d_in[2]; p.wq = (const u16*)d_in[3]; p.wo = (const u16*)d_in[4];
    p.w1 = (const u16*)d_in[6]; p.b1 = (const u16*)d_in[7]; p.w2 = (const u16*)d_in[8]; p.b2 = (const u16*)d_in[9];
    p.out = (u16*)d_out; p.ws = (unsigned char*)d_ws;
    (void)hipFuncSetAttribute((const void*)fused_decode, hipFuncAttributeMaxDynamicSharedMemorySize, L_END);
    hipLaunchKernelGGL(fused_decode, dim3(NWG), dim3(NTHR), L_END, stream, p);
}
```

```cpp
#include <hip/hip_runtime.h>
#include <hip/hip_bf16.h>
#include <stdint.h>

#define GAS __attribute__((address_space(1)))
#define LAS __attribute__((address_space(3)))
typedef unsigned u32;
typedef unsigned long long u64;
typedef unsigned short u16;
typedef unsigned v4u __attribute__((ext_vector_type(4)));
typedef unsigned v2u __attribute__((ext_vector_type(2)));
typedef float f32x4 __attribute__((ext_vector_type(4)));
typedef float f32x2 __attribute__((ext_vector_type(2)));
typedef short s16x4 __attribute__((ext_vector_type(4)));
typedef __bf16 bf16x8 __attribute__((ext_vector_type(8)));
typedef __bf16 bf16x2 __attribute__((ext_vector_type(2)));

#define K_PF 1
constexpr int DM = 512, HD = 128, SK = 32768, FF = 1792;
constexpr int NWG = 256, NSW = 4, NTHR = (NSW + 1) * 64;
constexpr int NS = 29, NST = NS + 1;
constexpr int LP = 4, RP = 8, NPRO = LP + RP + 2;
constexpr int NKU = NS - NPRO;
constexpr int POOL = SK / 16 / NSW - 16 * NST;
constexpr int NBURST = 16;
static_assert(NKU >= 3 && NS <= 32 && POOL >= NBURST, "");
constexpr int W0 = 6, WK = 5, WV = 6, WKD = 6, WVD = 7;
constexpr int WQ = 8, WW1 = 3, WW2 = 4;
constexpr float SC = 0.08838834764831845f * 1.4426950408889634f;
constexpr u32 SENT = 0xFFFFFFFFu;
constexpr u32 SPIN_CAP = 1u << 21;
constexpr u32 OOR = 0xFFFFFF00u;

constexpr u32 WS_EPOCH = 0;
constexpr u32 WS_QG = 4096;
constexpr u32 WS_HG = WS_QG + 8 * 2 * 512 * 8;
constexpr u32 WS_TK = 102400;
constexpr u32 WS_ML = 118784;
constexpr u32 WS_SLOT = 131072, SLOT = 4352;
constexpr u32 SL_PS = 2048, SL_PM = 2560, SL_FA = 3072, SL_FB = 3200;
constexpr u32 WS_END = WS_SLOT + NWG * SLOT;
static_assert(WS_HG + 2 * FF * 8 <= WS_TK && WS_TK + 64 * 256 <= WS_ML && WS_ML + 8192 <= WS_SLOT, "");

constexpr u32 L_WQ = 0, L_W1 = 0, L_WO = 16384, L_W2 = 32768;
constexpr u32 L_XN = 40960, L_Y = L_XN + 2048, L_Q = L_Y + 2048, L_H = L_Q + 1024, L_O = L_H + 256;
constexpr u32 L_MRG = L_O + 512;
constexpr u32 L_MML = L_MRG + NSW * 4 * 132 * 4;
constexpr u32 L_CNT = L_MML + NSW * 4 * 2 * 4;
constexpr u32 L_SC = 57344;
constexpr u32 L_PARK = L_SC + NSW * 8192;
constexpr u32 L_END = L_PARK + NSW * LP * 4096;
static_assert(L_CNT + 16 <= L_SC && L_END > 80 * 1024 && L_END <= 160 * 1024, "more than half of a CU's LDS: one workgroup per CU");

struct Params {
    const u16 *x, *k, *v, *wq, *wo, *w1, *b1, *w2, *b2;
    u16* out;
    unsigned char* ws;
};

__device__ __forceinline__ f32x4 mfma32(v4u a, v4u b, f32x4 c) {
    return __builtin_amdgcn_mfma_f32_16x16x32_bf16(__builtin_bit_cast(bf16x8, a), __builtin_bit_cast(bf16x8, b), c, 0, 0, 0);
}
__device__ __forceinline__ f32x4 mfma16(u32 a0, u32 a1, u32 b0, u32 b1, f32x4 c) {
    return __builtin_amdgcn_mfma_f32_16x16x16bf16_1k(__builtin_bit_cast(s16x4, v2u{a0, a1}), __builtin_bit_cast(s16x4, v2u{b0, b1}), c, 0, 0, 0);
}
__device__ __forceinline__ u32 pk_bf16(float lo, float hi) {
    return __builtin_bit_cast(u32, __builtin_convertvector(f32x2{lo, hi}, bf16x2));
}
__device__ __forceinline__ float bf_lo(u32 w) { return __builtin_bit_cast(float, w << 16); }
__device__ __forceinline__ float bf_hi(u32 w) { return __builtin_bit_cast(float, w & 0xffff0000u); }
__device__ __forceinline__ float rbf(float f) { return bf_lo(pk_bf16(f, f)); }
__device__ __forceinline__ float ex2(float x) { return __builtin_amdgcn_exp2f(x); }
template <int CTRL> __device__ __forceinline__ float dpp(float x) {
    return __builtin_bit_cast(float, __builtin_amdgcn_mov_dpp(__builtin_bit_cast(int, x), CTRL, 0xf, 0xf, true));
}
constexpr int XOR1 = 0xB1, XOR2 = 0x4E, XOR7 = 0x141, ROR8 = 0x128;
__device__ __forceinline__ float x16_max(float x) {
    auto s = __builtin_amdgcn_permlane16_swap(__float_as_uint(x), __float_as_uint(x), false, false);
    return fmaxf(__uint_as_float(s[0]), __uint_as_float(s[1]));
}
__device__ __forceinline__ float x32_max(float x) {
    auto s = __builtin_amdgcn_permlane32_swap(__float_as_uint(x), __float_as_uint(x), false, false);
    return fmaxf(__uint_as_float(s[0]), __uint_as_float(s[1]));
}
__device__ __forceinline__ float x16_sum(float x) {
    auto s = __builtin_amdgcn_permlane16_swap(__float_as_uint(x), __float_as_uint(x), false, false);
    return __uint_as_float(s[0]) + __uint_as_float(s[1]);
}
__device__ __forceinline__ float x32_sum(float x) {
    auto s = __builtin_amdgcn_permlane32_swap(__float_as_uint(x), __float_as_uint(x), false, false);
    return __uint_as_float(s[0]) + __uint_as_float(s[1]);
}
__device__ __forceinline__ float row_sum(float x) {
    x += dpp<XOR1>(x); x += dpp<XOR2>(x); x += dpp<XOR7>(x); x += dpp<ROR8>(x); return x;
}
__device__ __forceinline__ float row_max(float x) {
    float r;
    asm("s_nop 1\n\tv_max_f32_dpp %0, %1, %1 quad_perm:[1,0,3,2] row_mask:0xf bank_mask:0xf\n\ts_nop 1\n\t"
        "v_max_f32_dpp %0, %0, %0 quad_perm:[2,3,0,1] row_mask:0xf bank_mask:0xf\n\ts_nop 1\n\t"
        "v_max_f32_dpp %0, %0, %0 row_half_mirror row_mask:0xf bank_mask:0xf\n\ts_nop 1\n\t"
        "v_max_f32_dpp %0, %0, %0 row_ror:8 row_mask:0xf bank_mask:0xf" : "=&v"(r) : "v"(x));
    return r;
}
__device__ __forceinline__ float wave_sum(float x) { return x32_sum(x16_sum(row_sum(x))); }
__device__ __forceinline__ u32 umax4(v4u a) { return max(max(a.x, a.y), max(a.z, a.w)); }
__device__ __forceinline__ u32 no_sent(float f) { return min(__float_as_uint(f), 0xFFFFFFFEu); }

#define RLX_AGENT __ATOMIC_RELAXED, __HIP_MEMORY_SCOPE_AGENT
constexpr int AUX_NT = 2, AUX_SC1 = 16;
__device__ __forceinline__ __amdgpu_buffer_rsrc_t mk_rsrc(const void* p, u32 bytes) {
    return __builtin_amdgcn_make_buffer_rsrc((void*)p, 0, bytes, 0x00020000);
}
__device__ __forceinline__ void glds_nt(const void* src, u32 dst) {
    asm volatile("s_mov_b32 m0, %1\n\ts_nop 0\n\tglobal_load_lds_dwordx4 %0, off nt" ::"v"(src), "s"(dst) : "memory");
}
__device__ __forceinline__ void glds_sh(const void* src, u32 dst) {
    asm volatile("s_mov_b32 m0, %1\n\ts_nop 0\n\tglobal_load_lds_dwordx4 %0, off" ::"v"(src), "s"(dst) : "memory");
}
template <int OFF> __device__ __forceinline__ void glds_nt_off(const void* src, u32 dst) {
    asm volatile("s_mov_b32 m0, %1\n\ts_nop 0\n\tglobal_load_lds_dwordx4 %0, off offset:%2 nt" ::"v"(src), "s"(dst), "i"(OFF) : "memory");
}
template <int N> __device__ __forceinline__ void wait_vm() { asm volatile("s_waitcnt vmcnt(%0)" ::"i"(N) : "memory"); }
__device__ __forceinline__ void wg_barrier() { asm volatile("s_waitcnt lgkmcnt(0)\n\ts_barrier" ::: "memory"); }
__device__ __forceinline__ v2u tr_read(const LAS unsigned char* p) {
    return __builtin_bit_cast(v2u, __builtin_amdgcn_ds_read_tr16_b64_v4i16((LAS s16x4*)p));
}
__device__ __forceinline__ v4u tr_frag(const LAS unsigned char* img, int pitch, int row0, int lane) {
    const int g = lane >> 4, q = (lane >> 2) & 3, p = lane & 3;
    const LAS unsigned char* a = img + (row0 + 8 * g + q) * pitch + 8 * p;
    const v2u lo = tr_read(a), hi = tr_read(a + 4 * pitch);
    return v4u{lo.x, lo.y, hi.x, hi.y};
}

__device__ __forceinline__ void load_act(v4u (&B)[16], const LAS unsigned char* act, int lane) {
    const LAS unsigned char* ap = act + (lane & 1) * 1024 + (lane >> 4) * 16;
#pragma unroll
    for (int s = 0; s < 16; ++s) B[s] = *(const LAS v4u*)(ap + 64 * s);
}
__device__ __forceinline__ f32x4 gemv16(const LAS unsigned char* img, const v4u (&B)[16], int lane) {
    v4u A[16];
#pragma unroll
    for (int s = 0; s < 16; ++s) A[s] = tr_frag(img, 32, 32 * s, lane);
    __builtin_amdgcn_sched_barrier(0);
    f32x4 a[4];
#pragma unroll
    for (int s = 0; s < 16; ++s) a[s & 3] = mfma32(A[s], B[s], s < 4 ? f32x4{0.f, 0.f, 0.f, 0.f} : a[s & 3]);
    return (a[0] + a[1]) + (a[2] + a[3]);
}
__device__ __forceinline__ void layer_norm(float (&v)[2][8], LAS unsigned char* dst, int lane) {
    float mean[2], rs[2];
#pragma unroll
    for (int bb = 0; bb < 2; ++bb) mean[bb] = wave_sum(((v[bb][0] + v[bb][1]) + (v[bb][2] + v[bb][3])) + ((v[bb][4] + v[bb][5]) + (v[bb][6] + v[bb][7]))) * (1.f / DM);
#pragma unroll
    for (int bb = 0; bb < 2; ++bb) {
        float sq = 0.f;
#pragma unroll
        for (int u = 0; u < 8; ++u) { v[bb][u] -= mean[bb]; sq = fmaf(v[bb][u], v[bb][u], sq); }
        rs[bb] = __builtin_amdgcn_rsqf(wave_sum(sq) * (1.f / DM) + 1e-5f);
    }
#pragma unroll
    for (int bb = 0; bb < 2; ++bb) {
        v4u o;
#pragma unroll
        for (int u = 0; u < 4; ++u) {
            o[u] = pk_bf16(v[bb][2 * u] * rs[bb], v[bb][2 * u + 1] * rs[bb]);
            v[bb][2 * u] = bf_lo(o[u]); v[bb][2 * u + 1] = bf_hi(o[u]);
        }
        *(LAS v4u*)(dst + bb * 1024 + lane * 16) = o;
    }
}
__device__ __forceinline__ float sel4(f32x4 v, int r) {
    const bool b0 = r & 1, b1 = r & 2;
    const float lo = b0 ? v[1] : v[0], hi = b0 ? v[3] : v[2];
    return b1 ? hi : lo;
}
__device__ __forceinline__ v4u clean4(f32x4 v) { return v4u{no_sent(v[0]), no_sent(v[1]), no_sent(v[2]), no_sent(v[3])}; }

__global__ void __launch_bounds__(NTHR, 2) fused_decode(Params p) {
    u64 pc0;
    asm volatile("s_getpc_b64 %0" : "=s"(pc0));
    extern __shared__ __attribute__((aligned(16))) unsigned char lds_raw[];
    LAS unsigned char* L = (LAS unsigned char*)lds_raw;
    const u32 lb = (u32)(size_t)L;
    const int tid = threadIdx.x, lane = tid & 63, w = __builtin_amdgcn_readfirstlane(tid >> 6);
    const int bid = blockIdx.x;
    const int pair = bid >> 4, b = pair >> 3, kvh = pair & 7, s = bid & 15;
    const int jq = 4 * (s & 7) + 2 * b + (s >> 3);
    const int m1i = (s - kvh) & 7, jm = 4 * (7 * (kvh >> 1) + 4 * (kvh & 1) + m1i) + (jq & 3);
    const int x8 = bid & 7, hi = 4 * x8 + (bid >> 6), cg = (bid >> 3) & 7;
    const bool do_m1 = m1i < 4 - (kvh & 1), do_m2 = hi < 28;
    const int g = lane >> 4, n = lane & 15;
    const __amdgpu_buffer_rsrc_t rws = mk_rsrc(p.ws, WS_END);
    const u32 myslot = WS_SLOT + bid * SLOT;

    asm volatile("" ::"s"(p.k), "s"(p.v), "s"(p.x), "s"(p.wq), "s"(p.ws));
    if (w >= NSW) {
        __builtin_amdgcn_s_setprio(3);
        v4u xr0, xr1; v2u ep;
        {
            const void* xa = p.x + 8 * lane;
            const void* ea = p.ws + WS_EPOCH;
            asm volatile("global_load_dwordx4 %0, %3, off\n\tglobal_load_dwordx4 %1, %3, off offset:1024\n\tglobal_load_dwordx2 %2, %4, off sc1"
                         : "=&v"(xr0), "=&v"(xr1), "=&v"(ep) : "v"(xa), "v"(ea) : "memory");
        }
        {
            const v4u sv = {SENT, SENT, SENT, SENT};
#pragma unroll
            for (int u = 0; u < 3; ++u) __builtin_amdgcn_raw_buffer_store_b128(sv, rws, myslot + u * 1024 + lane * 16, 0, AUX_SC1);
            const bool zt = s == 0 && (lane >> 2) == 2;
            const u32 o4 = lane < 4 ? WS_ML + ((pair * 4 + lane) * 16 + s) * 8 : lane < 6 ? myslot + SL_FA + (lane - 4) * 128 : zt ? WS_TK + (pair * NSW + (lane & 3)) * 256 : OOR;
            __builtin_amdgcn_raw_buffer_store_b64(zt ? v2u{0u, 0u} : v2u{SENT, SENT}, rws, o4, 0, AUX_SC1);
        }
        u32 cpf[3];
        {
            const __amdgpu_buffer_rsrc_t rc = mk_rsrc((const void*)(pc0 & ~255ull), 24576);
#pragma unroll
            for (int u = 0; u < 3; ++u) cpf[u] = __builtin_amdgcn_raw_buffer_load_b32(rc, (bid & (256 / K_PF - 8)) == 0 ? lane * 128 + u * 8192 : OOR, 0, 0);
            __builtin_amdgcn_sched_barrier(0);
        }
        const int rot = 4 * (jq & 3);
        const u16* wqs = p.wq + (size_t)(lane >> 1) * 4096 + kvh * 512 + 16 * jq + 8 * (lane & 1);
#pragma unroll
        for (int u = 0; u < WQ; ++u) { const int ur = (u + rot) & 15; glds_sh(wqs + (size_t)ur * 32 * 4096, lb + L_WQ + ur * 1024); }
        asm volatile("s_waitcnt vmcnt(%3)" : "+v"(xr0), "+v"(xr1), "+v"(ep) : "i"(4 + WQ) : "memory");
#pragma unroll
        for (int u = WQ; u < 16; ++u) { const int ur = (u + rot) & 15; wait_vm<WQ - 1>(); glds_sh(wqs + (size_t)ur * 32 * 4096, lb + L_WQ + ur * 1024); }
        const u64 tag = ((((u64)ep.y << 32) | ep.x) + 1) & 0xFFFFFFFFFFFFull;
        float xv[2][8];
#pragma unroll
        for (int u = 0; u < 4; ++u) { xv[0][2 * u] = bf_lo(xr0[u]); xv[0][2 * u + 1] = bf_hi(xr0[u]); xv[1][2 * u] = bf_lo(xr1[u]); xv[1][2 * u + 1] = bf_hi(xr1[u]); }
        layer_norm(xv, L + L_XN, lane);
        {
            v4u B[16];
            load_act(B, L + L_XN, lane);
            __builtin_amdgcn_sched_barrier(0);
            wait_vm<0>();
            asm volatile("" ::"v"(cpf[0]), "v"(cpf[1]), "v"(cpf[2]));
            const f32x4 qv = gemv16(L + L_WQ, B, lane);
            const u32 qb = pk_bf16(sel4(qv, n >> 1), 0.f) & 0xffffu;
            GAS u64* dst = (GAS u64*)(p.ws + WS_QG) + (kvh * 2 + (n & 1)) * 512 + 16 * jq + 4 * g + (n >> 1);
            if (n < 8) __hip_atomic_store(dst, (tag << 16) | qb, RLX_AGENT);
        }
        {
            GAS u64* src = (GAS u64*)(p.ws + WS_QG) + (kvh * 2 + b) * 512 + lane;
            u64 A[8];
            __builtin_amdgcn_s_sleep(80);
#pragma unroll 1
            for (u32 spin = 0; spin < SPIN_CAP; ++spin) {
                bool ok = true;
#pragma unroll
                for (int k = 0; k < 8; ++k) A[k] = __hip_atomic_load(src + 64 * k, RLX_AGENT);
#pragma unroll
                for (int k = 0; k < 8; ++k) ok &= (A[k] >> 16) == tag;
                if (__all(ok)) break;
                __builtin_amdgcn_s_sleep(24);
            }
#pragma unroll
            for (int k = 0; k < 8; ++k) *(LAS u16*)(L + L_Q + (64 * k + lane) * 2) = (u16)A[k];
        }
        *(LAS u32*)(L + L_CNT) = 0u;
        wg_barrier();
        __builtin_amdgcn_s_setprio(1);
        layer_norm(xv, L + L_Y, lane);
        const int fb = bid >> 7, fq = (bid >> 3) & 15, hf = lane >> 5, fc = 64 * x8 + 4 * fq + 2 * hf;
        const u32 xo = *(const GAS u32*)(p.x + fb * DM + fc), b2o = *(const GAS u32*)(p.b2 + fc);
        const int col1 = do_m1 ? 16 * jm + 4 * g + ((n >> 1) & 3) : 0;
        const float bias1 = bf_lo(p.b1[col1]);
        if (do_m1) {
            const u16* s1 = p.w1 + (size_t)(lane >> 1) * FF + 16 * jm + 8 * (lane & 1);
#pragma unroll
            for (int u = 0; u < 16; ++u) { const int ur = (u + rot) & 15; wait_vm<WW1 - 1>(); glds_sh(s1 + (size_t)ur * 32 * FF, lb + L_W1 + ur * 1024); }
        }
        if (do_m2) {
            const u16* s2 = p.w2 + (size_t)(hi * 64 + (lane >> 3)) * DM + 64 * cg + 8 * (lane & 7);
#pragma unroll
            for (int u = 0; u < 8; ++u) { wait_vm<WW2 - 1>(); glds_nt(s2 + (size_t)u * 8 * DM, lb + L_W2 + u * 1024); }
        }
        wait_vm<0>();
        if (do_m1) {
            v4u B[16];
            load_act(B, L + L_Y, lane);
            const float u = rbf(rbf(sel4(gemv16(L + L_W1, B, lane), n >> 1)) + bias1);
            const float z = 1.5957691216057308f * fmaf(0.044715f * u * u, u, u);
            const float h = u / (1.f + __expf(-z));
            if (n < 8) __hip_atomic_store((GAS u64*)(p.ws + WS_HG) + (n & 1) * FF + col1, (tag << 16) | (pk_bf16(h, 0.f) & 0xffffu), RLX_AGENT);
        }
        const int pi = lane & 31, pw = ((pi & 3) * 8 + x8) * 8 + (pi >> 2);
        const u32 pslot = WS_SLOT + pw * SLOT + fb * 256 + fq * 16 + hf * 8;
        {
            const int ek = hf ? (pw >> 4) & 7 : x8, ej = hf ? 4 * (pw & 7) + 2 * (pw >> 7) + ((pw >> 3) & 1) : pi;
            GAS u64* ev = (GAS u64*)(p.ws + WS_QG) + ek * 1024 + 16 * ej;
            GAS u64* hs = do_m2 ? (GAS u64*)(p.ws + WS_HG) + 64 * hi + lane : ev;
            const int hstr = do_m2 ? FF : 0;
            u64 e, h0, h1;
            if (!do_m1) { __builtin_amdgcn_s_sleep(64); __builtin_amdgcn_s_sleep(64); }
            __builtin_amdgcn_s_sleep(64);
#pragma unroll 1
            for (u32 spin = 0; spin < SPIN_CAP; ++spin) {
                e = __hip_atomic_load(ev, RLX_AGENT); h0 = __hip_atomic_load(hs, RLX_AGENT); h1 = __hip_atomic_load(hs + hstr, RLX_AGENT);
                if (__all(((e >> 16) == tag) & ((h0 >> 16) == tag) & ((h1 >> 16) == tag))) break;
                __builtin_amdgcn_s_sleep(64);
            }
            *(LAS u16*)(L + L_H + lane * 2) = (u16)h0;
            *(LAS u16*)(L + L_H + 128 + lane * 2) = (u16)h1;
        }
        if (do_m2) {
#pragma unroll
            for (int cb = 0; cb < 4; ++cb) {
                f32x4 mp = {0.f, 0.f, 0.f, 0.f};
#pragma unroll
                for (int u = 0; u < 2; ++u) mp = mfma32(tr_frag(L + L_W2 + 32 * cb, 128, 32 * u, lane), *(const LAS v4u*)(L + L_H + (n & 1) * 128 + 64 * u + 16 * g), mp);
                __builtin_amdgcn_raw_buffer_store_b128(clean4(mp), rws, n < 2 ? myslot + SL_PM + n * 256 + cb * 64 + g * 16 : OOR, 0, AUX_SC1);
            }
        }
        float gate[2];
        {
            v2u a;
            __builtin_amdgcn_s_sleep(64);
#pragma unroll 1
            for (u32 spin = 0; spin < SPIN_CAP; ++spin) {
                asm volatile("" ::: "memory");
                a = __builtin_amdgcn_raw_buffer_load_b64(rws, pi < 28 ? pslot + SL_PM : OOR, 0, AUX_SC1);
                if (__all(max(a.x, a.y) != SENT)) break;
                __builtin_amdgcn_s_sleep(64);
            }
#pragma unroll
            for (int k = 0; k < 2; ++k) {
                const float mm = rbf(rbf(x16_sum(row_sum(__uint_as_float(a[k])))) + (k ? bf_hi(b2o) : bf_lo(b2o)));
                gate[k] = rbf(mm / (1.f + __expf(-mm)));
            }
        }
        __builtin_amdgcn_s_setprio(3);
        const u32 fme = lane == 0 ? myslot + SL_FA : OOR;
        const u32 fao = lane < 32 ? WS_SLOT + (((lane >> 4) * 8 + x8) * 16 + (lane & 15)) * SLOT + SL_FA : OOR;
        const u32 fbo = lane < 32 ? WS_SLOT + pw * SLOT + SL_FB : OOR;
#define WAIT_WORDS(o)                                                                                          \
    _Pragma("unroll 1") for (u32 spin = 0; spin < SPIN_CAP; ++spin) {                                          \
        asm volatile("" ::: "memory");                                                                         \
        if (__all(__builtin_amdgcn_raw_buffer_load_b32(rws, o, 0, AUX_SC1) != SENT)) break;                    \
        __builtin_amdgcn_s_sleep(1);                                                                           \
    }
#pragma unroll 1
        for (u32 spin = 0; spin < 16 * SPIN_CAP; ++spin) {
            if (__hip_atomic_load((LAS u32*)(L + L_CNT), __ATOMIC_RELAXED, __HIP_MEMORY_SCOPE_WORKGROUP) >= NSW) break;
            __builtin_amdgcn_s_sleep(1);
        }
        __builtin_amdgcn_raw_buffer_store_b32(1u, rws, fme, 0, AUX_SC1);
        wg_barrier();
        WAIT_WORDS(fao);
        wg_barrier();
        __builtin_amdgcn_s_sleep(8);
        __builtin_amdgcn_raw_buffer_store_b32(1u, rws, fme + (SL_FB - SL_FA), 0, AUX_SC1);
        wg_barrier();
        WAIT_WORDS(fbo);
#undef WAIT_WORDS
        {
            v2u a;
#pragma unroll 1
            for (u32 spin = 0; spin < SPIN_CAP; ++spin) {
                asm volatile("" ::: "memory");
                a = __builtin_amdgcn_raw_buffer_load_b64(rws, pslot + SL_PS, 0, AUX_SC1);
                if (__all(max(a.x, a.y) != SENT)) break;
            }
            const float s0 = rbf(x16_sum(row_sum(__uint_as_float(a.x)))), s1 = rbf(x16_sum(row_sum(__uint_as_float(a.y))));
            const u32 r = pk_bf16(bf_lo(xo) + rbf(s0 * gate[0]), bf_hi(xo) + rbf(s1 * gate[1]));
            if (pi == 0) *(GAS u32*)(p.out + fb * DM + fc) = r;
            if (bid == 0 && lane == 0) __hip_atomic_store((GAS u64*)(p.ws + WS_EPOCH), (((u64)ep.y << 32) | ep.x) + 1, RLX_AGENT);
        }
    } else {
        const u16* kb = p.k + (size_t)pair * SK * HD;
        const u16* vb = p.v + (size_t)pair * SK * HD;
        const __amdgpu_buffer_rsrc_t rk = mk_rsrc(kb, SK * HD * 2), rv = mk_rsrc(vb, SK * HD * 2);
        constexpr u32 TSTR = NSW * 4096;
        const u32 kl = (n >> 2) * 256 + g * 64 + (n & 3) * 16, vl = lane * 16;
        u32 ko = (s * NST * NSW + w) * 4096 + kl, vo = (s * NST * NSW + w) * 4096 + vl;
        v4u R[3][4], KX[RP][4], Ka[4], Va[4], Kb[4], Vb[4];
#define LK(X, u) do { wait_vm<WK - 1>(); X[u] = __builtin_amdgcn_raw_buffer_load_b128(rk, ko + 1024 * (u), 0, AUX_NT); __builtin_amdgcn_sched_barrier(0); } while (0)
#define LV(X, u) do { wait_vm<WV - 1>(); X[u] = __builtin_amdgcn_raw_buffer_load_b128(rv, vo + 1024 * (u), 0, AUX_NT); __builtin_amdgcn_sched_barrier(0); } while (0)
        const u32 park = lb + L_PARK + w * LP * 4096;
#pragma unroll
        for (int t = 0; t < LP; ++t) {
            const unsigned char* ks = (const unsigned char*)kb + ko;
            wait_vm<W0 - 1>(); glds_nt_off<0>(ks, park + t * 4096);
            wait_vm<W0 - 1>(); glds_nt_off<1024>(ks, park + t * 4096);
            wait_vm<W0 - 1>(); glds_nt_off<2048>(ks, park + t * 4096);
            wait_vm<W0 - 1>(); glds_nt_off<3072>(ks, park + t * 4096);
            ko += TSTR;
        }
#pragma unroll
        for (int t = 0; t < RP; ++t) { LK(KX[t], 0); LK(KX[t], 1); LK(KX[t], 2); LK(KX[t], 3); ko += TSTR; }
#pragma unroll
        for (int t = 0; t < 2; ++t) { LK(R[t], 0); LK(R[t], 1); LK(R[t], 2); LK(R[t], 3); ko += TSTR; }
        wg_barrier();
        const int jj = n & 3;
        const bool j0 = jj & 1, j1 = jj & 2;
        const v4u Q = *(const LAS v4u*)(L + L_Q + (n >> 2) * 256 + 64 * g + 16 * jj);
        const u32 mask0 = jj == 0 ? 0x0000ffffu : jj == 1 ? 0xffff0000u : 0u, mask1 = jj == 2 ? 0x0000ffffu : jj == 3 ? 0xffff0000u : 0u;
        float m = -INFINITY, l = 0.f, sc[4], pr[4];
        f32x4 acc0 = {0.f, 0.f, 0.f, 0.f}, acc1 = {0.f, 0.f, 0.f, 0.f};
#define QKD(KR)                                                                                                \
        f32x4 dd[4];                                                                                           \
        _Pragma("unroll") for (int u = 0; u < 4; ++u) dd[u] = mfma32(KR[u], Q, f32x4{0.f, 0.f, 0.f, 0.f});     \
        asm volatile("" : "+v"(dd[0]), "+v"(dd[1]), "+v"(dd[2]), "+v"(dd[3]));     \
        _Pragma("unroll") for (int u = 0; u < 4; ++u) sc[u] = sel4(dd[u], jj);
        LAS float* scp = (LAS float*)(L + L_SC + w * 8192) + lane;
#define KP(KR, idx)                                                                                            \
    do {                                                                                                       \
        QKD(KR)                                                                                                \
        const float y01 = (j0 ? sc[1] : sc[0]) + dpp<XOR1>(j0 ? sc[0] : sc[1]), y23 = (j0 ? sc[3] : sc[2]) + dpp<XOR1>(j0 ? sc[2] : sc[3]); \
        const float y = (j1 ? y23 : y01) + dpp<XOR2>(j1 ? y01 : y23);                                          \
        m = fmaxf(m, y);                                                                                       \
        scp[(idx) * 64] = y;                                                                                   \
        __builtin_amdgcn_sched_barrier(0);                                                                     \
    } while (0)
#define UNPARK(t, KR)                                                                                          \
    do {                                                                                                       \
        const LAS unsigned char* pk = L + L_PARK + (w * LP + (t)) * 4096 + lane * 16;                          \
        _Pragma("unroll") for (int u = 0; u < 4; ++u) KR[u] = *(const LAS v4u*)(pk + 1024 * u);                \
    } while (0)
#define VP(VR, sv)                                                                                             \
    do {                                                                                                       \
        const float p1 = ex2(fmaf(sv, SC, -mc));                                                               \
        l += p1;                                                                                               \
        const int pb = pk_bf16(p1, p1);                                                                        \
        _Pragma("unroll") for (int r = 0; r < 4; ++r) {                                                        \
            const u32 t_ = r == 0 ? __builtin_amdgcn_mov_dpp(pb, 0x00, 0xf, 0xf, true) : r == 1 ? __builtin_amdgcn_mov_dpp(pb, 0x55, 0xf, 0xf, true) \
                         : r == 2 ? __builtin_amdgcn_mov_dpp(pb, 0xAA, 0xf, 0xf, true) : __builtin_amdgcn_mov_dpp(pb, 0xFF, 0xf, 0xf, true); \
            acc0 = mfma16(VR[r].x, VR[r].y, t_ & mask0, t_ & mask1, acc0);                                     \
            acc1 = mfma16(VR[r].z, VR[r].w, t_ & mask0, t_ & mask1, acc1);                                     \
        }                                                                                                      \
        __builtin_amdgcn_sched_barrier(0);                                                                     \
    } while (0)
#define UKK(u, idx) do { LK(R[((u) + 2) % 3], 0); LK(R[((u) + 2) % 3], 1); KP(R[(u) % 3], idx); LK(R[((u) + 2) % 3], 2); LK(R[((u) + 2) % 3], 3); ko += TSTR; } while (0)
#define UVK(u, idx) do { LV(R[((u) + 2) % 3], 0); LV(R[((u) + 2) % 3], 1); KP(R[(u) % 3], idx); LV(R[((u) + 2) % 3], 2); LV(R[((u) + 2) % 3], 3); vo += TSTR; } while (0)
#define UVV(u, idx) do { const float sv_ = scp[(idx) * 64]; LV(R[((u) + 2) % 3], 0); LV(R[((u) + 2) % 3], 1); VP(R[(u) % 3], sv_); LV(R[((u) + 2) % 3], 2); LV(R[((u) + 2) % 3], 3); vo += TSTR; } while (0)
        static_assert(LP == 4 && RP == 8, "");
        LK(R[2], 0); KP(KX[0], 4); UNPARK(0, KX[0]); LK(R[2], 1); KP(R[0], 12); KP(KX[1], 5); UNPARK(1, KX[1]); LK(R[2], 2); KP(KX[2], 6); LK(R[2], 3); KP(KX[3], 7); ko += TSTR;
        LK(R[0], 0); KP(KX[0], 0); UNPARK(2, KX[2]); LK(R[0], 1); KP(R[1], 13); KP(KX[1], 1); UNPARK(3, KX[3]); LK(R[0], 2); KP(KX[4], 8); LK(R[0], 3); KP(KX[5], 9); ko += TSTR;
        LK(R[1], 0); KP(KX[2], 2); LK(R[1], 1); KP(R[2], 14); KP(KX[3], 3); LK(R[1], 2); KP(KX[6], 10); LK(R[1], 3); KP(KX[7], 11); ko += TSTR;
        constexpr int KA = (NKU - 3) / 3, KR_ = (NKU - 3) % 3;
        scp += 15 * 64;
#pragma unroll 1
        for (int it = 0; it < KA; ++it) { UKK(0, 0); UKK(1, 1); UKK(2, 2); scp += 3 * 64; }
        if (KR_ > 0) UKK(0, 0);
        if (KR_ > 1) UKK(1, 1);
        UVK(KR_, KR_); UVK(KR_ + 1, KR_ + 1);
        m = fmaxf(m, dpp<XOR1>(m)); m = fmaxf(m, dpp<XOR2>(m)); m = x32_max(x16_max(m));
        const float mc = m * SC;
        constexpr int U0 = KR_ + 2, VA = (NS - 2) / 3, VR_ = (NS - 2) % 3;
        scp -= (15 + 3 * KA) * 64;
#pragma unroll 1
        for (int it = 0; it < VA; ++it) { UVV(U0, 0); UVV(U0 + 1, 1); UVV(U0 + 2, 2); scp += 3 * 64; }
        if (VR_ > 0) UVV(U0, 0);
        if (VR_ > 1) UVV(U0 + 1, 1);
        u32 tko = lane == 0 ? WS_TK + (pair * NSW + w) * 256 : OOR;
#define TICKET() __builtin_amdgcn_raw_ptr_buffer_atomic_add_i32(1, rws, tko, 0, AUX_SC1)
        int tk;
        {
            constexpr int U1 = U0 + VR_;
            const float s0 = scp[VR_ * 64], s1 = scp[(VR_ + 1) * 64];
            LK(Ka, 0); LK(Ka, 1); VP(R[U1 % 3], s0); LK(Ka, 2); LK(Ka, 3);
            tk = TICKET();
            LV(Va, 0); LV(Va, 1); VP(R[(U1 + 1) % 3], s1); LV(Va, 2); LV(Va, 3);
            l += dpp<XOR1>(l); l += dpp<XOR2>(l);
        }
        const int bb = w & 1, dh = w >> 1;
        u32 cnt_a = lb + L_CNT, cnt_v = lane == 0;
        u32 a_mw = L_MRG + ((w * 4 + (n >> 2)) * 132 + 32 * g + jj) * 4, a_lw = L_MML + (w * 4 + (n >> 2)) * 8;
        u32 a_mr = L_MRG + (w * 132 + 4 * (lane & 31)) * 4, a_lr = L_MML + w * 8;
        u32 o_pa = lane < 32 ? myslot + w * 512 + lane * 16 : OOR, o_pl = lane == 0 ? WS_ML + ((pair * 4 + w) * 16 + s) * 8 : OOR;
        u32 o_ga = WS_SLOT + ((bb * 8 + x8) * 16 + 4 * g) * SLOT + (hi & 3) * 512 + dh * 256 + n * 16;
        u32 o_gl = WS_ML + (((bb * 8 + x8) * 4 + (hi & 3)) * 16 + ((n + 4 * g) & 15)) * 8;
        u32 a_wo = L_WO + 32 * w + (8 * g + ((lane >> 2) & 3)) * 128 + 8 * jj, a_ow = L_O + bb * 256 + dh * 128 + n * 8 + g * 2;
        u32 a_or = L_O + (n & 1) * 256 + 16 * g, o_ps = n < 2 ? myslot + SL_PS + n * 256 + w * 64 + g * 16 : OOR;
        asm volatile("" : "+v"(cnt_a), "+v"(cnt_v), "+v"(a_mw), "+v"(a_lw), "+v"(a_mr), "+v"(a_lr), "+v"(o_pa), "+v"(o_pl));
        asm volatile("" : "+v"(o_ga), "+v"(o_gl), "+v"(a_wo), "+v"(a_ow), "+v"(a_or), "+v"(o_ps));
        bool more;
        u32 t, ord = 0;
#define WAITC(N) asm volatile("s_cmpk_ge_u32 %0, %2\n\ts_cbranch_scc1 1f\n\ts_waitcnt vmcnt(%1)\n1:" ::"s"(t), "i"(N), "i"(POOL - NBURST) : "memory", "scc")
#define LKC(X, u) do { WAITC(WKD - 1); X[u] = __builtin_amdgcn_raw_buffer_load_b128(rk, ko + 1024 * (u), 0, AUX_NT); __builtin_amdgcn_sched_barrier(0); } while (0)
#define LVC(X, u) do { WAITC(WVD - 1); X[u] = __builtin_amdgcn_raw_buffer_load_b128(rv, vo + 1024 * (u), 0, AUX_NT); __builtin_amdgcn_sched_barrier(0); } while (0)
#define STEP(KC, VC, KN, VN)                                                                                   \
    do {                                                                                                       \
        t = __builtin_amdgcn_readfirstlane(tk);                                                                \
        more = t < (u32)POOL;                                                                                  \
        const u32 to = more ? (16 * NST * NSW + NSW * t + w) * 4096 : 0x40000000u, tm_ = more ? 0u : OOR;      \
        asm volatile("v_add_u32 %0, %3, %4\n\tv_add_u32 %1, %3, %5\n\tv_or_b32 %2, %6, %2" : "+v"(ko), "+v"(vo), "+v"(tko) : "s"(to), "v"(kl), "v"(vl), "s"(tm_)); \
        if (!more) asm volatile("ds_add_rtn_u32 %0, %1, %2" : "+v"(ord) : "v"(cnt_a), "v"(cnt_v) : "memory");  \
        LKC(KN, 0); LKC(KN, 1); LKC(KN, 2); LKC(KN, 3); LVC(VN, 0);                                            \
        tk = TICKET(); __builtin_amdgcn_sched_barrier(0);                                                      \
        LVC(VN, 1); LVC(VN, 2); LVC(VN, 3);                                                                    \
        {                                                                                                      \
            QKD(KC)                                                                                            \
            _Pragma("unroll") for (int u = 0; u < 4; ++u) sc[u] += dpp<XOR1>(sc[u]);                           \
            _Pragma("unroll") for (int u = 0; u < 4; ++u) sc[u] += dpp<XOR2>(sc[u]);                           \
            __builtin_amdgcn_sched_barrier(0);                                                                 \
        }                                                                                                      \
        {                                                                                                      \
            const float tm = x32_max(x16_max(fmaxf(fmaxf(sc[0], sc[1]), fmaxf(sc[2], sc[3]))));                \
            const float mn = fmaxf(m, tm), mc2 = mn * SC, alpha = ex2((m - mn) * SC);                          \
            _Pragma("unroll") for (int r = 0; r < 4; ++r) pr[r] = ex2(fmaf(sc[r], SC, -mc2));                  \
            l = fmaf(l, alpha, (pr[0] + pr[1]) + (pr[2] + pr[3]));                                             \
            m = mn;                                                                                            \
            acc0 *= alpha; acc1 *= alpha;                                                                      \
            __builtin_amdgcn_sched_barrier(0);                                                                 \
        }                                                                                                      \
        _Pragma("unroll") for (int r = 0; r < 4; ++r) {                                                        \
            const u32 pb = pk_bf16(pr[r], pr[r]), x0 = pb & mask0, x1 = pb & mask1;                            \
            acc0 = mfma16(VC[r].x, VC[r].y, x0, x1, acc0);                                                     \
            acc1 = mfma16(VC[r].z, VC[r].w, x0, x1, acc1);                                                     \
        }                                                                                                      \
        __builtin_amdgcn_sched_barrier(0);                                                                     \
    } while (0)
#pragma unroll 1
        for (;;) {
            STEP(Ka, Va, Kb, Vb);
            if (!more) break;
            STEP(Kb, Vb, Ka, Va);
            if (!more) break;
        }
        asm volatile("s_waitcnt lgkmcnt(0)" : "+v"(ord)::"memory");
        const u32 ord_s = __builtin_amdgcn_readfirstlane(ord);
        {
            LAS float* mr = (LAS float*)(L + a_mw);
#pragma unroll
            for (int r = 0; r < 4; ++r) { mr[8 * r] = acc0[r]; mr[8 * r + 4] = acc1[r]; }
            *(LAS f32x2*)(L + a_lw) = f32x2{m * SC, x32_sum(x16_sum(l))};
        }
        if (ord_s == 0) {
            const u16* so = p.wo + (size_t)(hi * 128 + (lane >> 3)) * DM + 64 * cg + 8 * (lane & 7);
#pragma unroll
            for (int u = 0; u < 16; ++u) glds_nt(so + (size_t)u * 8 * DM, lb + L_WO + u * 1024);
        }
        wg_barrier();
        {
            f32x2 ml[NSW]; f32x4 ov[NSW];
#pragma unroll
            for (int u = 0; u < NSW; ++u) ml[u] = *(const LAS f32x2*)(L + a_lr + u * 32);
#pragma unroll
            for (int u = 0; u < NSW; ++u) ov[u] = *(const LAS f32x4*)(L + a_mr + u * (4 * 132 * 4));
            const float M = fmaxf(fmaxf(ml[0].x, ml[1].x), fmaxf(ml[2].x, ml[3].x));
            float f[NSW];
#pragma unroll
            for (int u = 0; u < NSW; ++u) f[u] = ex2(ml[u].x - M);
            const f32x4 o = (f[0] * ov[0] + f[1] * ov[1]) + (f[2] * ov[2] + f[3] * ov[3]);
            __builtin_amdgcn_raw_buffer_store_b128(clean4(o), rws, o_pa, 0, AUX_SC1);
            const float Ls = fmaf(f[0], ml[0].y, f[1] * ml[1].y) + fmaf(f[2], ml[2].y, f[3] * ml[3].y);
            __builtin_amdgcn_raw_buffer_store_b64(v2u{no_sent(M), no_sent(Ls)}, rws, o_pl, 0, AUX_SC1);
        }
        v4u WO[4];
        {
            v4u a[4]; v2u q2;
#define GATHER()                                                                                               \
    do {                                                                                                       \
        q2 = __builtin_amdgcn_raw_buffer_load_b64(rws, o_gl, 0, AUX_SC1);                                      \
        _Pragma("unroll") for (int k = 0; k < 4; ++k) a[k] = __builtin_amdgcn_raw_buffer_load_b128(rws, o_ga, k * SLOT, AUX_SC1); \
    } while (0)
#define ARRIVED() __all(max(max(max(umax4(a[0]), umax4(a[1])), max(umax4(a[2]), umax4(a[3]))), max(q2.x, q2.y)) != SENT)
            wait_vm<2>();
            wg_barrier();
            GATHER();
            __builtin_amdgcn_sched_barrier(0);
#pragma unroll
            for (int u = 0; u < 4; ++u) {
                const v2u lo = tr_read(L + a_wo + 4096 * u), hi2 = tr_read(L + a_wo + 4096 * u + 512);
                WO[u] = v4u{lo.x, lo.y, hi2.x, hi2.y};
            }
            __builtin_amdgcn_sched_barrier(0);
            if (!ARRIVED()) {
#pragma unroll 1
                for (u32 spin = 0; spin < SPIN_CAP; ++spin) {
                    asm volatile("" ::: "memory");
                    GATHER();
                    if (ARRIVED()) break;
                }
            }
            const float mp = __uint_as_float(q2.x);
            const float M = row_max(mp);
            const float wt = ex2(mp - M);
            const float inv = __builtin_amdgcn_rcpf(row_sum(wt * __uint_as_float(q2.y)));
            const f32x4 o = (dpp<0x150>(wt) * __builtin_bit_cast(f32x4, a[0]) + dpp<0x151>(wt) * __builtin_bit_cast(f32x4, a[1])) +
                            (dpp<0x152>(wt) * __builtin_bit_cast(f32x4, a[2]) + dpp<0x153>(wt) * __builtin_bit_cast(f32x4, a[3]));
            const auto s02 = __builtin_amdgcn_permlane32_swap(__float_as_uint(o[0]), __float_as_uint(o[2]), false, false);
            const auto s13 = __builtin_amdgcn_permlane32_swap(__float_as_uint(o[1]), __float_as_uint(o[3]), false, false);
            const float xs = __uint_as_float(s02[0]) + __uint_as_float(s02[1]), ys = __uint_as_float(s13[0]) + __uint_as_float(s13[1]);
            const auto sz = __builtin_amdgcn_permlane16_swap(__float_as_uint(xs), __float_as_uint(ys), false, false);
            const float z = (__uint_as_float(sz[0]) + __uint_as_float(sz[1])) * inv;
            *(LAS u16*)(L + a_ow) = (u16)pk_bf16(z, z);
        }
        wg_barrier();
        {
            v4u ob[4];
#pragma unroll
            for (int u = 0; u < 4; ++u) ob[u] = *(const LAS v4u*)(L + a_or + 64 * u);
            __builtin_amdgcn_sched_barrier(0);
            const f32x4 z = {0.f, 0.f, 0.f, 0.f};
            const f32x4 sp = mfma32(WO[2], ob[2], mfma32(WO[0], ob[0], z)) + mfma32(WO[3], ob[3], mfma32(WO[1], ob[1], z));
            __builtin_amdgcn_raw_buffer_store_b128(clean4(sp), rws, o_ps, 0, AUX_SC1);
        }
        asm volatile("" ::"v"(Ka[0]), "v"(Ka[1]), "v"(Ka[2]), "v"(Ka[3]), "v"(Va[0]), "v"(Va[1]), "v"(Va[2]), "v"(Va[3]), "v"(tk));
        asm volatile("" ::"v"(Kb[0]), "v"(Kb[1]), "v"(Kb[2]), "v"(Kb[3]), "v"(Vb[0]), "v"(Vb[1]), "v"(Vb[2]), "v"(Vb[3]));
    }
}

extern "C" void kernel_launch(void* const* d_in, const int* in_sizes, int n_in, void* d_out, int out_size, void* d_ws, size_t ws_size,
                              hipStream_t stream) {
    Params p;
    p.x = (const u16*)d_in[0]; p.k = (const u16*)d_in[1]; p.v = (const u16*)d_in[2]; p.wq = (const u16*)d_in[3]; p.wo = (const u16*)d_in[4];
    p.w1 = (const u16*)d_in[6]; p.b1 = (const u16*)d_in[7]; p.w2 = (const u16*)d_in[8]; p.b2 = (const u16*)d_in[9];
    p.out = (u16*)d_out; p.ws = (unsigned char*)d_ws;
    (void)hipFuncSetAttribute((const void*)fused_decode, hipFuncAttributeMaxDynamicSharedMemorySize, L_END);
    hipLaunchKernelGGL(fused_decode, dim3(NWG), dim3(NTHR), L_END, stream, p);
}
```

```cpp
#include <hip/hip_runtime.h>
#include <hip/hip_bf16.h>
#include <stdint.h>

#define GAS __attribute__((address_space(1)))
#define LAS __attribute__((address_space(3)))
typedef unsigned u32;
typedef unsigned long long u64;
typedef unsigned short u16;
typedef unsigned v4u __attribute__((ext_vector_type(4)));
typedef unsigned v2u __attribute__((ext_vector_type(2)));
typedef float f32x4 __attribute__((ext_vector_type(4)));
typedef float f32x2 __attribute__((ext_vector_type(2)));
typedef short s16x4 __attribute__((ext_vector_type(4)));
typedef __bf16 bf16x8 __attribute__((ext_vector_type(8)));
typedef __bf16 bf16x2 __attribute__((ext_vector_type(2)));

constexpr int DM = 512, HD = 128, SK = 32768, FF = 1792;
constexpr int NWG = 256, NSW = 4, NTHR = (NSW + 1) * 64;
constexpr int NS = SK / 16 / 16 / NSW;
constexpr int LP = 4, RP = 8, NPRO = LP + RP + 2;
constexpr int NBL = 6, KA = (NS - NPRO - NBL) / 3, VA = (NS - 2) / 3 - 1;
static_assert(NS == 32 && NPRO + NBL + 3 * KA == NS && 3 * VA + 5 == NS, "");
constexpr int T_OFF = 130 * 16, T_K = 927, T_V = 886;
constexpr int WQ = 8, WW1 = 2, WW2 = 2;
constexpr float SC = 0.08838834764831845f * 1.4426950408889634f;
constexpr u32 SENT = 0xFFFFFFFFu;
constexpr u32 SPIN_CAP = 1u << 21;
constexpr u32 OOR = 0xFFFFFF00u;

constexpr u32 WS_EPOCH = 0;
constexpr u32 WS_QG = 4096;
constexpr u32 WS_HG = WS_QG + 8 * 2 * 512 * 8;
constexpr u32 WS_TG = 102400;
constexpr u32 WS_ML = 118784;
constexpr u32 WS_SLOT = 131072, SLOT = 4352;
constexpr u32 SL_PS = 2048, SL_PM = 2560, SL_FA = 3072, SL_FB = 3200;
constexpr u32 WS_END = WS_SLOT + NWG * SLOT;
static_assert(WS_HG + 2 * FF * 8 <= WS_TG && WS_TG + 256 * 8 <= WS_ML && WS_ML + 8192 <= WS_SLOT, "");

constexpr u32 L_WQ = 0, L_W1 = 0, L_WO = 16384, L_W2 = 32768;
constexpr u32 L_XN = 40960, L_Y = L_XN + 2048, L_Q = L_Y + 2048, L_H = L_Q + 1024, L_O = L_H + 256;
constexpr u32 L_MRG = L_O + 512;
constexpr u32 L_MML = L_MRG + NSW * 4 * 132 * 4;
constexpr u32 L_CNT = L_MML + NSW * 4 * 2 * 4;
constexpr u32 L_SC = 57344;
constexpr u32 L_PARK = L_SC + NSW * 8192;
constexpr u32 L_END = L_PARK + NSW * LP * 4096;
static_assert(L_CNT + 16 <= L_SC && L_END > 80 * 1024 && L_END <= 160 * 1024, "more than half of a CU's LDS: one workgroup per CU");

struct Params {
    const u16 *x, *k, *v, *wq, *wo, *w1, *b1, *w2, *b2;
    u16* out;
    unsigned char* ws;
};

__device__ __forceinline__ f32x4 mfma32(v4u a, v4u b, f32x4 c) {
    return __builtin_amdgcn_mfma_f32_16x16x32_bf16(__builtin_bit_cast(bf16x8, a), __builtin_bit_cast(bf16x8, b), c, 0, 0, 0);
}
__device__ __forceinline__ f32x4 mfma16(u32 a0, u32 a1, u32 b0, u32 b1, f32x4 c) {
    return __builtin_amdgcn_mfma_f32_16x16x16bf16_1k(__builtin_bit_cast(s16x4, v2u{a0, a1}), __builtin_bit_cast(s16x4, v2u{b0, b1}), c, 0, 0, 0);
}
__device__ __forceinline__ u32 pk_bf16(float lo, float hi) {
    return __builtin_bit_cast(u32, __builtin_convertvector(f32x2{lo, hi}, bf16x2));
}
__device__ __forceinline__ float bf_lo(u32 w) { return __builtin_bit_cast(float, w << 16); }
__device__ __forceinline__ float bf_hi(u32 w) { return __builtin_bit_cast(float, w & 0xffff0000u); }
__device__ __forceinline__ float rbf(float f) { return bf_lo(pk_bf16(f, f)); }
__device__ __forceinline__ float ex2(float x) { return __builtin_amdgcn_exp2f(x); }
template <int CTRL> __device__ __forceinline__ float dpp(float x) {
    return __builtin_bit_cast(float, __builtin_amdgcn_mov_dpp(__builtin_bit_cast(int, x), CTRL, 0xf, 0xf, true));
}
constexpr int XOR1 = 0xB1, XOR2 = 0x4E, XOR7 = 0x141, ROR8 = 0x128;
__device__ __forceinline__ float x16_max(float x) {
    auto s = __builtin_amdgcn_permlane16_swap(__float_as_uint(x), __float_as_uint(x), false, false);
    return fmaxf(__uint_as_float(s[0]), __uint_as_float(s[1]));
}
__device__ __forceinline__ float x32_max(float x) {
    auto s = __builtin_amdgcn_permlane32_swap(__float_as_uint(x), __float_as_uint(x), false, false);
    return fmaxf(__uint_as_float(s[0]), __uint_as_float(s[1]));
}
__device__ __forceinline__ float x16_sum(float x) {
    auto s = __builtin_amdgcn_permlane16_swap(__float_as_uint(x), __float_as_uint(x), false, false);
    return __uint_as_float(s[0]) + __uint_as_float(s[1]);
}
__device__ __forceinline__ float x32_sum(float x) {
    auto s = __builtin_amdgcn_permlane32_swap(__float_as_uint(x), __float_as_uint(x), false, false);
    return __uint_as_float(s[0]) + __uint_as_float(s[1]);
}
__device__ __forceinline__ float row_sum(float x) {
    x += dpp<XOR1>(x); x += dpp<XOR2>(x); x += dpp<XOR7>(x); x += dpp<ROR8>(x); return x;
}
__device__ __forceinline__ float row_max(float x) {
    float r;
    asm("s_nop 1\n\tv_max_f32_dpp %0, %1, %1 quad_perm:[1,0,3,2] row_mask:0xf bank_mask:0xf\n\ts_nop 1\n\t"
        "v_max_f32_dpp %0, %0, %0 quad_perm:[2,3,0,1] row_mask:0xf bank_mask:0xf\n\ts_nop 1\n\t"
        "v_max_f32_dpp %0, %0, %0 row_half_mirror row_mask:0xf bank_mask:0xf\n\ts_nop 1\n\t"
        "v_max_f32_dpp %0, %0, %0 row_ror:8 row_mask:0xf bank_mask:0xf" : "=&v"(r) : "v"(x));
    return r;
}
__device__ __forceinline__ float wave_sum(float x) { return x32_sum(x16_sum(row_sum(x))); }
__device__ __forceinline__ u32 umax4(v4u a) { return max(max(a.x, a.y), max(a.z, a.w)); }
__device__ __forceinline__ u32 no_sent(float f) { return min(__float_as_uint(f), 0xFFFFFFFEu); }

#define RLX_AGENT __ATOMIC_RELAXED, __HIP_MEMORY_SCOPE_AGENT
constexpr int AUX_NT = 2, AUX_SC1 = 16;
__device__ __forceinline__ __amdgpu_buffer_rsrc_t mk_rsrc(const void* p, u32 bytes) {
    return __builtin_amdgcn_make_buffer_rsrc((void*)p, 0, bytes, 0x00020000);
}
__device__ __forceinline__ void glds_nt(const void* src, u32 dst) {
    asm volatile("s_mov_b32 m0, %1\n\ts_nop 0\n\tglobal_load_lds_dwordx4 %0, off nt" ::"v"(src), "s"(dst) : "memory");
}
__device__ __forceinline__ void glds_sh(const void* src, u32 dst) {
    asm volatile("s_mov_b32 m0, %1\n\ts_nop 0\n\tglobal_load_lds_dwordx4 %0, off" ::"v"(src), "s"(dst) : "memory");
}
template <int OFF> __device__ __forceinline__ void glds_nt_off(const void* src, u32 dst) {
    asm volatile("s_mov_b32 m0, %1\n\ts_nop 0\n\tglobal_load_lds_dwordx4 %0, off offset:%2 nt" ::"v"(src), "s"(dst), "i"(OFF) : "memory");
}
template <int N> __device__ __forceinline__ void wait_vm() { asm volatile("s_waitcnt vmcnt(%0)" ::"i"(N) : "memory"); }
__device__ __forceinline__ void wg_barrier() { asm volatile("s_waitcnt lgkmcnt(0)\n\ts_barrier" ::: "memory"); }
__device__ __forceinline__ v2u tr_read(const LAS unsigned char* p) {
    return __builtin_bit_cast(v2u, __builtin_amdgcn_ds_read_tr16_b64_v4i16((LAS s16x4*)p));
}
__device__ __forceinline__ v4u tr_frag(const LAS unsigned char* img, int pitch, int row0, int lane) {
    const int g = lane >> 4, q = (lane >> 2) & 3, p = lane & 3;
    const LAS unsigned char* a = img + (row0 + 8 * g + q) * pitch + 8 * p;
    const v2u lo = tr_read(a), hi = tr_read(a + 4 * pitch);
    return v4u{lo.x, lo.y, hi.x, hi.y};
}

__device__ __forceinline__ void load_act(v4u (&B)[16], const LAS unsigned char* act, int lane) {
    const LAS unsigned char* ap = act + (lane & 1) * 1024 + (lane >> 4) * 16;
#pragma unroll
    for (int s = 0; s < 16; ++s) B[s] = *(const LAS v4u*)(ap + 64 * s);
}
__device__ __forceinline__ f32x4 gemv16(const LAS unsigned char* img, const v4u (&B)[16], int lane) {
    v4u A[16];
#pragma unroll
    for (int s = 0; s < 16; ++s) A[s] = tr_frag(img, 32, 32 * s, lane);
    __builtin_amdgcn_sched_barrier(0);
    f32x4 a[4];
#pragma unroll
    for (int s = 0; s < 16; ++s) a[s & 3] = mfma32(A[s], B[s], s < 4 ? f32x4{0.f, 0.f, 0.f, 0.f} : a[s & 3]);
    return (a[0] + a[1]) + (a[2] + a[3]);
}
__device__ __forceinline__ void layer_norm(float (&v)[2][8], LAS unsigned char* dst, int lane) {
    float mean[2], rs[2];
#pragma unroll
    for (int bb = 0; bb < 2; ++bb) mean[bb] = wave_sum(((v[bb][0] + v[bb][1]) + (v[bb][2] + v[bb][3])) + ((v[bb][4] + v[bb][5]) + (v[bb][6] + v[bb][7]))) * (1.f / DM);
#pragma unroll
    for (int bb = 0; bb < 2; ++bb) {
        float sq = 0.f;
#pragma unroll
        for (int u = 0; u < 8; ++u) { v[bb][u] -= mean[bb]; sq = fmaf(v[bb][u], v[bb][u], sq); }
        rs[bb] = __builtin_amdgcn_rsqf(wave_sum(sq) * (1.f / DM) + 1e-5f);
    }
#pragma unroll
    for (int bb = 0; bb < 2; ++bb) {
        v4u o;
#pragma unroll
        for (int u = 0; u < 4; ++u) {
            o[u] = pk_bf16(v[bb][2 * u] * rs[bb], v[bb][2 * u + 1] * rs[bb]);
            v[bb][2 * u] = bf_lo(o[u]); v[bb][2 * u + 1] = bf_hi(o[u]);
        }
        *(LAS v4u*)(dst + bb * 1024 + lane * 16) = o;
    }
}
__device__ __forceinline__ float sel4(f32x4 v, int r) {
    const bool b0 = r & 1, b1 = r & 2;
    const float lo = b0 ? v[1] : v[0], hi = b0 ? v[3] : v[2];
    return b1 ? hi : lo;
}
__device__ __forceinline__ v4u clean4(f32x4 v) { return v4u{no_sent(v[0]), no_sent(v[1]), no_sent(v[2]), no_sent(v[3])}; }

__global__ void __launch_bounds__(NTHR, 2) fused_decode(Params p) {
    extern __shared__ __attribute__((aligned(16))) unsigned char lds_raw[];
    LAS unsigned char* L = (LAS unsigned char*)lds_raw;
    const u32 lb = (u32)(size_t)L;
    const int tid = threadIdx.x, lane = tid & 63, w = __builtin_amdgcn_readfirstlane(tid >> 6);
    const int bid = blockIdx.x;
    const int pair = bid >> 4, b = pair >> 3, kvh = pair & 7, s = bid & 15;
    const int jq = 4 * (s & 7) + 2 * b + (s >> 3);
    const int m1i = (s - kvh) & 7, jm = 4 * (7 * (kvh >> 1) + 4 * (kvh & 1) + m1i) + (jq & 3);
    const int x8 = bid & 7, hi = 4 * x8 + (bid >> 6), cg = (bid >> 3) & 7;
    const bool do_m1 = m1i < 4 - (kvh & 1), do_m2 = hi < 28;
    const int g = lane >> 4, n = lane & 15;
    const __amdgpu_buffer_rsrc_t rws = mk_rsrc(p.ws, WS_END);
    const u32 myslot = WS_SLOT + bid * SLOT;

    const u32 t_start = (u32)__builtin_amdgcn_s_memrealtime();
    asm volatile("" ::"s"(p.k), "s"(p.v), "s"(p.x), "s"(p.wq), "s"(p.ws));
    if (w >= NSW) {
        __builtin_amdgcn_s_setprio(3);
        v4u xr0, xr1; v2u ep;
        {
            const void* xa = p.x + 8 * lane;
            const void* ea = p.ws + WS_EPOCH;
            asm volatile("global_load_dwordx4 %0, %3, off\n\tglobal_load_dwordx4 %1, %3, off offset:1024\n\tglobal_load_dwordx2 %2, %4, off sc1"
                         : "=&v"(xr0), "=&v"(xr1), "=&v"(ep) : "v"(xa), "v"(ea) : "memory");
        }
        {
            const v4u sv = {SENT, SENT, SENT, SENT};
#pragma unroll
            for (int u = 0; u < 3; ++u) __builtin_amdgcn_raw_buffer_store_b128(sv, rws, myslot + u * 1024 + lane * 16, 0, AUX_SC1);
            const u32 o4 = lane < 4 ? WS_ML + ((pair * 4 + lane) * 16 + s) * 8 : lane < 6 ? myslot + SL_FA + (lane - 4) * 128 : OOR;
            __builtin_amdgcn_raw_buffer_store_b64(v2u{SENT, SENT}, rws, o4, 0, AUX_SC1);
        }
        const int rot = 4 * (jq & 3);
        const u16* wqs = p.wq + (size_t)(lane >> 1) * 4096 + kvh * 512 + 16 * jq + 8 * (lane & 1);
#pragma unroll
        for (int u = 0; u < WQ; ++u) { const int ur = (u + rot) & 15; glds_sh(wqs + (size_t)ur * 32 * 4096, lb + L_WQ + ur * 1024); }
        asm volatile("s_waitcnt vmcnt(%3)" : "+v"(xr0), "+v"(xr1), "+v"(ep) : "i"(4 + WQ) : "memory");
#pragma unroll
        for (int u = WQ; u < 16; ++u) { const int ur = (u + rot) & 15; wait_vm<WQ - 1>(); glds_sh(wqs + (size_t)ur * 32 * 4096, lb + L_WQ + ur * 1024); }
        const u64 tag = ((((u64)ep.y << 32) | ep.x) + 1) & 0xFFFFFFFFFFFFull;
        float xv[2][8];
#pragma unroll
        for (int u = 0; u < 4; ++u) { xv[0][2 * u] = bf_lo(xr0[u]); xv[0][2 * u + 1] = bf_hi(xr0[u]); xv[1][2 * u] = bf_lo(xr1[u]); xv[1][2 * u + 1] = bf_hi(xr1[u]); }
        layer_norm(xv, L + L_XN, lane);
        {
            v4u B[16];
            load_act(B, L + L_XN, lane);
            __builtin_amdgcn_sched_barrier(0);
            wait_vm<0>();
            const f32x4 qv = gemv16(L + L_WQ, B, lane);
            const u32 qb = pk_bf16(sel4(qv, n >> 1), 0.f) & 0xffffu;
            GAS u64* dst = (GAS u64*)(p.ws + WS_QG) + (kvh * 2 + (n & 1)) * 512 + 16 * jq + 4 * g + (n >> 1);
            if (n < 8) __hip_atomic_store(dst, (tag << 16) | qb, RLX_AGENT);
            if (lane == 8) __hip_atomic_store((GAS u64*)(p.ws + WS_TG) + kvh * 32 + jq, (tag << 16) | (t_start & 0xffffu), RLX_AGENT);
        }
        {
            GAS u64* src = (GAS u64*)(p.ws + WS_QG) + (kvh * 2 + b) * 512 + lane;
            GAS u64* tsrc = (GAS u64*)(p.ws + WS_TG) + kvh * 32 + (lane & 31);
            u64 A[8], T;
            __builtin_amdgcn_s_sleep(80);
#pragma unroll 1
            for (u32 spin = 0; spin < SPIN_CAP; ++spin) {
                bool ok = true;
#pragma unroll
                for (int k = 0; k < 8; ++k) A[k] = __hip_atomic_load(src + 64 * k, RLX_AGENT);
                T = __hip_atomic_load(tsrc, RLX_AGENT);
#pragma unroll
                for (int k = 0; k < 8; ++k) ok &= (A[k] >> 16) == tag;
                ok &= (T >> 16) == tag;
                if (__all(ok)) break;
                __builtin_amdgcn_s_sleep(24);
            }
#pragma unroll
            for (int k = 0; k < 8; ++k) *(LAS u16*)(L + L_Q + (64 * k + lane) * 2) = (u16)A[k];
            const float early = -x16_max(row_max(-(float)(short)((u32)T - t_start)));
            *(LAS v2u*)(L + L_CNT) = v2u{0u, t_start + (int)early};
        }
        wg_barrier();
        __builtin_amdgcn_s_setprio(1);
        layer_norm(xv, L + L_Y, lane);
        const int fb = bid >> 7, fq = (bid >> 3) & 15, hf = lane >> 5, fc = 64 * x8 + 4 * fq + 2 * hf;
        const u32 xo = *(const GAS u32*)(p.x + fb * DM + fc), b2o = *(const GAS u32*)(p.b2 + fc);
        const int col1 = do_m1 ? 16 * jm + 4 * g + ((n >> 1) & 3) : 0;
        const float bias1 = bf_lo(p.b1[col1]);
        if (do_m1) {
            const u16* s1 = p.w1 + (size_t)(lane >> 1) * FF + 16 * jm + 8 * (lane & 1);
#pragma unroll
            for (int u = 0; u < 16; ++u) { const int ur = (u + rot) & 15; wait_vm<WW1 - 1>(); glds_sh(s1 + (size_t)ur * 32 * FF, lb + L_W1 + ur * 1024); }
        }
        if (do_m2) {
            const u16* s2 = p.w2 + (size_t)(hi * 64 + (lane >> 3)) * DM + 64 * cg + 8 * (lane & 7);
#pragma unroll
            for (int u = 0; u < 8; ++u) { wait_vm<WW2 - 1>(); glds_nt(s2 + (size_t)u * 8 * DM, lb + L_W2 + u * 1024); }
        }
        wait_vm<0>();
        if (do_m1) {
            v4u B[16];
            load_act(B, L + L_Y, lane);
            const float u = rbf(rbf(sel4(gemv16(L + L_W1, B, lane), n >> 1)) + bias1);
            const float z = 1.5957691216057308f * fmaf(0.044715f * u * u, u, u);
            const float h = u / (1.f + __expf(-z));
            if (n < 8) __hip_atomic_store((GAS u64*)(p.ws + WS_HG) + (n & 1) * FF + col1, (tag << 16) | (pk_bf16(h, 0.f) & 0xffffu), RLX_AGENT);
        }
        const int pi = lane & 31, pw = ((pi & 3) * 8 + x8) * 8 + (pi >> 2);
        const u32 pslot = WS_SLOT + pw * SLOT + fb * 256 + fq * 16 + hf * 8;
        {
            const int ek = hf ? (pw >> 4) & 7 : x8, ej = hf ? 4 * (pw & 7) + 2 * (pw >> 7) + ((pw >> 3) & 1) : pi;
            GAS u64* ev = (GAS u64*)(p.ws + WS_QG) + ek * 1024 + 16 * ej;
            GAS u64* hs = do_m2 ? (GAS u64*)(p.ws + WS_HG) + 64 * hi + lane : ev;
            const int hstr = do_m2 ? FF : 0;
            u64 e, h0, h1;
            if (!do_m1) { __builtin_amdgcn_s_sleep(64); __builtin_amdgcn_s_sleep(64); }
            __builtin_amdgcn_s_sleep(64);
#pragma unroll 1
            for (u32 spin = 0; spin < SPIN_CAP; ++spin) {
                e = __hip_atomic_load(ev, RLX_AGENT); h0 = __hip_atomic_load(hs, RLX_AGENT); h1 = __hip_atomic_load(hs + hstr, RLX_AGENT);
                if (__all(((e >> 16) == tag) & ((h0 >> 16) == tag) & ((h1 >> 16) == tag))) break;
                __builtin_amdgcn_s_sleep(64);
            }
            *(LAS u16*)(L + L_H + lane * 2) = (u16)h0;
            *(LAS u16*)(L + L_H + 128 + lane * 2) = (u16)h1;
        }
        if (do_m2) {
#pragma unroll
            for (int cb = 0; cb < 4; ++cb) {
                f32x4 mp = {0.f, 0.f, 0.f, 0.f};
#pragma unroll
                for (int u = 0; u < 2; ++u) mp = mfma32(tr_frag(L + L_W2 + 32 * cb, 128, 32 * u, lane), *(const LAS v4u*)(L + L_H + (n & 1) * 128 + 64 * u + 16 * g), mp);
                __builtin_amdgcn_raw_buffer_store_b128(clean4(mp), rws, n < 2 ? myslot + SL_PM + n * 256 + cb * 64 + g * 16 : OOR, 0, AUX_SC1);
            }
        }
        float gate[2];
        {
            v2u a;
            __builtin_amdgcn_s_sleep(64);
#pragma unroll 1
            for (u32 spin = 0; spin < SPIN_CAP; ++spin) {
                asm volatile("" ::: "memory");
                a = __builtin_amdgcn_raw_buffer_load_b64(rws, pi < 28 ? pslot + SL_PM : OOR, 0, AUX_SC1);
                if (__all(max(a.x, a.y) != SENT)) break;
                __builtin_amdgcn_s_sleep(64);
            }
#pragma unroll
            for (int k = 0; k < 2; ++k) {
                const float mm = rbf(rbf(x16_sum(row_sum(__uint_as_float(a[k])))) + (k ? bf_hi(b2o) : bf_lo(b2o)));
                gate[k] = rbf(mm / (1.f + __expf(-mm)));
            }
        }
        __builtin_amdgcn_s_setprio(3);
        const u32 fme = lane == 0 ? myslot + SL_FA : OOR;
        const u32 fao = lane < 32 ? WS_SLOT + (((lane >> 4) * 8 + x8) * 16 + (lane & 15)) * SLOT + SL_FA : OOR;
        const u32 fbo = lane < 32 ? WS_SLOT + pw * SLOT + SL_FB : OOR;
#define WAIT_WORDS(o)                                                                                          \
    _Pragma("unroll 1") for (u32 spin = 0; spin < SPIN_CAP; ++spin) {                                          \
        asm volatile("" ::: "memory");                                                                         \
        if (__all(__builtin_amdgcn_raw_buffer_load_b32(rws, o, 0, AUX_SC1) != SENT)) break;                    \
        __builtin_amdgcn_s_sleep(1);                                                                           \
    }
#pragma unroll 1
        for (u32 spin = 0; spin < 16 * SPIN_CAP; ++spin) {
            if (__hip_atomic_load((LAS u32*)(L + L_CNT), __ATOMIC_RELAXED, __HIP_MEMORY_SCOPE_WORKGROUP) >= NSW) break;
            __builtin_amdgcn_s_sleep(1);
        }
        __builtin_amdgcn_raw_buffer_store_b32(1u, rws, fme, 0, AUX_SC1);
        wg_barrier();
        WAIT_WORDS(fao);
        wg_barrier();
        __builtin_amdgcn_s_sleep(8);
        __builtin_amdgcn_raw_buffer_store_b32(1u, rws, fme + (SL_FB - SL_FA), 0, AUX_SC1);
        wg_barrier();
        WAIT_WORDS(fbo);
#undef WAIT_WORDS
        {
            v2u a;
#pragma unroll 1
            for (u32 spin = 0; spin < SPIN_CAP; ++spin) {
                asm volatile("" ::: "memory");
                a = __builtin_amdgcn_raw_buffer_load_b64(rws, pslot + SL_PS, 0, AUX_SC1);
                if (__all(max(a.x, a.y) != SENT)) break;
            }
            const float s0 = rbf(x16_sum(row_sum(__uint_as_float(a.x)))), s1 = rbf(x16_sum(row_sum(__uint_as_float(a.y))));
            const u32 r = pk_bf16(bf_lo(xo) + rbf(s0 * gate[0]), bf_hi(xo) + rbf(s1 * gate[1]));
            if (pi == 0) *(GAS u32*)(p.out + fb * DM + fc) = r;
            if (bid == 0 && lane == 0) __hip_atomic_store((GAS u64*)(p.ws + WS_EPOCH), (((u64)ep.y << 32) | ep.x) + 1, RLX_AGENT);
        }
    } else {
        const u16* kb = p.k + (size_t)pair * SK * HD;
        const u16* vb = p.v + (size_t)pair * SK * HD;
        const __amdgpu_buffer_rsrc_t rk = mk_rsrc(kb, SK * HD * 2), rv = mk_rsrc(vb, SK * HD * 2);
        constexpr u32 TSTR = NSW * 4096;
        const u32 kl = (n >> 2) * 256 + g * 64 + (n & 3) * 16, vl = lane * 16;
        u32 ko = (s * NS * NSW + w) * 4096 + kl, vo = (s * NS * NSW + w) * 4096 + vl;
        v4u R[3][4], KX[RP][4];
        u64 stub0, stub;
        asm volatile("s_getpc_b64 %0\n\ts_branch 1f\n\t"
                     "s_waitcnt vmcnt(3)\n\ts_setpc_b64 s[80:81]\n\ts_waitcnt vmcnt(4)\n\ts_setpc_b64 s[80:81]\n\ts_waitcnt vmcnt(5)\n\ts_setpc_b64 s[80:81]\n\t"
                     "s_waitcnt vmcnt(6)\n\ts_setpc_b64 s[80:81]\n\ts_waitcnt vmcnt(7)\n\ts_setpc_b64 s[80:81]\n1:" : "=s"(stub0));
        stub0 -= 28;
        stub = stub0 + 8 * 6;
        u32 due = t_start * 16 + T_OFF;
#define WAITW() asm volatile("s_swappc_b64 s[80:81], %0" ::"s"(stub) : "memory", "s80", "s81")
#define CLOCK() const u32 now_ = (u32)__builtin_amdgcn_s_memrealtime(); __builtin_amdgcn_sched_barrier(0)
#define PACE(tau)                                                  \
    do {                                                                                                       \
        due += (tau);                                                                                          \
        const int win = __builtin_amdgcn_readfirstlane(min(max(6 + (((int)(now_ * 16 - due) + 512) >> 10), 4), 8)); \
        stub = stub0 + 8 * win;                                                                                \
        __builtin_amdgcn_sched_barrier(0);                                                                     \
    } while (0)
#define LK(X, u) do { WAITW(); X[u] = __builtin_amdgcn_raw_buffer_load_b128(rk, ko + 1024 * (u), 0, AUX_NT); __builtin_amdgcn_sched_barrier(0); } while (0)
#define LV(X, u) do { WAITW(); X[u] = __builtin_amdgcn_raw_buffer_load_b128(rv, vo + 1024 * (u), 0, AUX_NT); __builtin_amdgcn_sched_barrier(0); } while (0)
#define LK4(X) do { LK(X, 0); LK(X, 1); LK(X, 2); LK(X, 3); ko += TSTR; } while (0)
#define LV4(X) do { LV(X, 0); LV(X, 1); LV(X, 2); LV(X, 3); vo += TSTR; } while (0)
        const u32 park = lb + L_PARK + w * LP * 4096;
#pragma unroll
        for (int t = 0; t < LP; ++t) {
            const unsigned char* ks = (const unsigned char*)kb + ko;
            WAITW(); glds_nt_off<0>(ks, park + t * 4096);
            WAITW(); glds_nt_off<1024>(ks, park + t * 4096);
            WAITW(); glds_nt_off<2048>(ks, park + t * 4096);
            WAITW(); glds_nt_off<3072>(ks, park + t * 4096);
            ko += TSTR;
            CLOCK(); PACE(T_K);
        }
#pragma unroll
        for (int t = 0; t < RP; ++t) { LK4(KX[t]); CLOCK(); PACE(T_K); }
#pragma unroll
        for (int t = 0; t < 2; ++t) { LK4(R[t]); CLOCK(); PACE(T_K); }
        wg_barrier();
        due += (__builtin_amdgcn_readfirstlane(*(const LAS u32*)(L + L_CNT + 4)) - t_start) * 16;
        const int jj = n & 3;
        const bool j0 = jj & 1, j1 = jj & 2;
        const v4u Q = *(const LAS v4u*)(L + L_Q + (n >> 2) * 256 + 64 * g + 16 * jj);
        const u32 mask0 = jj == 0 ? 0x0000ffffu : jj == 1 ? 0xffff0000u : 0u, mask1 = jj == 2 ? 0x0000ffffu : jj == 3 ? 0xffff0000u : 0u;
        float m = -INFINITY, l = 0.f;
        f32x4 acc0 = {0.f, 0.f, 0.f, 0.f}, acc1 = {0.f, 0.f, 0.f, 0.f};
        LAS float* scp = (LAS float*)(L + L_SC + w * 8192) + lane;
#define KP(KR, idx)                                                                                            \
    do {                                                                                                       \
        f32x4 dd[4]; float sc[4];                                                                              \
        _Pragma("unroll") for (int u = 0; u < 4; ++u) dd[u] = mfma32(KR[u], Q, f32x4{0.f, 0.f, 0.f, 0.f});     \
        asm volatile("" : "+v"(dd[0]), "+v"(dd[1]), "+v"(dd[2]), "+v"(dd[3]));     \
        _Pragma("unroll") for (int u = 0; u < 4; ++u) sc[u] = sel4(dd[u], jj);                                 \
        const float y01 = (j0 ? sc[1] : sc[0]) + dpp<XOR1>(j0 ? sc[0] : sc[1]), y23 = (j0 ? sc[3] : sc[2]) + dpp<XOR1>(j0 ? sc[2] : sc[3]); \
        const float y = (j1 ? y23 : y01) + dpp<XOR2>(j1 ? y01 : y23);                                          \
        m = fmaxf(m, y);                                                                                       \
        scp[(idx) * 64] = y;                                                                                   \
        __builtin_amdgcn_sched_barrier(0);                                                                     \
    } while (0)
#define UNPARK(t, KR)                                                                                          \
    do {                                                                                                       \
        const LAS unsigned char* pk = L + L_PARK + (w * LP + (t)) * 4096 + lane * 16;                          \
        _Pragma("unroll") for (int u = 0; u < 4; ++u) KR[u] = *(const LAS v4u*)(pk + 1024 * u);                \
    } while (0)
#define VP(VR, sv)                                                                                             \
    do {                                                                                                       \
        const float p1 = ex2(fmaf(sv, SC, -mc));                                                               \
        l += p1;                                                                                               \
        const int pb = pk_bf16(p1, p1);                                                                        \
        _Pragma("unroll") for (int r = 0; r < 4; ++r) {                                                        \
            const u32 t_ = r == 0 ? __builtin_amdgcn_mov_dpp(pb, 0x00, 0xf, 0xf, true) : r == 1 ? __builtin_amdgcn_mov_dpp(pb, 0x55, 0xf, 0xf, true) \
                         : r == 2 ? __builtin_amdgcn_mov_dpp(pb, 0xAA, 0xf, 0xf, true) : __builtin_amdgcn_mov_dpp(pb, 0xFF, 0xf, 0xf, true); \
            acc0 = mfma16(VR[r].x, VR[r].y, t_ & mask0, t_ & mask1, acc0);                                     \
            acc1 = mfma16(VR[r].z, VR[r].w, t_ & mask0, t_ & mask1, acc1);                                     \
        }                                                                                                      \
        __builtin_amdgcn_sched_barrier(0);                                                                     \
    } while (0)
#define UKK(u, idx) do { LK4(R[((u) + 2) % 3]); CLOCK(); KP(R[(u) % 3], idx); PACE(T_K); } while (0)
#define UVK(u, idx) do { LV4(R[((u) + 2) % 3]); CLOCK(); KP(R[(u) % 3], idx); PACE(T_V); } while (0)
#define UVV(u, idx) do { float sv_ = scp[(idx) * 64]; LV4(R[((u) + 2) % 3]); asm volatile("" : "+v"(sv_)); CLOCK(); VP(R[(u) % 3], sv_); PACE(T_V); } while (0)
#define UKB(u, XA, ia, XB, ib, PRE)                                                                            \
    do {                                                                                                       \
        LK(R[((u) + 2) % 3], 0); PRE; KP(XA, ia); LK(R[((u) + 2) % 3], 1); LK(R[((u) + 2) % 3], 2); KP(XB, ib); LK(R[((u) + 2) % 3], 3); ko += TSTR; \
        CLOCK(); KP(R[(u) % 3], 12 + (u)); PACE(T_K);                                                          \
    } while (0)
        static_assert(LP == 4 && RP == 8 && NBL == 6, "");
        UKB(0, KX[0], 4, KX[1], 5, (void)0);
        UKB(1, KX[2], 6, KX[3], 7, { UNPARK(0, KX[0]); UNPARK(1, KX[1]); });
        UKB(2, KX[0], 0, KX[1], 1, { UNPARK(2, KX[2]); UNPARK(3, KX[3]); });
        UKB(3, KX[2], 2, KX[3], 3, (void)0);
        UKB(4, KX[4], 8, KX[5], 9, (void)0);
        UKB(5, KX[6], 10, KX[7], 11, (void)0);
        scp += (12 + NBL) * 64;
#pragma unroll 1
        for (int it = 0; it < KA; ++it) { UKK(0, 0); UKK(1, 1); UKK(2, 2); scp += 3 * 64; }
        UVK(0, 0); UVK(1, 1);
        m = fmaxf(m, dpp<XOR1>(m)); m = fmaxf(m, dpp<XOR2>(m)); m = x32_max(x16_max(m));
        const float mc = m * SC;
        const int bb = w & 1, dh = w >> 1;
        u32 cnt_a = lb + L_CNT, cnt_v = lane == 0;
        u32 a_mw = L_MRG + ((w * 4 + (n >> 2)) * 132 + 32 * g + jj) * 4, a_lw = L_MML + (w * 4 + (n >> 2)) * 8;
        u32 a_mr = L_MRG + (w * 132 + 4 * (lane & 31)) * 4, a_lr = L_MML + w * 8;
        u32 o_pa = lane < 32 ? myslot + w * 512 + lane * 16 : OOR, o_pl = lane == 0 ? WS_ML + ((pair * 4 + w) * 16 + s) * 8 : OOR;
        u32 o_ga = WS_SLOT + ((bb * 8 + x8) * 16 + 4 * g) * SLOT + (hi & 3) * 512 + dh * 256 + n * 16;
        u32 o_gl = WS_ML + (((bb * 8 + x8) * 4 + (hi & 3)) * 16 + ((n + 4 * g) & 15)) * 8;
        u32 a_wo = L_WO + 32 * w + (8 * g + ((lane >> 2) & 3)) * 128 + 8 * jj, a_ow = L_O + bb * 256 + dh * 128 + n * 8 + g * 2;
        u32 a_or = L_O + (n & 1) * 256 + 16 * g, o_ps = n < 2 ? myslot + SL_PS + n * 256 + w * 64 + g * 16 : OOR;
        asm volatile("" : "+v"(cnt_a), "+v"(cnt_v), "+v"(a_mw), "+v"(a_lw), "+v"(a_mr), "+v"(a_lr), "+v"(o_pa), "+v"(o_pl));
        asm volatile("" : "+v"(o_ga), "+v"(o_gl), "+v"(a_wo), "+v"(a_ow), "+v"(a_or), "+v"(o_ps));
        scp -= (NS - 2) * 64;
#pragma unroll 1
        for (int it = 0; it < VA; ++it) { UVV(2, 0); UVV(3, 1); UVV(4, 2); scp += 3 * 64; }
        UVV(2, 0); UVV(3, 1);
        u32 ord = 0;
        asm volatile("ds_add_rtn_u32 %0, %1, %2" : "+v"(ord) : "v"(cnt_a), "v"(cnt_v) : "memory");
        {
            const float s0 = scp[2 * 64], s1 = scp[3 * 64], s2 = scp[4 * 64];
            LV4(R[0]);
            VP(R[1], s0); VP(R[2], s1); VP(R[0], s2);
            l += dpp<XOR1>(l); l += dpp<XOR2>(l);
        }
        asm volatile("s_waitcnt lgkmcnt(0)" : "+v"(ord)::"memory");
        const u32 ord_s = __builtin_amdgcn_readfirstlane(ord);
        {
            LAS float* mr = (LAS float*)(L + a_mw);
#pragma unroll
            for (int r = 0; r < 4; ++r) { mr[8 * r] = acc0[r]; mr[8 * r + 4] = acc1[r]; }
            *(LAS f32x2*)(L + a_lw) = f32x2{mc, x32_sum(x16_sum(l))};
        }
        if (ord_s == 0) {
            const u16* so = p.wo + (size_t)(hi * 128 + (lane >> 3)) * DM + 64 * cg + 8 * (lane & 7);
#pragma unroll
            for (int u = 0; u < 16; ++u) glds_nt(so + (size_t)u * 8 * DM, lb + L_WO + u * 1024);
        }
        wg_barrier();
        {
            f32x2 ml[NSW]; f32x4 ov[NSW];
#pragma unroll
            for (int u = 0; u < NSW; ++u) ml[u] = *(const LAS f32x2*)(L + a_lr + u * 32);
#pragma unroll
            for (int u = 0; u < NSW; ++u) ov[u] = *(const LAS f32x4*)(L + a_mr + u * (4 * 132 * 4));
            const float M = fmaxf(fmaxf(ml[0].x, ml[1].x), fmaxf(ml[2].x, ml[3].x));
            float f[NSW];
#pragma unroll
            for (int u = 0; u < NSW; ++u) f[u] = ex2(ml[u].x - M);
            const f32x4 o = (f[0] * ov[0] + f[1] * ov[1]) + (f[2] * ov[2] + f[3] * ov[3]);
            __builtin_amdgcn_raw_buffer_store_b128(clean4(o), rws, o_pa, 0, AUX_SC1);
            const float Ls = fmaf(f[0], ml[0].y, f[1] * ml[1].y) + fmaf(f[2], ml[2].y, f[3] * ml[3].y);
            __builtin_amdgcn_raw_buffer_store_b64(v2u{no_sent(M), no_sent(Ls)}, rws, o_pl, 0, AUX_SC1);
        }
        v4u WO[4];
        {
            v4u a[4]; v2u q2;
#define GATHER()                                                                                               \
    do {                                                                                                       \
        q2 = __builtin_amdgcn_raw_buffer_load_b64(rws, o_gl, 0, AUX_SC1);                                      \
        _Pragma("unroll") for (int k = 0; k < 4; ++k) a[k] = __builtin_amdgcn_raw_buffer_load_b128(rws, o_ga, k * SLOT, AUX_SC1); \
    } while (0)
#define ARRIVED() __all(max(max(max(umax4(a[0]), umax4(a[1])), max(umax4(a[2]), umax4(a[3]))), max(q2.x, q2.y)) != SENT)
            wait_vm<2>();
            wg_barrier();
            GATHER();
            __builtin_amdgcn_sched_barrier(0);
#pragma unroll
            for (int u = 0; u < 4; ++u) {
                const v2u lo = tr_read(L + a_wo + 4096 * u), hi2 = tr_read(L + a_wo + 4096 * u + 512);
                WO[u] = v4u{lo.x, lo.y, hi2.x, hi2.y};
            }
            __builtin_amdgcn_sched_barrier(0);
            if (!ARRIVED()) {
#pragma unroll 1
                for (u32 spin = 0; spin < SPIN_CAP; ++spin) {
                    asm volatile("" ::: "memory");
                    GATHER();
                    if (ARRIVED()) break;
                }
            }
            const float mp = __uint_as_float(q2.x);
            const float M = row_max(mp);
            const float wt = ex2(mp - M);
            const float inv = __builtin_amdgcn_rcpf(row_sum(wt * __uint_as_float(q2.y)));
            const f32x4 o = (dpp<0x150>(wt) * __builtin_bit_cast(f32x4, a[0]) + dpp<0x151>(wt) * __builtin_bit_cast(f32x4, a[1])) +
                            (dpp<0x152>(wt) * __builtin_bit_cast(f32x4, a[2]) + dpp<0x153>(wt) * __builtin_bit_cast(f32x4, a[3]));
            const auto s02 = __builtin_amdgcn_permlane32_swap(__float_as_uint(o[0]), __float_as_uint(o[2]), false, false);
            const auto s13 = __builtin_amdgcn_permlane32_swap(__float_as_uint(o[1]), __float_as_uint(o[3]), false, false);
            const float xs = __uint_as_float(s02[0]) + __uint_as_float(s02[1]), ys = __uint_as_float(s13[0]) + __uint_as_float(s13[1]);
            const auto sz = __builtin_amdgcn_permlane16_swap(__float_as_uint(xs), __float_as_uint(ys), false, false);
            const float z = (__uint_as_float(sz[0]) + __uint_as_float(sz[1])) * inv;
            *(LAS u16*)(L + a_ow) = (u16)pk_bf16(z, z);
        }
        wg_barrier();
        {
            v4u ob[4];
#pragma unroll
            for (int u = 0; u < 4; ++u) ob[u] = *(const LAS v4u*)(L + a_or + 64 * u);
            __builtin_amdgcn_sched_barrier(0);
            const f32x4 z = {0.f, 0.f, 0.f, 0.f};
            const f32x4 sp = mfma32(WO[2], ob[2], mfma32(WO[0], ob[0], z)) + mfma32(WO[3], ob[3], mfma32(WO[1], ob[1], z));
            __builtin_amdgcn_raw_buffer_store_b128(clean4(sp), rws, o_ps, 0, AUX_SC1);
        }
    }
}

extern "C" void kernel_launch(void* const* d_in, const int* in_sizes, int n_in, void* d_out, int out_size, void* d_ws, size_t ws_size,
                              hipStream_t stream) {
    Params p;
    p.x = (const u16*)d_in[0]; p.k = (const u16*)d_in[1]; p.v = (const u16*)d_in[2]; p.wq = (const u16*)d_in[3]; p.wo = (const u16*)d_in[4];
    p.w1 = (const u16*)d_in[6]; p.b1 = (const u16*)d_in[7]; p.w2 = (const u16*)d_in[8]; p.b2 = (const u16*)d_in[9];
    p.out = (u16*)d_out; p.ws = (unsigned char*)d_ws;
    (void)hipFuncSetAttribute((const void*)fused_decode, hipFuncAttributeMaxDynamicSharedMemorySize, L_END);
    hipLaunchKernelGGL(fused_decode, dim3(NWG), dim3(NTHR), L_END, stream, p);
}
```

```cpp
#include <hip/hip_runtime.h>
#include <hip/hip_bf16.h>
#include <stdint.h>

#define GAS __attribute__((address_space(1)))
#define LAS __attribute__((address_space(3)))
typedef unsigned u32;
typedef unsigned long long u64;
typedef unsigned short u16;
typedef unsigned v4u __attribute__((ext_vector_type(4)));
typedef unsigned v2u __attribute__((ext_vector_type(2)));
typedef float f32x4 __attribute__((ext_vector_type(4)));
typedef float f32x2 __attribute__((ext_vector_type(2)));
typedef short s16x4 __attribute__((ext_vector_type(4)));
typedef __bf16 bf16x8 __attribute__((ext_vector_type(8)));
typedef __bf16 bf16x2 __attribute__((ext_vector_type(2)));

constexpr int DM = 512, HD = 128, SK = 32768, FF = 1792;
constexpr int NWG = 256, NSW = 4, NTHR = (NSW + 1) * 64;
constexpr int NS = SK / 16 / 16 / NSW;
constexpr int LP = 4, RP = 8, NPRO = LP + RP + 2;
constexpr int NBL = 6, KA = (NS - NPRO - NBL) / 3, VA = (NS - 2) / 3 - 1;
static_assert(NS == 32 && NPRO + NBL + 3 * KA == NS && 3 * VA + 5 == NS, "");
constexpr int T_OFF = 130 * 16, T_K = 922, T_V = 904;
constexpr int WQ = 8, WW1 = 2, WW2 = 2;
constexpr float SC = 0.08838834764831845f * 1.4426950408889634f;
constexpr u32 SENT = 0xFFFFFFFFu;
constexpr u32 SPIN_CAP = 1u << 21;
constexpr u32 OOR = 0xFFFFFF00u;

constexpr u32 WS_EPOCH = 0;
constexpr u32 WS_QG = 4096;
constexpr u32 WS_HG = WS_QG + 8 * 2 * 512 * 8;
constexpr u32 WS_TG = 102400;
constexpr u32 WS_ML = 118784;
constexpr u32 WS_SLOT = 131072, SLOT = 4352;
constexpr u32 SL_PS = 2048, SL_PM = 2560, SL_FA = 3072, SL_FB = 3200;
constexpr u32 WS_END = WS_SLOT + NWG * SLOT;
static_assert(WS_HG + 2 * FF * 8 <= WS_TG && WS_TG + 256 * 8 <= WS_ML && WS_ML + 8192 <= WS_SLOT, "");

constexpr u32 L_WQ = 0, L_W1 = 0, L_WO = 16384, L_W2 = 32768;
constexpr u32 L_XN = 40960, L_Y = L_XN + 2048, L_Q = L_Y + 2048, L_H = L_Q + 1024, L_O = L_H + 256;
constexpr u32 L_MRG = L_O + 512;
constexpr u32 L_MML = L_MRG + NSW * 4 * 132 * 4;
constexpr u32 L_CNT = L_MML + NSW * 4 * 2 * 4;
constexpr u32 L_SC = 57344;
constexpr u32 L_PARK = L_SC + NSW * 8192;
constexpr u32 L_END = L_PARK + NSW * LP * 4096;
static_assert(L_CNT + 16 <= L_SC && L_END > 80 * 1024 && L_END <= 160 * 1024, "more than half of a CU's LDS: one workgroup per CU");

struct Params {
    const u16 *x, *k, *v, *wq, *wo, *w1, *b1, *w2, *b2;
    u16* out;
    unsigned char* ws;
};

__device__ __forceinline__ f32x4 mfma32(v4u a, v4u b, f32x4 c) {
    return __builtin_amdgcn_mfma_f32_16x16x32_bf16(__builtin_bit_cast(bf16x8, a), __builtin_bit_cast(bf16x8, b), c, 0, 0, 0);
}
__device__ __forceinline__ f32x4 mfma16(u32 a0, u32 a1, u32 b0, u32 b1, f32x4 c) {
    return __builtin_amdgcn_mfma_f32_16x16x16bf16_1k(__builtin_bit_cast(s16x4, v2u{a0, a1}), __builtin_bit_cast(s16x4, v2u{b0, b1}), c, 0, 0, 0);
}
__device__ __forceinline__ u32 pk_bf16(float lo, float hi) {
    return __builtin_bit_cast(u32, __builtin_convertvector(f32x2{lo, hi}, bf16x2));
}
__device__ __forceinline__ float bf_lo(u32 w) { return __builtin_bit_cast(float, w << 16); }
__device__ __forceinline__ float bf_hi(u32 w) { return __builtin_bit_cast(float, w & 0xffff0000u); }
__device__ __forceinline__ float rbf(float f) { return bf_lo(pk_bf16(f, f)); }
__device__ __forceinline__ float ex2(float x) { return __builtin_amdgcn_exp2f(x); }
template <int CTRL> __device__ __forceinline__ float dpp(float x) {
    return __builtin_bit_cast(float, __builtin_amdgcn_mov_dpp(__builtin_bit_cast(int, x), CTRL, 0xf, 0xf, true));
}
constexpr int XOR1 = 0xB1, XOR2 = 0x4E, XOR7 = 0x141, ROR8 = 0x128;
__device__ __forceinline__ float x16_max(float x) {
    auto s = __builtin_amdgcn_permlane16_swap(__float_as_uint(x), __float_as_uint(x), false, false);
    return fmaxf(__uint_as_float(s[0]), __uint_as_float(s[1]));
}
__device__ __forceinline__ float x32_max(float x) {
    auto s = __builtin_amdgcn_permlane32_swap(__float_as_uint(x), __float_as_uint(x), false, false);
    return fmaxf(__uint_as_float(s[0]), __uint_as_float(s[1]));
}
__device__ __forceinline__ float x16_sum(float x) {
    auto s = __builtin_amdgcn_permlane16_swap(__float_as_uint(x), __float_as_uint(x), false, false);
    return __uint_as_float(s[0]) + __uint_as_float(s[1]);
}
__device__ __forceinline__ float x32_sum(float x) {
    auto s = __builtin_amdgcn_permlane32_swap(__float_as_uint(x), __float_as_uint(x), false, false);
    return __uint_as_float(s[0]) + __uint_as_float(s[1]);
}
__device__ __forceinline__ float row_sum(float x) {
    x += dpp<XOR1>(x); x += dpp<XOR2>(x); x += dpp<XOR7>(x); x += dpp<ROR8>(x); return x;
}
__device__ __forceinline__ float row_max(float x) {
    float r;
    asm("s_nop 1\n\tv_max_f32_dpp %0, %1, %1 quad_perm:[1,0,3,2] row_mask:0xf bank_mask:0xf\n\ts_nop 1\n\t"
        "v_max_f32_dpp %0, %0, %0 quad_perm:[2,3,0,1] row_mask:0xf bank_mask:0xf\n\ts_nop 1\n\t"
        "v_max_f32_dpp %0, %0, %0 row_half_mirror row_mask:0xf bank_mask:0xf\n\ts_nop 1\n\t"
        "v_max_f32_dpp %0, %0, %0 row_ror:8 row_mask:0xf bank_mask:0xf" : "=&v"(r) : "v"(x));
    return r;
}
__device__ __forceinline__ float wave_sum(float x) { return x32_sum(x16_sum(row_sum(x))); }
__device__ __forceinline__ u32 umax4(v4u a) { return max(max(a.x, a.y), max(a.z, a.w)); }
__device__ __forceinline__ u32 no_sent(float f) { return min(__float_as_uint(f), 0xFFFFFFFEu); }

#define RLX_AGENT __ATOMIC_RELAXED, __HIP_MEMORY_SCOPE_AGENT
constexpr int AUX_NT = 2, AUX_SC1 = 16;
__device__ __forceinline__ __amdgpu_buffer_rsrc_t mk_rsrc(const void* p, u32 bytes) {
    return __builtin_amdgcn_make_buffer_rsrc((void*)p, 0, bytes, 0x00020000);
}
__device__ __forceinline__ void glds_nt(const void* src, u32 dst) {
    asm volatile("s_mov_b32 m0, %1\n\ts_nop 0\n\tglobal_load_lds_dwordx4 %0, off nt" ::"v"(src), "s"(dst) : "memory");
}
__device__ __forceinline__ void glds_sh(const void* src, u32 dst) {
    asm volatile("s_mov_b32 m0, %1\n\ts_nop 0\n\tglobal_load_lds_dwordx4 %0, off" ::"v"(src), "s"(dst) : "memory");
}
template <int OFF> __device__ __forceinline__ void glds_nt_off(const void* src, u32 dst) {
    asm volatile("s_mov_b32 m0, %1\n\ts_nop 0\n\tglobal_load_lds_dwordx4 %0, off offset:%2 nt" ::"v"(src), "s"(dst), "i"(OFF) : "memory");
}
template <int N> __device__ __forceinline__ void wait_vm() { asm volatile("s_waitcnt vmcnt(%0)" ::"i"(N) : "memory"); }
__device__ __forceinline__ void wg_barrier() { asm volatile("s_waitcnt lgkmcnt(0)\n\ts_barrier" ::: "memory"); }
__device__ __forceinline__ v2u tr_read(const LAS unsigned char* p) {
    return __builtin_bit_cast(v2u, __builtin_amdgcn_ds_read_tr16_b64_v4i16((LAS s16x4*)p));
}
__device__ __forceinline__ v4u tr_frag(const LAS unsigned char* img, int pitch, int row0, int lane) {
    const int g = lane >> 4, q = (lane >> 2) & 3, p = lane & 3;
    const LAS unsigned char* a = img + (row0 + 8 * g + q) * pitch + 8 * p;
    const v2u lo = tr_read(a), hi = tr_read(a + 4 * pitch);
    return v4u{lo.x, lo.y, hi.x, hi.y};
}

__device__ __forceinline__ void load_act(v4u (&B)[16], const LAS unsigned char* act, int lane) {
    const LAS unsigned char* ap = act + (lane & 1) * 1024 + (lane >> 4) * 16;
#pragma unroll
    for (int s = 0; s < 16; ++s) B[s] = *(const LAS v4u*)(ap + 64 * s);
}
__device__ __forceinline__ f32x4 gemv16(const LAS unsigned char* img, const v4u (&B)[16], int lane) {
    v4u A[16];
#pragma unroll
    for (int s = 0; s < 16; ++s) A[s] = tr_frag(img, 32, 32 * s, lane);
    __builtin_amdgcn_sched_barrier(0);
    f32x4 a[4];
#pragma unroll
    for (int s = 0; s < 16; ++s) a[s & 3] = mfma32(A[s], B[s], s < 4 ? f32x4{0.f, 0.f, 0.f, 0.f} : a[s & 3]);
    return (a[0] + a[1]) + (a[2] + a[3]);
}
__device__ __forceinline__ void layer_norm(float (&v)[2][8], LAS unsigned char* dst, int lane) {
    float mean[2], rs[2];
#pragma unroll
    for (int bb = 0; bb < 2; ++bb) mean[bb] = wave_sum(((v[bb][0] + v[bb][1]) + (v[bb][2] + v[bb][3])) + ((v[bb][4] + v[bb][5]) + (v[bb][6] + v[bb][7]))) * (1.f / DM);
#pragma unroll
    for (int bb = 0; bb < 2; ++bb) {
        float sq = 0.f;
#pragma unroll
        for (int u = 0; u < 8; ++u) { v[bb][u] -= mean[bb]; sq = fmaf(v[bb][u], v[bb][u], sq); }
        rs[bb] = __builtin_amdgcn_rsqf(wave_sum(sq) * (1.f / DM) + 1e-5f);
    }
#pragma unroll
    for (int bb = 0; bb < 2; ++bb) {
        v4u o;
#pragma unroll
        for (int u = 0; u < 4; ++u) {
            o[u] = pk_bf16(v[bb][2 * u] * rs[bb], v[bb][2 * u + 1] * rs[bb]);
            v[bb][2 * u] = bf_lo(o[u]); v[bb][2 * u + 1] = bf_hi(o[u]);
        }
        *(LAS v4u*)(dst + bb * 1024 + lane * 16) = o;
    }
}
__device__ __forceinline__ float sel4(f32x4 v, int r) {
    const bool b0 = r & 1, b1 = r & 2;
    const float lo = b0 ? v[1] : v[0], hi = b0 ? v[3] : v[2];
    return b1 ? hi : lo;
}
__device__ __forceinline__ v4u clean4(f32x4 v) { return v4u{no_sent(v[0]), no_sent(v[1]), no_sent(v[2]), no_sent(v[3])}; }

__global__ void __launch_bounds__(NTHR, 2) fused_decode(Params p) {
    extern __shared__ __attribute__((aligned(16))) unsigned char lds_raw[];
    LAS unsigned char* L = (LAS unsigned char*)lds_raw;
    const u32 lb = (u32)(size_t)L;
    const int tid = threadIdx.x, lane = tid & 63, w = __builtin_amdgcn_readfirstlane(tid >> 6);
    const int bid = blockIdx.x;
    const int pair = bid >> 4, b = pair >> 3, kvh = pair & 7, s = bid & 15;
    const int jq = 4 * (s & 7) + 2 * b + (s >> 3);
    const int m1i = (s - kvh) & 7, jm = 4 * (7 * (kvh >> 1) + 4 * (kvh & 1) + m1i) + (jq & 3);
    const int x8 = bid & 7, hi = 4 * x8 + (bid >> 6), cg = (bid >> 3) & 7;
    const bool do_m1 = m1i < 4 - (kvh & 1), do_m2 = hi < 28;
    const int g = lane >> 4, n = lane & 15;
    const __amdgpu_buffer_rsrc_t rws = mk_rsrc(p.ws, WS_END);
    const u32 myslot = WS_SLOT + bid * SLOT;

    const u32 t_start = (u32)__builtin_amdgcn_s_memrealtime();
    asm volatile("" ::"s"(p.k), "s"(p.v), "s"(p.x), "s"(p.wq), "s"(p.ws));
    if (w >= NSW) {
        __builtin_amdgcn_s_setprio(3);
        v4u xr0, xr1; v2u ep;
        {
            const void* xa = p.x + 8 * lane;
            const void* ea = p.ws + WS_EPOCH;
            asm volatile("global_load_dwordx4 %0, %3, off\n\tglobal_load_dwordx4 %1, %3, off offset:1024\n\tglobal_load_dwordx2 %2, %4, off sc1"
                         : "=&v"(xr0), "=&v"(xr1), "=&v"(ep) : "v"(xa), "v"(ea) : "memory");
        }
        {
            const v4u sv = {SENT, SENT, SENT, SENT};
#pragma unroll
            for (int u = 0; u < 3; ++u) __builtin_amdgcn_raw_buffer_store_b128(sv, rws, myslot + u * 1024 + lane * 16, 0, AUX_SC1);
            const u32 o4 = lane < 4 ? WS_ML + ((pair * 4 + lane) * 16 + s) * 8 : lane < 6 ? myslot + SL_FA + (lane - 4) * 128 : OOR;
            __builtin_amdgcn_raw_buffer_store_b64(v2u{SENT, SENT}, rws, o4, 0, AUX_SC1);
        }
        const int rot = 4 * (jq & 3);
        const u16* wqs = p.wq + (size_t)(lane >> 1) * 4096 + kvh * 512 + 16 * jq + 8 * (lane & 1);
#pragma unroll
        for (int u = 0; u < WQ; ++u) { const int ur = (u + rot) & 15; glds_sh(wqs + (size_t)ur * 32 * 4096, lb + L_WQ + ur * 1024); }
        asm volatile("s_waitcnt vmcnt(%3)" : "+v"(xr0), "+v"(xr1), "+v"(ep) : "i"(4 + WQ) : "memory");
#pragma unroll
        for (int u = WQ; u < 16; ++u) { const int ur = (u + rot) & 15; wait_vm<WQ - 1>(); glds_sh(wqs + (size_t)ur * 32 * 4096, lb + L_WQ + ur * 1024); }
        const u64 tag = ((((u64)ep.y << 32) | ep.x) + 1) & 0xFFFFFFFFFFFFull;
        float xv[2][8];
#pragma unroll
        for (int u = 0; u < 4; ++u) { xv[0][2 * u] = bf_lo(xr0[u]); xv[0][2 * u + 1] = bf_hi(xr0[u]); xv[1][2 * u] = bf_lo(xr1[u]); xv[1][2 * u + 1] = bf_hi(xr1[u]); }
        layer_norm(xv, L + L_XN, lane);
        {
            v4u B[16];
            load_act(B, L + L_XN, lane);
            __builtin_amdgcn_sched_barrier(0);
            wait_vm<0>();
            const f32x4 qv = gemv16(L + L_WQ, B, lane);
            const u32 qb = pk_bf16(sel4(qv, n >> 1), 0.f) & 0xffffu;
            GAS u64* dst = (GAS u64*)(p.ws + WS_QG) + (kvh * 2 + (n & 1)) * 512 + 16 * jq + 4 * g + (n >> 1);
            if (n < 8) __hip_atomic_store(dst, (tag << 16) | qb, RLX_AGENT);
            if (lane == 8) __hip_atomic_store((GAS u64*)(p.ws + WS_TG) + kvh * 32 + jq, (tag << 16) | (t_start & 0xffffu), RLX_AGENT);
        }
        {
            GAS u64* src = (GAS u64*)(p.ws + WS_QG) + (kvh * 2 + b) * 512 + lane;
            GAS u64* tsrc = (GAS u64*)(p.ws + WS_TG) + kvh * 32 + (lane & 31);
            u64 A[8], T;
            __builtin_amdgcn_s_sleep(80);
#pragma unroll 1
            for (u32 spin = 0; spin < SPIN_CAP; ++spin) {
                bool ok = true;
#pragma unroll
                for (int k = 0; k < 8; ++k) A[k] = __hip_atomic_load(src + 64 * k, RLX_AGENT);
                T = __hip_atomic_load(tsrc, RLX_AGENT);
#pragma unroll
                for (int k = 0; k < 8; ++k) ok &= (A[k] >> 16) == tag;
                ok &= (T >> 16) == tag;
                if (__all(ok)) break;
                __builtin_amdgcn_s_sleep(24);
            }
#pragma unroll
            for (int k = 0; k < 8; ++k) *(LAS u16*)(L + L_Q + (64 * k + lane) * 2) = (u16)A[k];
            const float early = -x16_max(row_max(-(float)(short)((u32)T - t_start)));
            *(LAS v2u*)(L + L_CNT) = v2u{0u, t_start + (int)early};
        }
        wg_barrier();
        __builtin_amdgcn_s_setprio(1);
        layer_norm(xv, L + L_Y, lane);
        const int fb = bid >> 7, fq = (bid >> 3) & 15, hf = lane >> 5, fc = 64 * x8 + 4 * fq + 2 * hf;
        const u32 xo = *(const GAS u32*)(p.x + fb * DM + fc), b2o = *(const GAS u32*)(p.b2 + fc);
        const int col1 = do_m1 ? 16 * jm + 4 * g + ((n >> 1) & 3) : 0;
        const float bias1 = bf_lo(p.b1[col1]);
        if (do_m1) {
            const u16* s1 = p.w1 + (size_t)(lane >> 1) * FF + 16 * jm + 8 * (lane & 1);
#pragma unroll
            for (int u = 0; u < 16; ++u) { const int ur = (u + rot) & 15; wait_vm<WW1 - 1>(); glds_sh(s1 + (size_t)ur * 32 * FF, lb + L_W1 + ur * 1024); }
        }
        if (do_m2) {
            const u16* s2 = p.w2 + (size_t)(hi * 64 + (lane >> 3)) * DM + 64 * cg + 8 * (lane & 7);
#pragma unroll
            for (int u = 0; u < 8; ++u) { wait_vm<WW2 - 1>(); glds_nt(s2 + (size_t)u * 8 * DM, lb + L_W2 + u * 1024); }
        }
        wait_vm<0>();
        if (do_m1) {
            v4u B[16];
            load_act(B, L + L_Y, lane);
            const float u = rbf(rbf(sel4(gemv16(L + L_W1, B, lane), n >> 1)) + bias1);
            const float z = 1.5957691216057308f * fmaf(0.044715f * u * u, u, u);
            const float h = u / (1.f + __expf(-z));
            if (n < 8) __hip_atomic_store((GAS u64*)(p.ws + WS_HG) + (n & 1) * FF + col1, (tag << 16) | (pk_bf16(h, 0.f) & 0xffffu), RLX_AGENT);
        }
        const int pi = lane & 31, pw = ((pi & 3) * 8 + x8) * 8 + (pi >> 2);
        const u32 pslot = WS_SLOT + pw * SLOT + fb * 256 + fq * 16 + hf * 8;
        {
            const int ek = hf ? (pw >> 4) & 7 : x8, ej = hf ? 4 * (pw & 7) + 2 * (pw >> 7) + ((pw >> 3) & 1) : pi;
            GAS u64* ev = (GAS u64*)(p.ws + WS_QG) + ek * 1024 + 16 * ej;
            GAS u64* hs = do_m2 ? (GAS u64*)(p.ws + WS_HG) + 64 * hi + lane : ev;
            const int hstr = do_m2 ? FF : 0;
            u64 e, h0, h1;
            if (!do_m1) { __builtin_amdgcn_s_sleep(64); __builtin_amdgcn_s_sleep(64); }
            __builtin_amdgcn_s_sleep(64);
#pragma unroll 1
            for (u32 spin = 0; spin < SPIN_CAP; ++spin) {
                e = __hip_atomic_load(ev, RLX_AGENT); h0 = __hip_atomic_load(hs, RLX_AGENT); h1 = __hip_atomic_load(hs + hstr, RLX_AGENT);
                if (__all(((e >> 16) == tag) & ((h0 >> 16) == tag) & ((h1 >> 16) == tag))) break;
                __builtin_amdgcn_s_sleep(64);
            }
            *(LAS u16*)(L + L_H + lane * 2) = (u16)h0;
            *(LAS u16*)(L + L_H + 128 + lane * 2) = (u16)h1;
        }
        if (do_m2) {
#pragma unroll
            for (int cb = 0; cb < 4; ++cb) {
                f32x4 mp = {0.f, 0.f, 0.f, 0.f};
#pragma unroll
                for (int u = 0; u < 2; ++u) mp = mfma32(tr_frag(L + L_W2 + 32 * cb, 128, 32 * u, lane), *(const LAS v4u*)(L + L_H + (n & 1) * 128 + 64 * u + 16 * g), mp);
                __builtin_amdgcn_raw_buffer_store_b128(clean4(mp), rws, n < 2 ? myslot + SL_PM + n * 256 + cb * 64 + g * 16 : OOR, 0, AUX_SC1);
            }
        }
        float gate[2];
        {
            v2u a;
            __builtin_amdgcn_s_sleep(64);
#pragma unroll 1
            for (u32 spin = 0; spin < SPIN_CAP; ++spin) {
                asm volatile("" ::: "memory");
                a = __builtin_amdgcn_raw_buffer_load_b64(rws, pi < 28 ? pslot + SL_PM : OOR, 0, AUX_SC1);
                if (__all(max(a.x, a.y) != SENT)) break;
                __builtin_amdgcn_s_sleep(64);
            }
#pragma unroll
            for (int k = 0; k < 2; ++k) {
                const float mm = rbf(rbf(x16_sum(row_sum(__uint_as_float(a[k])))) + (k ? bf_hi(b2o) : bf_lo(b2o)));
                gate[k] = rbf(mm / (1.f + __expf(-mm)));
            }
        }
        __builtin_amdgcn_s_setprio(3);
        const u32 fme = lane == 0 ? myslot + SL_FA : OOR;
        const u32 fao = lane < 32 ? WS_SLOT + (((lane >> 4) * 8 + x8) * 16 + (lane & 15)) * SLOT + SL_FA : OOR;
        const u32 fbo = lane < 32 ? WS_SLOT + pw * SLOT + SL_FB : OOR;
#define WAIT_WORDS(o)                                                                                          \
    _Pragma("unroll 1") for (u32 spin = 0; spin < SPIN_CAP; ++spin) {                                          \
        asm volatile("" ::: "memory");                                                                         \
        if (__all(__builtin_amdgcn_raw_buffer_load_b32(rws, o, 0, AUX_SC1) != SENT)) break;                    \
        __builtin_amdgcn_s_sleep(1);                                                                           \
    }
#pragma unroll 1
        for (u32 spin = 0; spin < 16 * SPIN_CAP; ++spin) {
            if (__hip_atomic_load((LAS u32*)(L + L_CNT), __ATOMIC_RELAXED, __HIP_MEMORY_SCOPE_WORKGROUP) >= NSW) break;
            __builtin_amdgcn_s_sleep(1);
        }
        __builtin_amdgcn_raw_buffer_store_b32(1u, rws, fme, 0, AUX_SC1);
        wg_barrier();
        WAIT_WORDS(fao);
        wg_barrier();
        __builtin_amdgcn_s_sleep(8);
        __builtin_amdgcn_raw_buffer_store_b32(1u, rws, fme + (SL_FB - SL_FA), 0, AUX_SC1);
        wg_barrier();
        WAIT_WORDS(fbo);
#undef WAIT_WORDS
        {
            v2u a;
#pragma unroll 1
            for (u32 spin = 0; spin < SPIN_CAP; ++spin) {
                asm volatile("" ::: "memory");
                a = __builtin_amdgcn_raw_buffer_load_b64(rws, pslot + SL_PS, 0, AUX_SC1);
                if (__all(max(a.x, a.y) != SENT)) break;
            }
            const float s0 = rbf(x16_sum(row_sum(__uint_as_float(a.x)))), s1 = rbf(x16_sum(row_sum(__uint_as_float(a.y))));
            const u32 r = pk_bf16(bf_lo(xo) + rbf(s0 * gate[0]), bf_hi(xo) + rbf(s1 * gate[1]));
            if (pi == 0) *(GAS u32*)(p.out + fb * DM + fc) = r;
            if (bid == 0 && lane == 0) __hip_atomic_store((GAS u64*)(p.ws + WS_EPOCH), (((u64)ep.y << 32) | ep.x) + 1, RLX_AGENT);
        }
    } else {
        const u16* kb = p.k + (size_t)pair * SK * HD;
        const u16* vb = p.v + (size_t)pair * SK * HD;
        const __amdgpu_buffer_rsrc_t rk = mk_rsrc(kb, SK * HD * 2), rv = mk_rsrc(vb, SK * HD * 2);
        constexpr u32 TSTR = NSW * 4096;
        const u32 kl = (n >> 2) * 256 + g * 64 + (n & 3) * 16, vl = lane * 16;
        u32 ko = (s * NS * NSW + w) * 4096 + kl, vo = (s * NS * NSW + w) * 4096 + vl;
        v4u R[3][4], KX[RP][4];
        u64 stub0, stub;
        asm volatile("s_getpc_b64 %0\n\ts_branch 1f\n\t"
                     "s_waitcnt vmcnt(3)\n\ts_setpc_b64 s[80:81]\n\ts_waitcnt vmcnt(4)\n\ts_setpc_b64 s[80:81]\n\ts_waitcnt vmcnt(5)\n\ts_setpc_b64 s[80:81]\n\t"
                     "s_waitcnt vmcnt(6)\n\ts_setpc_b64 s[80:81]\n\ts_waitcnt vmcnt(7)\n\ts_setpc_b64 s[80:81]\n1:" : "=s"(stub0));
        stub0 -= 28;
        stub = stub0 + 8 * 6;
        u32 due = t_start * 16 + T_OFF;
#define WAITW() asm volatile("s_swappc_b64 s[80:81], %0" ::"s"(stub) : "memory", "s80", "s81")
#define CLOCK() const u32 now_ = (u32)__builtin_amdgcn_s_memrealtime(); __builtin_amdgcn_sched_barrier(0)
#define PACE(tau)                                                  \
    do {                                                                                                       \
        due += (tau);                                                                                          \
        const int win = __builtin_amdgcn_readfirstlane(min(max(6 + (((int)(now_ * 16 - due) + 512) >> 10), 4), 8)); \
        stub = stub0 + 8 * win;                                                                                \
        __builtin_amdgcn_sched_barrier(0);                                                                     \
    } while (0)
#define LK(X, u) do { WAITW(); X[u] = __builtin_amdgcn_raw_buffer_load_b128(rk, ko + 1024 * (u), 0, AUX_NT); __builtin_amdgcn_sched_barrier(0); } while (0)
#define LV(X, u) do { WAITW(); X[u] = __builtin_amdgcn_raw_buffer_load_b128(rv, vo + 1024 * (u), 0, AUX_NT); __builtin_amdgcn_sched_barrier(0); } while (0)
#define LK4(X) do { LK(X, 0); LK(X, 1); LK(X, 2); LK(X, 3); ko += TSTR; } while (0)
#define LV4(X) do { LV(X, 0); LV(X, 1); LV(X, 2); LV(X, 3); vo += TSTR; } while (0)
        const u32 park = lb + L_PARK + w * LP * 4096;
#pragma unroll
        for (int t = 0; t < LP; ++t) {
            const unsigned char* ks = (const unsigned char*)kb + ko;
            WAITW(); glds_nt_off<0>(ks, park + t * 4096);
            WAITW(); glds_nt_off<1024>(ks, park + t * 4096);
            WAITW(); glds_nt_off<2048>(ks, park + t * 4096);
            WAITW(); glds_nt_off<3072>(ks, park + t * 4096);
            ko += TSTR;
            CLOCK(); PACE(T_K);
        }
#pragma unroll
        for (int t = 0; t < RP; ++t) { LK4(KX[t]); CLOCK(); PACE(T_K); }
#pragma unroll
        for (int t = 0; t < 2; ++t) { LK4(R[t]); CLOCK(); PACE(T_K); }
        wg_barrier();
        due += (__builtin_amdgcn_readfirstlane(*(const LAS u32*)(L + L_CNT + 4)) - t_start) * 16;
        const int jj = n & 3;
        const bool j0 = jj & 1, j1 = jj & 2;
        const v4u Q = *(const LAS v4u*)(L + L_Q + (n >> 2) * 256 + 64 * g + 16 * jj);
        const u32 mask0 = jj == 0 ? 0x0000ffffu : jj == 1 ? 0xffff0000u : 0u, mask1 = jj == 2 ? 0x0000ffffu : jj == 3 ? 0xffff0000u : 0u;
        float m = -INFINITY, l = 0.f;
        f32x4 acc0 = {0.f, 0.f, 0.f, 0.f}, acc1 = {0.f, 0.f, 0.f, 0.f};
        LAS float* scp = (LAS float*)(L + L_SC + w * 8192) + lane;
#define KP(KR, idx)                                                                                            \
    do {                                                                                                       \
        f32x4 dd[4]; float sc[4];                                                                              \
        _Pragma("unroll") for (int u = 0; u < 4; ++u) dd[u] = mfma32(KR[u], Q, f32x4{0.f, 0.f, 0.f, 0.f});     \
        asm volatile("" : "+v"(dd[0]), "+v"(dd[1]), "+v"(dd[2]), "+v"(dd[3]));     \
        _Pragma("unroll") for (int u = 0; u < 4; ++u) sc[u] = sel4(dd[u], jj);                                 \
        const float y01 = (j0 ? sc[1] : sc[0]) + dpp<XOR1>(j0 ? sc[0] : sc[1]), y23 = (j0 ? sc[3] : sc[2]) + dpp<XOR1>(j0 ? sc[2] : sc[3]); \
        const float y = (j1 ? y23 : y01) + dpp<XOR2>(j1 ? y01 : y23);                                          \
        m = fmaxf(m, y);                                                                                       \
        scp[(idx) * 64] = y;                                                                                   \
        __builtin_amdgcn_sched_barrier(0);                                                                     \
    } while (0)
#define UNPARK(t, KR)                                                                                          \
    do {                                                                                                       \
        const LAS unsigned char* pk = L + L_PARK + (w * LP + (t)) * 4096 + lane * 16;                          \
        _Pragma("unroll") for (int u = 0; u < 4; ++u) KR[u] = *(const LAS v4u*)(pk + 1024 * u);                \
    } while (0)
#define VP(VR, sv)                                                                                             \
    do {                                                                                                       \
        const float p1 = ex2(fmaf(sv, SC, -mc));                                                               \
        l += p1;                                                                                               \
        const int pb = pk_bf16(p1, p1);                                                                        \
        _Pragma("unroll") for (int r = 0; r < 4; ++r) {                                                        \
            const u32 t_ = r == 0 ? __builtin_amdgcn_mov_dpp(pb, 0x00, 0xf, 0xf, true) : r == 1 ? __builtin_amdgcn_mov_dpp(pb, 0x55, 0xf, 0xf, true) \
                         : r == 2 ? __builtin_amdgcn_mov_dpp(pb, 0xAA, 0xf, 0xf, true) : __builtin_amdgcn_mov_dpp(pb, 0xFF, 0xf, 0xf, true); \
            acc0 = mfma16(VR[r].x, VR[r].y, t_ & mask0, t_ & mask1, acc0);                                     \
            acc1 = mfma16(VR[r].z, VR[r].w, t_ & mask0, t_ & mask1, acc1);                                     \
        }                                                                                                      \
        __builtin_amdgcn_sched_barrier(0);                                                                     \
    } while (0)
#define UKK(u, idx) do { LK4(R[((u) + 2) % 3]); CLOCK(); KP(R[(u) % 3], idx); PACE(T_K); } while (0)
#define UVK(u, idx) do { LV4(R[((u) + 2) % 3]); CLOCK(); KP(R[(u) % 3], idx); PACE(T_V); } while (0)
#define UVV(u, idx) do { float sv_ = scp[(idx) * 64]; LV4(R[((u) + 2) % 3]); asm volatile("" : "+v"(sv_)); CLOCK(); VP(R[(u) % 3], sv_); PACE(T_V); } while (0)
#define UKB(u, XA, ia, XB, ib, PRE)                                                                            \
    do {                                                                                                       \
        LK(R[((u) + 2) % 3], 0); PRE; KP(XA, ia); LK(R[((u) + 2) % 3], 1); LK(R[((u) + 2) % 3], 2); KP(XB, ib); LK(R[((u) + 2) % 3], 3); ko += TSTR; \
        CLOCK(); KP(R[(u) % 3], 12 + (u)); PACE(T_K);                                                          \
    } while (0)
        static_assert(LP == 4 && RP == 8 && NBL == 6, "");
        UKB(0, KX[0], 4, KX[1], 5, (void)0);
        UKB(1, KX[2], 6, KX[3], 7, { UNPARK(0, KX[0]); UNPARK(1, KX[1]); });
        UKB(2, KX[0], 0, KX[1], 1, { UNPARK(2, KX[2]); UNPARK(3, KX[3]); });
        UKB(3, KX[2], 2, KX[3], 3, (void)0);
        UKB(4, KX[4], 8, KX[5], 9, (void)0);
        UKB(5, KX[6], 10, KX[7], 11, (void)0);
        scp += (12 + NBL) * 64;
#pragma unroll 1
        for (int it = 0; it < KA; ++it) { UKK(0, 0); UKK(1, 1); UKK(2, 2); scp += 3 * 64; }
        UVK(0, 0); UVK(1, 1);
        m = fmaxf(m, dpp<XOR1>(m)); m = fmaxf(m, dpp<XOR2>(m)); m = x32_max(x16_max(m));
        const float mc = m * SC;
        const int bb = w & 1, dh = w >> 1;
        u32 cnt_a = lb + L_CNT, cnt_v = lane == 0;
        u32 a_mw = L_MRG + ((w * 4 + (n >> 2)) * 132 + 32 * g + jj) * 4, a_lw = L_MML + (w * 4 + (n >> 2)) * 8;
        u32 a_mr = L_MRG + (w * 132 + 4 * (lane & 31)) * 4, a_lr = L_MML + w * 8;
        u32 o_pa = lane < 32 ? myslot + w * 512 + lane * 16 : OOR, o_pl = lane == 0 ? WS_ML + ((pair * 4 + w) * 16 + s) * 8 : OOR;
        u32 o_ga = WS_SLOT + ((bb * 8 + x8) * 16 + 4 * g) * SLOT + (hi & 3) * 512 + dh * 256 + n * 16;
        u32 o_gl = WS_ML + (((bb * 8 + x8) * 4 + (hi & 3)) * 16 + ((n + 4 * g) & 15)) * 8;
        u32 a_wo = L_WO + 32 * w + (8 * g + ((lane >> 2) & 3)) * 128 + 8 * jj, a_ow = L_O + bb * 256 + dh * 128 + n * 8 + g * 2;
        u32 a_or = L_O + (n & 1) * 256 + 16 * g, o_ps = n < 2 ? myslot + SL_PS + n * 256 + w * 64 + g * 16 : OOR;
        asm volatile("" : "+v"(cnt_a), "+v"(cnt_v), "+v"(a_mw), "+v"(a_lw), "+v"(a_mr), "+v"(a_lr), "+v"(o_pa), "+v"(o_pl));
        const u16* wo_p = p.wo + (size_t)(hi * 128 + 32 * w + (lane >> 3)) * DM + 64 * cg + 8 * (lane & 7);
        asm volatile("" : "+v"(o_ga), "+v"(o_gl), "+v"(a_wo), "+v"(a_ow), "+v"(a_or), "+v"(o_ps), "+v"(wo_p));
        scp -= (NS - 2) * 64;
#pragma unroll 1
        for (int it = 0; it < VA; ++it) { UVV(2, 0); UVV(3, 1); UVV(4, 2); scp += 3 * 64; }
        UVV(2, 0); UVV(3, 1);
        asm volatile("ds_add_u32 %0, %1" ::"v"(cnt_a), "v"(cnt_v) : "memory");
        {
            const float s0 = scp[2 * 64], s1 = scp[3 * 64], s2 = scp[4 * 64];
            LV4(R[0]);
            VP(R[1], s0); VP(R[2], s1); VP(R[0], s2);
            l += dpp<XOR1>(l); l += dpp<XOR2>(l);
        }
        {
            LAS float* mr = (LAS float*)(L + a_mw);
#pragma unroll
            for (int r = 0; r < 4; ++r) { mr[8 * r] = acc0[r]; mr[8 * r + 4] = acc1[r]; }
            *(LAS f32x2*)(L + a_lw) = f32x2{mc, x32_sum(x16_sum(l))};
        }
#pragma unroll
        for (int u = 0; u < 4; ++u) glds_nt(wo_p + (size_t)u * 8 * DM, lb + L_WO + (4 * w + u) * 1024);
        wg_barrier();
        {
            f32x2 ml[NSW]; f32x4 ov[NSW];
#pragma unroll
            for (int u = 0; u < NSW; ++u) ml[u] = *(const LAS f32x2*)(L + a_lr + u * 32);
#pragma unroll
            for (int u = 0; u < NSW; ++u) ov[u] = *(const LAS f32x4*)(L + a_mr + u * (4 * 132 * 4));
            const float M = fmaxf(fmaxf(ml[0].x, ml[1].x), fmaxf(ml[2].x, ml[3].x));
            float f[NSW];
#pragma unroll
            for (int u = 0; u < NSW; ++u) f[u] = ex2(ml[u].x - M);
            const f32x4 o = (f[0] * ov[0] + f[1] * ov[1]) + (f[2] * ov[2] + f[3] * ov[3]);
            __builtin_amdgcn_raw_buffer_store_b128(clean4(o), rws, o_pa, 0, AUX_SC1);
            const float Ls = fmaf(f[0], ml[0].y, f[1] * ml[1].y) + fmaf(f[2], ml[2].y, f[3] * ml[3].y);
            __builtin_amdgcn_raw_buffer_store_b64(v2u{no_sent(M), no_sent(Ls)}, rws, o_pl, 0, AUX_SC1);
        }
        v4u WO[4];
        {
            v4u a[4]; v2u q2;
#define GATHER()                                                                                               \
    do {                                                                                                       \
        q2 = __builtin_amdgcn_raw_buffer_load_b64(rws, o_gl, 0, AUX_SC1);                                      \
        _Pragma("unroll") for (int k = 0; k < 4; ++k) a[k] = __builtin_amdgcn_raw_buffer_load_b128(rws, o_ga, k * SLOT, AUX_SC1); \
    } while (0)
#define ARRIVED() __all(max(max(max(umax4(a[0]), umax4(a[1])), max(umax4(a[2]), umax4(a[3]))), max(q2.x, q2.y)) != SENT)
            wait_vm<2>();
            wg_barrier();
            GATHER();
            __builtin_amdgcn_sched_barrier(0);
#pragma unroll
            for (int u = 0; u < 4; ++u) {
                const v2u lo = tr_read(L + a_wo + 4096 * u), hi2 = tr_read(L + a_wo + 4096 * u + 512);
                WO[u] = v4u{lo.x, lo.y, hi2.x, hi2.y};
            }
            __builtin_amdgcn_sched_barrier(0);
            if (!ARRIVED()) {
#pragma unroll 1
                for (u32 spin = 0; spin < SPIN_CAP; ++spin) {
                    asm volatile("" ::: "memory");
                    GATHER();
                    if (ARRIVED()) break;
                }
            }
            const float mp = __uint_as_float(q2.x);
            const float M = row_max(mp);
            const float wt = ex2(mp - M);
            const float inv = __builtin_amdgcn_rcpf(row_sum(wt * __uint_as_float(q2.y)));
            const f32x4 o = (dpp<0x150>(wt) * __builtin_bit_cast(f32x4, a[0]) + dpp<0x151>(wt) * __builtin_bit_cast(f32x4, a[1])) +
                            (dpp<0x152>(wt) * __builtin_bit_cast(f32x4, a[2]) + dpp<0x153>(wt) * __builtin_bit_cast(f32x4, a[3]));
            const auto s02 = __builtin_amdgcn_permlane32_swap(__float_as_uint(o[0]), __float_as_uint(o[2]), false, false);
            const auto s13 = __builtin_amdgcn_permlane32_swap(__float_as_uint(o[1]), __float_as_uint(o[3]), false, false);
            const float xs = __uint_as_float(s02[0]) + __uint_as_float(s02[1]), ys = __uint_as_float(s13[0]) + __uint_as_float(s13[1]);
            const auto sz = __builtin_amdgcn_permlane16_swap(__float_as_uint(xs), __float_as_uint(ys), false, false);
            const float z = (__uint_as_float(sz[0]) + __uint_as_float(sz[1])) * inv;
            *(LAS u16*)(L + a_ow) = (u16)pk_bf16(z, z);
        }
        wg_barrier();
        {
            v4u ob[4];
#pragma unroll
            for (int u = 0; u < 4; ++u) ob[u] = *(const LAS v4u*)(L + a_or + 64 * u);
            __builtin_amdgcn_sched_barrier(0);
            const f32x4 z = {0.f, 0.f, 0.f, 0.f};
            const f32x4 sp = mfma32(WO[2], ob[2], mfma32(WO[0], ob[0], z)) + mfma32(WO[3], ob[3], mfma32(WO[1], ob[1], z));
            __builtin_amdgcn_raw_buffer_store_b128(clean4(sp), rws, o_ps, 0, AUX_SC1);
        }
    }
}

extern "C" void kernel_launch(void* const* d_in, const int* in_sizes, int n_in, void* d_out, int out_size, void* d_ws, size_t ws_size,
                              hipStream_t stream) {
    Params p;
    p.x = (const u16*)d_in[0]; p.k = (const u16*)d_in[1]; p.v = (const u16*)d_in[2]; p.wq = (const u16*)d_in[3]; p.wo = (const u16*)d_in[4];
    p.w1 = (const u16*)d_in[6]; p.b1 = (const u16*)d_in[7]; p.w2 = (const u16*)d_in[8]; p.b2 = (const u16*)d_in[9];
    p.out = (u16*)d_out; p.ws = (unsigned char*)d_ws;
    (void)hipFuncSetAttribute((const void*)fused_decode, hipFuncAttributeMaxDynamicSharedMemorySize, L_END);
    hipLaunchKernelGGL(fused_decode, dim3(NWG), dim3(NTHR), L_END, stream, p);
}
```

```cpp
#include <hip/hip_runtime.h>
#include <hip/hip_bf16.h>
#include <stdint.h>

#define GAS __attribute__((address_space(1)))
#define LAS __attribute__((address_space(3)))
typedef unsigned u32;
typedef unsigned long long u64;
typedef unsigned short u16;
typedef unsigned v4u __attribute__((ext_vector_type(4)));
typedef unsigned v2u __attribute__((ext_vector_type(2)));
typedef float f32x4 __attribute__((ext_vector_type(4)));
typedef float f32x2 __attribute__((ext_vector_type(2)));
typedef short s16x4 __attribute__((ext_vector_type(4)));
typedef __bf16 bf16x8 __attribute__((ext_vector_type(8)));
typedef __bf16 bf16x2 __attribute__((ext_vector_type(2)));

constexpr int DM = 512, HD = 128, SK = 32768, FF = 1792;
constexpr int NWG = 256, NSW = 4, NTHR = (NSW + 1) * 64;
constexpr int NS = SK / 16 / 16 / NSW;
constexpr int LP = 4, RP = 8, NPRO = LP + RP + 2;
constexpr int NBL = 6, KA = (NS - NPRO - NBL) / 3, VA = (NS - 2) / 3 - 1;
static_assert(NS == 32 && NPRO + NBL + 3 * KA == NS && 3 * VA + 5 == NS, "");
constexpr int T_OFF = 115 * 16, T_K = 922, T_V = 908;
constexpr int WQ = 8, WW1 = 2, WW2 = 2;
constexpr float SC = 0.08838834764831845f * 1.4426950408889634f;
constexpr u32 SENT = 0xFFFFFFFFu;
constexpr u32 SPIN_CAP = 1u << 21;
constexpr u32 OOR = 0xFFFFFF00u;

constexpr u32 WS_EPOCH = 0;
constexpr u32 WS_QG = 4096;
constexpr u32 WS_HG = WS_QG + 8 * 2 * 512 * 8;
constexpr u32 WS_TG = 102400;
constexpr u32 WS_ML = 118784;
constexpr u32 WS_SLOT = 131072, SLOT = 4352;
constexpr u32 SL_PS = 2048, SL_PM = 2560, SL_FA = 3072, SL_FB = 3200;
constexpr u32 WS_END = WS_SLOT + NWG * SLOT;
static_assert(WS_HG + 2 * FF * 8 <= WS_TG && WS_TG + 256 * 8 <= WS_ML && WS_ML + 8192 <= WS_SLOT, "");

constexpr u32 L_WQ = 0, L_W1 = 0, L_WO = 16384, L_W2 = 32768;
constexpr u32 L_XN = 40960, L_Y = L_XN + 2048, L_Q = L_Y + 2048, L_H = L_Q + 1024, L_O = L_H + 256;
constexpr u32 L_MRG = L_O + 512;
constexpr u32 L_MML = L_MRG + NSW * 4 * 132 * 4;
constexpr u32 L_CNT = L_MML + NSW * 4 * 2 * 4;
constexpr u32 L_SC = 57344;
constexpr u32 L_PARK = L_SC + NSW * 8192;
constexpr u32 L_END = L_PARK + NSW * LP * 4096;
static_assert(L_CNT + 16 <= L_SC && L_END > 80 * 1024 && L_END <= 160 * 1024, "more than half of a CU's LDS: one workgroup per CU");

struct Params {
    const u16 *x, *k, *v, *wq, *wo, *w1, *b1, *w2, *b2;
    u16* out;
    unsigned char* ws;
};

__device__ __forceinline__ f32x4 mfma32(v4u a, v4u b, f32x4 c) {
    return __builtin_amdgcn_mfma_f32_16x16x32_bf16(__builtin_bit_cast(bf16x8, a), __builtin_bit_cast(bf16x8, b), c, 0, 0, 0);
}
__device__ __forceinline__ f32x4 mfma16(u32 a0, u32 a1, u32 b0, u32 b1, f32x4 c) {
    return __builtin_amdgcn_mfma_f32_16x16x16bf16_1k(__builtin_bit_cast(s16x4, v2u{a0, a1}), __builtin_bit_cast(s16x4, v2u{b0, b1}), c, 0, 0, 0);
}
__device__ __forceinline__ u32 pk_bf16(float lo, float hi) {
    return __builtin_bit_cast(u32, __builtin_convertvector(f32x2{lo, hi}, bf16x2));
}
__device__ __forceinline__ float bf_lo(u32 w) { return __builtin_bit_cast(float, w << 16); }
__device__ __forceinline__ float bf_hi(u32 w) { return __builtin_bit_cast(float, w & 0xffff0000u); }
__device__ __forceinline__ float rbf(float f) { return bf_lo(pk_bf16(f, f)); }
__device__ __forceinline__ float ex2(float x) { return __builtin_amdgcn_exp2f(x); }
template <int CTRL> __device__ __forceinline__ float dpp(float x) {
    return __builtin_bit_cast(float, __builtin_amdgcn_mov_dpp(__builtin_bit_cast(int, x), CTRL, 0xf, 0xf, true));
}
constexpr int XOR1 = 0xB1, XOR2 = 0x4E, XOR7 = 0x141, ROR8 = 0x128;
__device__ __forceinline__ float x16_max(float x) {
    auto s = __builtin_amdgcn_permlane16_swap(__float_as_uint(x), __float_as_uint(x), false, false);
    return fmaxf(__uint_as_float(s[0]), __uint_as_float(s[1]));
}
__device__ __forceinline__ float x32_max(float x) {
    auto s = __builtin_amdgcn_permlane32_swap(__float_as_uint(x), __float_as_uint(x), false, false);
    return fmaxf(__uint_as_float(s[0]), __uint_as_float(s[1]));
}
__device__ __forceinline__ float x16_sum(float x) {
    auto s = __builtin_amdgcn_permlane16_swap(__float_as_uint(x), __float_as_uint(x), false, false);
    return __uint_as_float(s[0]) + __uint_as_float(s[1]);
}
__device__ __forceinline__ float x32_sum(float x) {
    auto s = __builtin_amdgcn_permlane32_swap(__float_as_uint(x), __float_as_uint(x), false, false);
    return __uint_as_float(s[0]) + __uint_as_float(s[1]);
}
__device__ __forceinline__ float row_sum(float x) {
    x += dpp<XOR1>(x); x += dpp<XOR2>(x); x += dpp<XOR7>(x); x += dpp<ROR8>(x); return x;
}
__device__ __forceinline__ float row_max(float x) {
    float r;
    asm("s_nop 1\n\tv_max_f32_dpp %0, %1, %1 quad_perm:[1,0,3,2] row_mask:0xf bank_mask:0xf\n\ts_nop 1\n\t"
        "v_max_f32_dpp %0, %0, %0 quad_perm:[2,3,0,1] row_mask:0xf bank_mask:0xf\n\ts_nop 1\n\t"
        "v_max_f32_dpp %0, %0, %0 row_half_mirror row_mask:0xf bank_mask:0xf\n\ts_nop 1\n\t"
        "v_max_f32_dpp %0, %0, %0 row_ror:8 row_mask:0xf bank_mask:0xf" : "=&v"(r) : "v"(x));
    return r;
}
__device__ __forceinline__ float wave_sum(float x) { return x32_sum(x16_sum(row_sum(x))); }
__device__ __forceinline__ u32 umax4(v4u a) { return max(max(a.x, a.y), max(a.z, a.w)); }
__device__ __forceinline__ u32 no_sent(float f) { return min(__float_as_uint(f), 0xFFFFFFFEu); }

#define RLX_AGENT __ATOMIC_RELAXED, __HIP_MEMORY_SCOPE_AGENT
constexpr int AUX_NT = 2, AUX_SC1 = 16;
__device__ __forceinline__ __amdgpu_buffer_rsrc_t mk_rsrc(const void* p, u32 bytes) {
    return __builtin_amdgcn_make_buffer_rsrc((void*)p, 0, bytes, 0x00020000);
}
__device__ __forceinline__ void glds_nt(const void* src, u32 dst) {
    asm volatile("s_mov_b32 m0, %1\n\ts_nop 0\n\tglobal_load_lds_dwordx4 %0, off nt" ::"v"(src), "s"(dst) : "memory");
}
__device__ __forceinline__ void glds_sh(const void* src, u32 dst) {
    asm volatile("s_mov_b32 m0, %1\n\ts_nop 0\n\tglobal_load_lds_dwordx4 %0, off" ::"v"(src), "s"(dst) : "memory");
}
template <int OFF> __device__ __forceinline__ void glds_nt_off(const void* src, u32 dst) {
    asm volatile("s_mov_b32 m0, %1\n\ts_nop 0\n\tglobal_load_lds_dwordx4 %0, off offset:%2 nt" ::"v"(src), "s"(dst), "i"(OFF) : "memory");
}
template <int N> __device__ __forceinline__ void wait_vm() { asm volatile("s_waitcnt vmcnt(%0)" ::"i"(N) : "memory"); }
__device__ __forceinline__ void wg_barrier() { asm volatile("s_waitcnt lgkmcnt(0)\n\ts_barrier" ::: "memory"); }
__device__ __forceinline__ v2u tr_read(const LAS unsigned char* p) {
    return __builtin_bit_cast(v2u, __builtin_amdgcn_ds_read_tr16_b64_v4i16((LAS s16x4*)p));
}
__device__ __forceinline__ v4u tr_frag(const LAS unsigned char* img, int pitch, int row0, int lane) {
    const int g = lane >> 4, q = (lane >> 2) & 3, p = lane & 3;
    const LAS unsigned char* a = img + (row0 + 8 * g + q) * pitch + 8 * p;
    const v2u lo = tr_read(a), hi = tr_read(a + 4 * pitch);
    return v4u{lo.x, lo.y, hi.x, hi.y};
}

__device__ __forceinline__ void load_act(v4u (&B)[16], const LAS unsigned char* act, int lane) {
    const LAS unsigned char* ap = act + (lane & 1) * 1024 + (lane >> 4) * 16;
#pragma unroll
    for (int s = 0; s < 16; ++s) B[s] = *(const LAS v4u*)(ap + 64 * s);
}
__device__ __forceinline__ f32x4 gemv16(const LAS unsigned char* img, const v4u (&B)[16], int lane) {
    v4u A[16];
#pragma unroll
    for (int s = 0; s < 16; ++s) A[s] = tr_frag(img, 32, 32 * s, lane);
    __builtin_amdgcn_sched_barrier(0);
    f32x4 a[4];
#pragma unroll
    for (int s = 0; s < 16; ++s) a[s & 3] = mfma32(A[s], B[s], s < 4 ? f32x4{0.f, 0.f, 0.f, 0.f} : a[s & 3]);
    return (a[0] + a[1]) + (a[2] + a[3]);
}
__device__ __forceinline__ void layer_norm(float (&v)[2][8], LAS unsigned char* dst, int lane) {
    float mean[2], rs[2];
#pragma unroll
    for (int bb = 0; bb < 2; ++bb) mean[bb] = wave_sum(((v[bb][0] + v[bb][1]) + (v[bb][2] + v[bb][3])) + ((v[bb][4] + v[bb][5]) + (v[bb][6] + v[bb][7]))) * (1.f / DM);
#pragma unroll
    for (int bb = 0; bb < 2; ++bb) {
        float sq = 0.f;
#pragma unroll
        for (int u = 0; u < 8; ++u) { v[bb][u] -= mean[bb]; sq = fmaf(v[bb][u], v[bb][u], sq); }
        rs[bb] = __builtin_amdgcn_rsqf(wave_sum(sq) * (1.f / DM) + 1e-5f);
    }
#pragma unroll
    for (int bb = 0; bb < 2; ++bb) {
        v4u o;
#pragma unroll
        for (int u = 0; u < 4; ++u) {
            o[u] = pk_bf16(v[bb][2 * u] * rs[bb], v[bb][2 * u + 1] * rs[bb]);
            v[bb][2 * u] = bf_lo(o[u]); v[bb][2 * u + 1] = bf_hi(o[u]);
        }
        *(LAS v4u*)(dst + bb * 1024 + lane * 16) = o;
    }
}
__device__ __forceinline__ float sel4(f32x4 v, int r) {
    const bool b0 = r & 1, b1 = r & 2;
    const float lo = b0 ? v[1] : v[0], hi = b0 ? v[3] : v[2];
    return b1 ? hi : lo;
}
__device__ __forceinline__ v4u clean4(f32x4 v) { return v4u{no_sent(v[0]), no_sent(v[1]), no_sent(v[2]), no_sent(v[3])}; }

__global__ void __launch_bounds__(NTHR, 2) fused_decode(Params p) {
    extern __shared__ __attribute__((aligned(16))) unsigned char lds_raw[];
    LAS unsigned char* L = (LAS unsigned char*)lds_raw;
    const u32 lb = (u32)(size_t)L;
    const int tid = threadIdx.x, lane = tid & 63, w = __builtin_amdgcn_readfirstlane(tid >> 6);
    const int bid = blockIdx.x;
    const int pair = bid >> 4, b = pair >> 3, kvh = pair & 7, s = bid & 15;
    const int jq = 4 * (s & 7) + 2 * b + (s >> 3);
    const int m1i = (s - kvh) & 7, jm = 4 * (7 * (kvh >> 1) + 4 * (kvh & 1) + m1i) + (jq & 3);
    const int x8 = bid & 7, hi = 4 * x8 + (bid >> 6), cg = (bid >> 3) & 7;
    const bool do_m1 = m1i < 4 - (kvh & 1), do_m2 = hi < 28;
    const int g = lane >> 4, n = lane & 15;
    const __amdgpu_buffer_rsrc_t rws = mk_rsrc(p.ws, WS_END);
    const u32 myslot = WS_SLOT + bid * SLOT;

    const u32 t_start = (u32)__builtin_amdgcn_s_memrealtime();
    asm volatile("" ::"s"(p.k), "s"(p.v), "s"(p.x), "s"(p.wq), "s"(p.ws));
    if (w >= NSW) {
        __builtin_amdgcn_s_setprio(3);
        v4u xr0, xr1; v2u ep;
        {
            const void* xa = p.x + 8 * lane;
            const void* ea = p.ws + WS_EPOCH;
            asm volatile("global_load_dwordx4 %0, %3, off\n\tglobal_load_dwordx4 %1, %3, off offset:1024\n\tglobal_load_dwordx2 %2, %4, off sc1"
                         : "=&v"(xr0), "=&v"(xr1), "=&v"(ep) : "v"(xa), "v"(ea) : "memory");
        }
        {
            const v4u sv = {SENT, SENT, SENT, SENT};
#pragma unroll
            for (int u = 0; u < 3; ++u) __builtin_amdgcn_raw_buffer_store_b128(sv, rws, myslot + u * 1024 + lane * 16, 0, AUX_SC1);
            const u32 o4 = lane < 4 ? WS_ML + ((pair * 4 + lane) * 16 + s) * 8 : lane < 6 ? myslot + SL_FA + (lane - 4) * 128 : OOR;
            __builtin_amdgcn_raw_buffer_store_b64(v2u{SENT, SENT}, rws, o4, 0, AUX_SC1);
        }
        const int rot = 4 * (jq & 3);
        const u16* wqs = p.wq + (size_t)(lane >> 1) * 4096 + kvh * 512 + 16 * jq + 8 * (lane & 1);
#pragma unroll
        for (int u = 0; u < WQ; ++u) { const int ur = (u + rot) & 15; glds_sh(wqs + (size_t)ur * 32 * 4096, lb + L_WQ + ur * 1024); }
        asm volatile("s_waitcnt vmcnt(%3)" : "+v"(xr0), "+v"(xr1), "+v"(ep) : "i"(4 + WQ) : "memory");
#pragma unroll
        for (int u = WQ; u < 16; ++u) { const int ur = (u + rot) & 15; wait_vm<WQ - 1>(); glds_sh(wqs + (size_t)ur * 32 * 4096, lb + L_WQ + ur * 1024); }
        const u64 tag = ((((u64)ep.y << 32) | ep.x) + 1) & 0xFFFFFFFFFFFFull;
        float xv[2][8];
#pragma unroll
        for (int u = 0; u < 4; ++u) { xv[0][2 * u] = bf_lo(xr0[u]); xv[0][2 * u + 1] = bf_hi(xr0[u]); xv[1][2 * u] = bf_lo(xr1[u]); xv[1][2 * u + 1] = bf_hi(xr1[u]); }
        layer_norm(xv, L + L_XN, lane);
        {
            v4u B[16];
            load_act(B, L + L_XN, lane);
            __builtin_amdgcn_sched_barrier(0);
            wait_vm<0>();
            const f32x4 qv = gemv16(L + L_WQ, B, lane);
            const u32 qb = pk_bf16(sel4(qv, n >> 1), 0.f) & 0xffffu;
            GAS u64* dst = (GAS u64*)(p.ws + WS_QG) + (kvh * 2 + (n & 1)) * 512 + 16 * jq + 4 * g + (n >> 1);
            if (n < 8) __hip_atomic_store(dst, (tag << 16) | qb, RLX_AGENT);
            if (lane == 8) __hip_atomic_store((GAS u64*)(p.ws + WS_TG) + kvh * 32 + jq, (tag << 16) | (t_start & 0xffffu), RLX_AGENT);
        }
        {
            GAS u64* src = (GAS u64*)(p.ws + WS_QG) + (kvh * 2 + b) * 512 + lane;
            GAS u64* tsrc = (GAS u64*)(p.ws + WS_TG) + kvh * 32 + (lane & 31);
            u64 A[8], T;
            __builtin_amdgcn_s_sleep(60);
#pragma unroll 1
            for (u32 spin = 0; spin < SPIN_CAP; ++spin) {
                bool ok = true;
#pragma unroll
                for (int k = 0; k < 8; ++k) A[k] = __hip_atomic_load(src + 64 * k, RLX_AGENT);
                T = __hip_atomic_load(tsrc, RLX_AGENT);
#pragma unroll
                for (int k = 0; k < 8; ++k) ok &= (A[k] >> 16) == tag;
                ok &= (T >> 16) == tag;
                if (__all(ok)) break;
                __builtin_amdgcn_s_sleep(24);
            }
#pragma unroll
            for (int k = 0; k < 8; ++k) *(LAS u16*)(L + L_Q + (64 * k + lane) * 2) = (u16)A[k];
            const float early = -x16_max(row_max(-(float)(short)((u32)T - t_start)));
            *(LAS v2u*)(L + L_CNT) = v2u{0u, t_start + (int)early};
        }
        wg_barrier();
        __builtin_amdgcn_s_setprio(1);
        layer_norm(xv, L + L_Y, lane);
        const int fb = bid >> 7, fq = (bid >> 3) & 15, hf = lane >> 5, fc = 64 * x8 + 4 * fq + 2 * hf;
        const u32 xo = *(const GAS u32*)(p.x + fb * DM + fc), b2o = *(const GAS u32*)(p.b2 + fc);
        const int col1 = do_m1 ? 16 * jm + 4 * g + ((n >> 1) & 3) : 0;
        const float bias1 = bf_lo(p.b1[col1]);
        if (do_m1) {
            const u16* s1 = p.w1 + (size_t)(lane >> 1) * FF + 16 * jm + 8 * (lane & 1);
#pragma unroll
            for (int u = 0; u < 16; ++u) { const int ur = (u + rot) & 15; wait_vm<WW1 - 1>(); glds_sh(s1 + (size_t)ur * 32 * FF, lb + L_W1 + ur * 1024); }
        }
        if (do_m2) {
            const u16* s2 = p.w2 + (size_t)(hi * 64 + (lane >> 3)) * DM + 64 * cg + 8 * (lane & 7);
#pragma unroll
            for (int u = 0; u < 8; ++u) { wait_vm<WW2 - 1>(); glds_nt(s2 + (size_t)u * 8 * DM, lb + L_W2 + u * 1024); }
        }
        wait_vm<0>();
        if (do_m1) {
            v4u B[16];
            load_act(B, L + L_Y, lane);
            const float u = rbf(rbf(sel4(gemv16(L + L_W1, B, lane), n >> 1)) + bias1);
            const float z = 1.5957691216057308f * fmaf(0.044715f * u * u, u, u);
            const float h = u / (1.f + __expf(-z));
            if (n < 8) __hip_atomic_store((GAS u64*)(p.ws + WS_HG) + (n & 1) * FF + col1, (tag << 16) | (pk_bf16(h, 0.f) & 0xffffu), RLX_AGENT);
        }
        const int pi = lane & 31, pw = ((pi & 3) * 8 + x8) * 8 + (pi >> 2);
        const u32 pslot = WS_SLOT + pw * SLOT + fb * 256 + fq * 16 + hf * 8;
        {
            const int ek = hf ? (pw >> 4) & 7 : x8, ej = hf ? 4 * (pw & 7) + 2 * (pw >> 7) + ((pw >> 3) & 1) : pi;
            GAS u64* ev = (GAS u64*)(p.ws + WS_QG) + ek * 1024 + 16 * ej;
            GAS u64* hs = do_m2 ? (GAS u64*)(p.ws + WS_HG) + 64 * hi + lane : ev;
            const int hstr = do_m2 ? FF : 0;
            u64 e, h0, h1;
            if (!do_m1) { __builtin_amdgcn_s_sleep(64); __builtin_amdgcn_s_sleep(64); }
            __builtin_amdgcn_s_sleep(64);
#pragma unroll 1
            for (u32 spin = 0; spin < SPIN_CAP; ++spin) {
                e = __hip_atomic_load(ev, RLX_AGENT); h0 = __hip_atomic_load(hs, RLX_AGENT); h1 = __hip_atomic_load(hs + hstr, RLX_AGENT);
                if (__all(((e >> 16) == tag) & ((h0 >> 16) == tag) & ((h1 >> 16) == tag))) break;
                __builtin_amdgcn_s_sleep(64);
            }
            *(LAS u16*)(L + L_H + lane * 2) = (u16)h0;
            *(LAS u16*)(L + L_H + 128 + lane * 2) = (u16)h1;
        }
        if (do_m2) {
#pragma unroll
            for (int cb = 0; cb < 4; ++cb) {
                f32x4 mp = {0.f, 0.f, 0.f, 0.f};
#pragma unroll
                for (int u = 0; u < 2; ++u) mp = mfma32(tr_frag(L + L_W2 + 32 * cb, 128, 32 * u, lane), *(const LAS v4u*)(L + L_H + (n & 1) * 128 + 64 * u + 16 * g), mp);
                __builtin_amdgcn_raw_buffer_store_b128(clean4(mp), rws, n < 2 ? myslot + SL_PM + n * 256 + cb * 64 + g * 16 : OOR, 0, AUX_SC1);
            }
        }
        float gate[2];
        {
            v2u a;
            __builtin_amdgcn_s_sleep(64);
#pragma unroll 1
            for (u32 spin = 0; spin < SPIN_CAP; ++spin) {
                asm volatile("" ::: "memory");
                a = __builtin_amdgcn_raw_buffer_load_b64(rws, pi < 28 ? pslot + SL_PM : OOR, 0, AUX_SC1);
                if (__all(max(a.x, a.y) != SENT)) break;
                __builtin_amdgcn_s_sleep(64);
            }
#pragma unroll
            for (int k = 0; k < 2; ++k) {
                const float mm = rbf(rbf(x16_sum(row_sum(__uint_as_float(a[k])))) + (k ? bf_hi(b2o) : bf_lo(b2o)));
                gate[k] = rbf(mm / (1.f + __expf(-mm)));
            }
        }
        __builtin_amdgcn_s_setprio(3);
        const u32 fme = lane == 0 ? myslot + SL_FA : OOR;
        const u32 fao = lane < 32 ? WS_SLOT + (((lane >> 4) * 8 + x8) * 16 + (lane & 15)) * SLOT + SL_FA : OOR;
        const u32 fbo = lane < 32 ? WS_SLOT + pw * SLOT + SL_FB : OOR;
#define WAIT_WORDS(o)                                                                                          \
    _Pragma("unroll 1") for (u32 spin = 0; spin < SPIN_CAP; ++spin) {                                          \
        asm volatile("" ::: "memory");                                                                         \
        if (__all(__builtin_amdgcn_raw_buffer_load_b32(rws, o, 0, AUX_SC1) != SENT)) break;                    \
        __builtin_amdgcn_s_sleep(1);                                                                           \
    }
#pragma unroll 1
        for (u32 spin = 0; spin < 16 * SPIN_CAP; ++spin) {
            if (__hip_atomic_load((LAS u32*)(L + L_CNT), __ATOMIC_RELAXED, __HIP_MEMORY_SCOPE_WORKGROUP) >= NSW) break;
            __builtin_amdgcn_s_sleep(1);
        }
        __builtin_amdgcn_raw_buffer_store_b32(1u, rws, fme, 0, AUX_SC1);
        wg_barrier();
        WAIT_WORDS(fao);
        wg_barrier();
        __builtin_amdgcn_s_sleep(8);
        __builtin_amdgcn_raw_buffer_store_b32(1u, rws, fme + (SL_FB - SL_FA), 0, AUX_SC1);
        wg_barrier();
        WAIT_WORDS(fbo);
#undef WAIT_WORDS
        {
            v2u a;
#pragma unroll 1
            for (u32 spin = 0; spin < SPIN_CAP; ++spin) {
                asm volatile("" ::: "memory");
                a = __builtin_amdgcn_raw_buffer_load_b64(rws, pslot + SL_PS, 0, AUX_SC1);
                if (__all(max(a.x, a.y) != SENT)) break;
            }
            const float s0 = rbf(x16_sum(row_sum(__uint_as_float(a.x)))), s1 = rbf(x16_sum(row_sum(__uint_as_float(a.y))));
            const u32 r = pk_bf16(bf_lo(xo) + rbf(s0 * gate[0]), bf_hi(xo) + rbf(s1 * gate[1]));
            if (pi == 0) *(GAS u32*)(p.out + fb * DM + fc) = r;
            if (bid == 0 && lane == 0) __hip_atomic_store((GAS u64*)(p.ws + WS_EPOCH), (((u64)ep.y << 32) | ep.x) + 1, RLX_AGENT);
        }
    } else {
        const u16* kb = p.k + (size_t)pair * SK * HD;
        const u16* vb = p.v + (size_t)pair * SK * HD;
        const __amdgpu_buffer_rsrc_t rk = mk_rsrc(kb, SK * HD * 2), rv = mk_rsrc(vb, SK * HD * 2);
        constexpr u32 TSTR = NSW * 4096;
        const u32 kl = (n >> 2) * 256 + g * 64 + (n & 3) * 16, vl = lane * 16;
        u32 ko = (s * NS * NSW + w) * 4096 + kl, vo = (s * NS * NSW + w) * 4096 + vl;
        v4u R[3][4], KX[RP][4];
        u64 stub0, stub;
        asm volatile("s_getpc_b64 %0\n\ts_branch 1f\n\t"
                     "s_waitcnt vmcnt(4)\n\ts_setpc_b64 s[80:81]\n\ts_waitcnt vmcnt(5)\n\ts_setpc_b64 s[80:81]\n\t"
                     "s_waitcnt vmcnt(6)\n\ts_setpc_b64 s[80:81]\n\ts_waitcnt vmcnt(7)\n\ts_setpc_b64 s[80:81]\n1:" : "=s"(stub0));
        stub0 -= 36;
        stub = stub0 + 8 * 6;
        u32 due = t_start * 16 + T_OFF;
#define WAITW() asm volatile("s_swappc_b64 s[80:81], %0" ::"s"(stub) : "memory", "s80", "s81")
#define CLOCK() const u32 now_ = (u32)__builtin_amdgcn_s_memrealtime(); __builtin_amdgcn_sched_barrier(0)
#define PACE2(tau, lo)                                            \
    do {                                                                                                       \
        due += (tau);                                                                                          \
        const int win = __builtin_amdgcn_readfirstlane(min(max(6 + (((int)(now_ * 16 - due) + 512) >> 10), lo), 8)); \
        stub = stub0 + 8 * win;                                                                                \
        __builtin_amdgcn_sched_barrier(0);                                                                     \
    } while (0)
#define PACE(tau) PACE2(tau, 5)
#define LK(X, u) do { WAITW(); X[u] = __builtin_amdgcn_raw_buffer_load_b128(rk, ko + 1024 * (u), 0, AUX_NT); __builtin_amdgcn_sched_barrier(0); } while (0)
#define LV(X, u) do { WAITW(); X[u] = __builtin_amdgcn_raw_buffer_load_b128(rv, vo + 1024 * (u), 0, AUX_NT); __builtin_amdgcn_sched_barrier(0); } while (0)
#define LK4(X) do { LK(X, 0); LK(X, 1); LK(X, 2); LK(X, 3); ko += TSTR; } while (0)
#define LV4(X) do { LV(X, 0); LV(X, 1); LV(X, 2); LV(X, 3); vo += TSTR; } while (0)
        const u32 park = lb + L_PARK + w * LP * 4096;
#pragma unroll
        for (int t = 0; t < LP; ++t) {
            const unsigned char* ks = (const unsigned char*)kb + ko;
            WAITW(); glds_nt_off<0>(ks, park + t * 4096);
            WAITW(); glds_nt_off<1024>(ks, park + t * 4096);
            WAITW(); glds_nt_off<2048>(ks, park + t * 4096);
            WAITW(); glds_nt_off<3072>(ks, park + t * 4096);
            ko += TSTR;
            CLOCK(); PACE2(T_K, 6);
        }
#pragma unroll
        for (int t = 0; t < RP; ++t) { LK4(KX[t]); CLOCK(); PACE2(T_K, 6); }
#pragma unroll
        for (int t = 0; t < 2; ++t) { LK4(R[t]); CLOCK(); PACE(T_K); }
        wg_barrier();
        due += (__builtin_amdgcn_readfirstlane(*(const LAS u32*)(L + L_CNT + 4)) - t_start) * 16;
        const int jj = n & 3;
        const bool j0 = jj & 1, j1 = jj & 2;
        const v4u Q = *(const LAS v4u*)(L + L_Q + (n >> 2) * 256 + 64 * g + 16 * jj);
        const u32 mask0 = jj == 0 ? 0x0000ffffu : jj == 1 ? 0xffff0000u : 0u, mask1 = jj == 2 ? 0x0000ffffu : jj == 3 ? 0xffff0000u : 0u;
        float m = -INFINITY, l = 0.f;
        f32x4 acc0 = {0.f, 0.f, 0.f, 0.f}, acc1 = {0.f, 0.f, 0.f, 0.f};
        LAS float* scp = (LAS float*)(L + L_SC + w * 8192) + lane;
#define KP(KR, idx)                                                                                            \
    do {                                                                                                       \
        f32x4 dd[4]; float sc[4];                                                                              \
        _Pragma("unroll") for (int u = 0; u < 4; ++u) dd[u] = mfma32(KR[u], Q, f32x4{0.f, 0.f, 0.f, 0.f});     \
        asm volatile("" : "+v"(dd[0]), "+v"(dd[1]), "+v"(dd[2]), "+v"(dd[3]));     \
        _Pragma("unroll") for (int u = 0; u < 4; ++u) sc[u] = sel4(dd[u], jj);                                 \
        const float y01 = (j0 ? sc[1] : sc[0]) + dpp<XOR1>(j0 ? sc[0] : sc[1]), y23 = (j0 ? sc[3] : sc[2]) + dpp<XOR1>(j0 ? sc[2] : sc[3]); \
        const float y = (j1 ? y23 : y01) + dpp<XOR2>(j1 ? y01 : y23);                                          \
        m = fmaxf(m, y);                                                                                       \
        scp[(idx) * 64] = y;                                                                                   \
        __builtin_amdgcn_sched_barrier(0);                                                                     \
    } while (0)
#define UNPARK(t, KR)                                                                                          \
    do {                                                                                                       \
        const LAS unsigned char* pk = L + L_PARK + (w * LP + (t)) * 4096 + lane * 16;                          \
        _Pragma("unroll") for (int u = 0; u < 4; ++u) KR[u] = *(const LAS v4u*)(pk + 1024 * u);                \
    } while (0)
#define VP(VR, sv)                                                                                             \
    do {                                                                                                       \
        const float p1 = ex2(fmaf(sv, SC, -mc));                                                               \
        l += p1;                                                                                               \
        const int pb = pk_bf16(p1, p1);                                                                        \
        _Pragma("unroll") for (int r = 0; r < 4; ++r) {                                                        \
            const u32 t_ = r == 0 ? __builtin_amdgcn_mov_dpp(pb, 0x00, 0xf, 0xf, true) : r == 1 ? __builtin_amdgcn_mov_dpp(pb, 0x55, 0xf, 0xf, true) \
                         : r == 2 ? __builtin_amdgcn_mov_dpp(pb, 0xAA, 0xf, 0xf, true) : __builtin_amdgcn_mov_dpp(pb, 0xFF, 0xf, 0xf, true); \
            acc0 = mfma16(VR[r].x, VR[r].y, t_ & mask0, t_ & mask1, acc0);                                     \
            acc1 = mfma16(VR[r].z, VR[r].w, t_ & mask0, t_ & mask1, acc1);                                     \
        }                                                                                                      \
        __builtin_amdgcn_sched_barrier(0);                                                                     \
    } while (0)
#define UKK(u, idx) do { LK4(R[((u) + 2) % 3]); CLOCK(); KP(R[(u) % 3], idx); PACE(T_K); } while (0)
#define UVK(u, idx) do { LV4(R[((u) + 2) % 3]); CLOCK(); KP(R[(u) % 3], idx); PACE(T_V); } while (0)
#define UVV(u, idx) do { float sv_ = scp[(idx) * 64]; LV4(R[((u) + 2) % 3]); asm volatile("" : "+v"(sv_)); CLOCK(); VP(R[(u) % 3], sv_); PACE(T_V); } while (0)
#define UKB(u, XA, ia, XB, ib, PRE)                                                                            \
    do {                                                                                                       \
        LK(R[((u) + 2) % 3], 0); PRE; KP(XA, ia); LK(R[((u) + 2) % 3], 1); LK(R[((u) + 2) % 3], 2); KP(XB, ib); LK(R[((u) + 2) % 3], 3); ko += TSTR; \
        CLOCK(); KP(R[(u) % 3], 12 + (u)); PACE(T_K);                                                          \
    } while (0)
        static_assert(LP == 4 && RP == 8 && NBL == 6, "");
        UKB(0, KX[0], 4, KX[1], 5, (void)0);
        UKB(1, KX[2], 6, KX[3], 7, { UNPARK(0, KX[0]); UNPARK(1, KX[1]); });
        UKB(2, KX[0], 0, KX[1], 1, { UNPARK(2, KX[2]); UNPARK(3, KX[3]); });
        UKB(3, KX[2], 2, KX[3], 3, (void)0);
        UKB(4, KX[4], 8, KX[5], 9, (void)0);
        UKB(5, KX[6], 10, KX[7], 11, (void)0);
        scp += (12 + NBL) * 64;
#pragma unroll 1
        for (int it = 0; it < KA; ++it) { UKK(0, 0); UKK(1, 1); UKK(2, 2); scp += 3 * 64; }
        UVK(0, 0); UVK(1, 1);
        m = fmaxf(m, dpp<XOR1>(m)); m = fmaxf(m, dpp<XOR2>(m)); m = x32_max(x16_max(m));
        const float mc = m * SC;
        const int bb = w & 1, dh = w >> 1;
        u32 cnt_a = lb + L_CNT, cnt_v = lane == 0;
        u32 a_mw = L_MRG + ((w * 4 + (n >> 2)) * 132 + 32 * g + jj) * 4, a_lw = L_MML + (w * 4 + (n >> 2)) * 8;
        u32 a_mr = L_MRG + (w * 132 + 4 * (lane & 31)) * 4, a_lr = L_MML + w * 8;
        u32 o_pa = lane < 32 ? myslot + w * 512 + lane * 16 : OOR, o_pl = lane == 0 ? WS_ML + ((pair * 4 + w) * 16 + s) * 8 : OOR;
        u32 o_ga = WS_SLOT + ((bb * 8 + x8) * 16 + 4 * g) * SLOT + (hi & 3) * 512 + dh * 256 + n * 16;
        u32 o_gl = WS_ML + (((bb * 8 + x8) * 4 + (hi & 3)) * 16 + ((n + 4 * g) & 15)) * 8;
        u32 a_wo = L_WO + 32 * w + (8 * g + ((lane >> 2) & 3)) * 128 + 8 * jj, a_ow = L_O + bb * 256 + dh * 128 + n * 8 + g * 2;
        u32 a_or = L_O + (n & 1) * 256 + 16 * g, o_ps = n < 2 ? myslot + SL_PS + n * 256 + w * 64 + g * 16 : OOR;
        asm volatile("" : "+v"(cnt_a), "+v"(cnt_v), "+v"(a_mw), "+v"(a_lw), "+v"(a_mr), "+v"(a_lr), "+v"(o_pa), "+v"(o_pl));
        const u16* wo_p = p.wo + (size_t)(hi * 128 + 32 * w + (lane >> 3)) * DM + 64 * cg + 8 * (lane & 7);
        asm volatile("" : "+v"(o_ga), "+v"(o_gl), "+v"(a_wo), "+v"(a_ow), "+v"(a_or), "+v"(o_ps), "+v"(wo_p));
        scp -= (NS - 2) * 64;
#pragma unroll 1
        for (int it = 0; it < VA; ++it) { UVV(2, 0); UVV(3, 1); UVV(4, 2); scp += 3 * 64; }
        UVV(2, 0); UVV(3, 1);
        asm volatile("ds_add_u32 %0, %1" ::"v"(cnt_a), "v"(cnt_v) : "memory");
        {
            const float s0 = scp[2 * 64], s1 = scp[3 * 64], s2 = scp[4 * 64];
            LV4(R[0]);
            VP(R[1], s0); VP(R[2], s1); VP(R[0], s2);
            l += dpp<XOR1>(l); l += dpp<XOR2>(l);
        }
        {
            LAS float* mr = (LAS float*)(L + a_mw);
#pragma unroll
            for (int r = 0; r < 4; ++r) { mr[8 * r] = acc0[r]; mr[8 * r + 4] = acc1[r]; }
            *(LAS f32x2*)(L + a_lw) = f32x2{mc, x32_sum(x16_sum(l))};
        }
#pragma unroll
        for (int u = 0; u < 4; ++u) glds_nt(wo_p + (size_t)u * 8 * DM, lb + L_WO + (4 * w + u) * 1024);
        wg_barrier();
        {
            f32x2 ml[NSW]; f32x4 ov[NSW];
#pragma unroll
            for (int u = 0; u < NSW; ++u) ml[u] = *(const LAS f32x2*)(L + a_lr + u * 32);
#pragma unroll
            for (int u = 0; u < NSW; ++u) ov[u] = *(const LAS f32x4*)(L + a_mr + u * (4 * 132 * 4));
            const float M = fmaxf(fmaxf(ml[0].x, ml[1].x), fmaxf(ml[2].x, ml[3].x));
            float f[NSW];
#pragma unroll
            for (int u = 0; u < NSW; ++u) f[u] = ex2(ml[u].x - M);
            const f32x4 o = (f[0] * ov[0] + f[1] * ov[1]) + (f[2] * ov[2] + f[3] * ov[3]);
            __builtin_amdgcn_raw_buffer_store_b128(clean4(o), rws, o_pa, 0, AUX_SC1);
            const float Ls = fmaf(f[0], ml[0].y, f[1] * ml[1].y) + fmaf(f[2], ml[2].y, f[3] * ml[3].y);
            __builtin_amdgcn_raw_buffer_store_b64(v2u{no_sent(M), no_sent(Ls)}, rws, o_pl, 0, AUX_SC1);
        }
        v4u WO[4];
        {
            v4u a[4]; v2u q2;
#define GATHER()                                                                                               \
    do {                                                                                                       \
        q2 = __builtin_amdgcn_raw_buffer_load_b64(rws, o_gl, 0, AUX_SC1);                                      \
        _Pragma("unroll") for (int k = 0; k < 4; ++k) a[k] = __builtin_amdgcn_raw_buffer_load_b128(rws, o_ga, k * SLOT, AUX_SC1); \
    } while (0)
#define ARRIVED() __all(max(max(max(umax4(a[0]), umax4(a[1])), max(umax4(a[2]), umax4(a[3]))), max(q2.x, q2.y)) != SENT)
            wait_vm<2>();
            wg_barrier();
            GATHER();
            __builtin_amdgcn_sched_barrier(0);
#pragma unroll
            for (int u = 0; u < 4; ++u) {
                const v2u lo = tr_read(L + a_wo + 4096 * u), hi2 = tr_read(L + a_wo + 4096 * u + 512);
                WO[u] = v4u{lo.x, lo.y, hi2.x, hi2.y};
            }
            __builtin_amdgcn_sched_barrier(0);
            if (!ARRIVED()) {
#pragma unroll 1
                for (u32 spin = 0; spin < SPIN_CAP; ++spin) {
                    asm volatile("" ::: "memory");
                    GATHER();
                    if (ARRIVED()) break;
                }
            }
            const float mp = __uint_as_float(q2.x);
            const float M = row_max(mp);
            const float wt = ex2(mp - M);
            const float inv = __builtin_amdgcn_rcpf(row_sum(wt * __uint_as_float(q2.y)));
            const f32x4 o = (dpp<0x150>(wt) * __builtin_bit_cast(f32x4, a[0]) + dpp<0x151>(wt) * __builtin_bit_cast(f32x4, a[1])) +
                            (dpp<0x152>(wt) * __builtin_bit_cast(f32x4, a[2]) + dpp<0x153>(wt) * __builtin_bit_cast(f32x4, a[3]));
            const auto s02 = __builtin_amdgcn_permlane32_swap(__float_as_uint(o[0]), __float_as_uint(o[2]), false, false);
            const auto s13 = __builtin_amdgcn_permlane32_swap(__float_as_uint(o[1]), __float_as_uint(o[3]), false, false);
            const float xs = __uint_as_float(s02[0]) + __uint_as_float(s02[1]), ys = __uint_as_float(s13[0]) + __uint_as_float(s13[1]);
            const auto sz = __builtin_amdgcn_permlane16_swap(__float_as_uint(xs), __float_as_uint(ys), false, false);
            const float z = (__uint_as_float(sz[0]) + __uint_as_float(sz[1])) * inv;
            *(LAS u16*)(L + a_ow) = (u16)pk_bf16(z, z);
        }
        wg_barrier();
        {
            v4u ob[4];
#pragma unroll
            for (int u = 0; u < 4; ++u) ob[u] = *(const LAS v4u*)(L + a_or + 64 * u);
            __builtin_amdgcn_sched_barrier(0);
            const f32x4 z = {0.f, 0.f, 0.f, 0.f};
            const f32x4 sp = mfma32(WO[2], ob[2], mfma32(WO[0], ob[0], z)) + mfma32(WO[3], ob[3], mfma32(WO[1], ob[1], z));
            __builtin_amdgcn_raw_buffer_store_b128(clean4(sp), rws, o_ps, 0, AUX_SC1);
        }
    }
}

extern "C" void kernel_launch(void* const* d_in, const int* in_sizes, int n_in, void* d_out, int out_size, void* d_ws, size_t ws_size,
                              hipStream_t stream) {
    Params p;
    p.x = (const u16*)d_in[0]; p.k = (const u16*)d_in[1]; p.v = (const u16*)d_in[2]; p.wq = (const u16*)d_in[3]; p.wo = (const u16*)d_in[4];
    p.w1 = (const u16*)d_in[6]; p.b1 = (const u16*)d_in[7]; p.w2 = (const u16*)d_in[8]; p.b2 = (const u16*)d_in[9];
    p.out = (u16*)d_out; p.ws = (unsigned char*)d_ws;
    (void)hipFuncSetAttribute((const void*)fused_decode, hipFuncAttributeMaxDynamicSharedMemorySize, L_END);
    hipLaunchKernelGGL(fused_decode, dim3(NWG), dim3(NTHR), L_END, stream, p);
}
```

```cpp
#include <hip/hip_runtime.h>
#include <hip/hip_bf16.h>
#include <stdint.h>

#define GAS __attribute__((address_space(1)))
#define LAS __attribute__((address_space(3)))
typedef unsigned u32;
typedef unsigned long long u64;
typedef unsigned short u16;
typedef unsigned v4u __attribute__((ext_vector_type(4)));
typedef unsigned v2u __attribute__((ext_vector_type(2)));
typedef float f32x4 __attribute__((ext_vector_type(4)));
typedef float f32x2 __attribute__((ext_vector_type(2)));
typedef short s16x4 __attribute__((ext_vector_type(4)));
typedef __bf16 bf16x8 __attribute__((ext_vector_type(8)));
typedef __bf16 bf16x2 __attribute__((ext_vector_type(2)));

constexpr int DM = 512, HD = 128, SK = 32768, FF = 1792;
constexpr int NWG = 256, NSW = 4, NTHR = (NSW + 1) * 64;
constexpr int NS = SK / 16 / 16 / NSW;
constexpr int LP = 4, RP = 8, NPRO = LP + RP + 2;
constexpr int NBL = 6, KA = (NS - NPRO - NBL) / 3, VA = (NS - 2) / 3 - 1;
static_assert(NS == 32 && NPRO + NBL + 3 * KA == NS && 3 * VA + 5 == NS, "");
constexpr int T_OFF = 115 * 16, T_K = 922, T_V = 908;
constexpr int T_ORG = 45;
constexpr int WQ = 8, WW1 = 2, WW2 = 2;
constexpr float SC = 0.08838834764831845f * 1.4426950408889634f;
constexpr u32 SENT = 0xFFFFFFFFu;
constexpr u32 SPIN_CAP = 1u << 21;
constexpr u32 OOR = 0xFFFFFF00u;

constexpr u32 WS_EPOCH = 0;
constexpr u32 WS_QG = 4096;
constexpr u32 WS_HG = WS_QG + 8 * 2 * 512 * 8;
constexpr u32 WS_TG = 102400;
constexpr u32 WS_ML = 118784;
constexpr u32 WS_SLOT = 131072, SLOT = 4352;
constexpr u32 SL_PS = 2048, SL_PM = 2560, SL_FB = 3072;
constexpr u32 WS_END = WS_SLOT + NWG * SLOT;
static_assert(WS_HG + 2 * FF * 8 <= WS_TG && WS_TG + 256 * 8 <= WS_ML && WS_ML + 8192 <= WS_SLOT, "");

constexpr u32 L_WQ = 0, L_W1 = 0, L_WO = 16384, L_W2 = 32768;
constexpr u32 L_XN = 40960, L_Y = L_XN + 2048, L_Q = L_Y + 2048, L_H = L_Q + 1024, L_O = L_H + 256;
constexpr u32 L_MRG = L_O + 512;
constexpr u32 L_MML = L_MRG + NSW * 4 * 132 * 4;
constexpr u32 L_CNT = L_MML + NSW * 4 * 2 * 4;
constexpr u32 L_WT = L_CNT + 16;
constexpr u32 L_SC = 57344;
constexpr u32 L_PARK = L_SC + NSW * 8192;
constexpr u32 L_END = L_PARK + NSW * LP * 4096;
static_assert(L_WT + 128 <= L_SC && L_END > 80 * 1024 && L_END <= 160 * 1024, "more than half of a CU's LDS: one workgroup per CU");

struct Params {
    const u16 *x, *k, *v, *wq, *wo, *w1, *b1, *w2, *b2;
    u16* out;
    unsigned char* ws;
};

__device__ __forceinline__ f32x4 mfma32(v4u a, v4u b, f32x4 c) {
    return __builtin_amdgcn_mfma_f32_16x16x32_bf16(__builtin_bit_cast(bf16x8, a), __builtin_bit_cast(bf16x8, b), c, 0, 0, 0);
}
__device__ __forceinline__ f32x4 mfma16(u32 a0, u32 a1, u32 b0, u32 b1, f32x4 c) {
    return __builtin_amdgcn_mfma_f32_16x16x16bf16_1k(__builtin_bit_cast(s16x4, v2u{a0, a1}), __builtin_bit_cast(s16x4, v2u{b0, b1}), c, 0, 0, 0);
}
__device__ __forceinline__ u32 pk_bf16(float lo, float hi) {
    return __builtin_bit_cast(u32, __builtin_convertvector(f32x2{lo, hi}, bf16x2));
}
__device__ __forceinline__ float bf_lo(u32 w) { return __builtin_bit_cast(float, w << 16); }
__device__ __forceinline__ float bf_hi(u32 w) { return __builtin_bit_cast(float, w & 0xffff0000u); }
__device__ __forceinline__ float rbf(float f) { return bf_lo(pk_bf16(f, f)); }
__device__ __forceinline__ float ex2(float x) { return __builtin_amdgcn_exp2f(x); }
template <int CTRL> __device__ __forceinline__ float dpp(float x) {
    return __builtin_bit_cast(float, __builtin_amdgcn_mov_dpp(__builtin_bit_cast(int, x), CTRL, 0xf, 0xf, true));
}
constexpr int XOR1 = 0xB1, XOR2 = 0x4E, XOR7 = 0x141, ROR8 = 0x128;
__device__ __forceinline__ float x16_max(float x) {
    auto s = __builtin_amdgcn_permlane16_swap(__float_as_uint(x), __float_as_uint(x), false, false);
    return fmaxf(__uint_as_float(s[0]), __uint_as_float(s[1]));
}
__device__ __forceinline__ float x32_max(float x) {
    auto s = __builtin_amdgcn_permlane32_swap(__float_as_uint(x), __float_as_uint(x), false, false);
    return fmaxf(__uint_as_float(s[0]), __uint_as_float(s[1]));
}
__device__ __forceinline__ float x16_sum(float x) {
    auto s = __builtin_amdgcn_permlane16_swap(__float_as_uint(x), __float_as_uint(x), false, false);
    return __uint_as_float(s[0]) + __uint_as_float(s[1]);
}
__device__ __forceinline__ float x32_sum(float x) {
    auto s = __builtin_amdgcn_permlane32_swap(__float_as_uint(x), __float_as_uint(x), false, false);
    return __uint_as_float(s[0]) + __uint_as_float(s[1]);
}
__device__ __forceinline__ float row_sum(float x) {
    x += dpp<XOR1>(x); x += dpp<XOR2>(x); x += dpp<XOR7>(x); x += dpp<ROR8>(x); return x;
}
__device__ __forceinline__ float row_max(float x) {
    float r;
    asm("s_nop 1\n\tv_max_f32_dpp %0, %1, %1 quad_perm:[1,0,3,2] row_mask:0xf bank_mask:0xf\n\ts_nop 1\n\t"
        "v_max_f32_dpp %0, %0, %0 quad_perm:[2,3,0,1] row_mask:0xf bank_mask:0xf\n\ts_nop 1\n\t"
        "v_max_f32_dpp %0, %0, %0 row_half_mirror row_mask:0xf bank_mask:0xf\n\ts_nop 1\n\t"
        "v_max_f32_dpp %0, %0, %0 row_ror:8 row_mask:0xf bank_mask:0xf" : "=&v"(r) : "v"(x));
    return r;
}
__device__ __forceinline__ float wave_sum(float x) { return x32_sum(x16_sum(row_sum(x))); }
__device__ __forceinline__ u32 umax4(v4u a) { return max(max(a.x, a.y), max(a.z, a.w)); }
__device__ __forceinline__ u32 no_sent(float f) { return min(__float_as_uint(f), 0xFFFFFFFEu); }

#define RLX_AGENT __ATOMIC_RELAXED, __HIP_MEMORY_SCOPE_AGENT
constexpr int AUX_NT = 2, AUX_SC1 = 16;
__device__ __forceinline__ __amdgpu_buffer_rsrc_t mk_rsrc(const void* p, u32 bytes) {
    return __builtin_amdgcn_make_buffer_rsrc((void*)p, 0, bytes, 0x00020000);
}
__device__ __forceinline__ void glds_nt(const void* src, u32 dst) {
    asm volatile("s_mov_b32 m0, %1\n\ts_nop 0\n\tglobal_load_lds_dwordx4 %0, off nt" ::"v"(src), "s"(dst) : "memory");
}
__device__ __forceinline__ void glds_sh(const void* src, u32 dst) {
    asm volatile("s_mov_b32 m0, %1\n\ts_nop 0\n\tglobal_load_lds_dwordx4 %0, off" ::"v"(src), "s"(dst) : "memory");
}
template <int OFF> __device__ __forceinline__ void glds_nt_off(const void* src, u32 dst) {
    asm volatile("s_mov_b32 m0, %1\n\ts_nop 0\n\tglobal_load_lds_dwordx4 %0, off offset:%2 nt" ::"v"(src), "s"(dst), "i"(OFF) : "memory");
}
template <int N> __device__ __forceinline__ void wait_vm() { asm volatile("s_waitcnt vmcnt(%0)" ::"i"(N) : "memory"); }
__device__ __forceinline__ void wg_barrier() { asm volatile("s_waitcnt lgkmcnt(0)\n\ts_barrier" ::: "memory"); }
__device__ __forceinline__ v2u tr_read(const LAS unsigned char* p) {
    return __builtin_bit_cast(v2u, __builtin_amdgcn_ds_read_tr16_b64_v4i16((LAS s16x4*)p));
}
__device__ __forceinline__ v4u tr_frag(const LAS unsigned char* img, int pitch, int row0, int lane) {
    const int g = lane >> 4, q = (lane >> 2) & 3, p = lane & 3;
    const LAS unsigned char* a = img + (row0 + 8 * g + q) * pitch + 8 * p;
    const v2u lo = tr_read(a), hi = tr_read(a + 4 * pitch);
    return v4u{lo.x, lo.y, hi.x, hi.y};
}

__device__ __forceinline__ void load_act(v4u (&B)[16], const LAS unsigned char* act, int lane) {
    const LAS unsigned char* ap = act + (lane & 1) * 1024 + (lane >> 4) * 16;
#pragma unroll
    for (int s = 0; s < 16; ++s) B[s] = *(const LAS v4u*)(ap + 64 * s);
}
__device__ __forceinline__ f32x4 gemv16(const LAS unsigned char* img, const v4u (&B)[16], int lane) {
    v4u A[16];
#pragma unroll
    for (int s = 0; s < 16; ++s) A[s] = tr_frag(img, 32, 32 * s, lane);
    __builtin_amdgcn_sched_barrier(0);
    f32x4 a[4];
#pragma unroll
    for (int s = 0; s < 16; ++s) a[s & 3] = mfma32(A[s], B[s], s < 4 ? f32x4{0.f, 0.f, 0.f, 0.f} : a[s & 3]);
    return (a[0] + a[1]) + (a[2] + a[3]);
}
__device__ __forceinline__ void layer_norm(float (&v)[2][8], LAS unsigned char* dst, int lane) {
    float mean[2], rs[2];
#pragma unroll
    for (int bb = 0; bb < 2; ++bb) mean[bb] = wave_sum(((v[bb][0] + v[bb][1]) + (v[bb][2] + v[bb][3])) + ((v[bb][4] + v[bb][5]) + (v[bb][6] + v[bb][7]))) * (1.f / DM);
#pragma unroll
    for (int bb = 0; bb < 2; ++bb) {
        float sq = 0.f;
#pragma unroll
        for (int u = 0; u < 8; ++u) { v[bb][u] -= mean[bb]; sq = fmaf(v[bb][u], v[bb][u], sq); }
        rs[bb] = __builtin_amdgcn_rsqf(wave_sum(sq) * (1.f / DM) + 1e-5f);
    }
#pragma unroll
    for (int bb = 0; bb < 2; ++bb) {
        v4u o;
#pragma unroll
        for (int u = 0; u < 4; ++u) {
            o[u] = pk_bf16(v[bb][2 * u] * rs[bb], v[bb][2 * u + 1] * rs[bb]);
            v[bb][2 * u] = bf_lo(o[u]); v[bb][2 * u + 1] = bf_hi(o[u]);
        }
        *(LAS v4u*)(dst + bb * 1024 + lane * 16) = o;
    }
}
__device__ __forceinline__ float sel4(f32x4 v, int r) {
    const bool b0 = r & 1, b1 = r & 2;
    const float lo = b0 ? v[1] : v[0], hi = b0 ? v[3] : v[2];
    return b1 ? hi : lo;
}
__device__ __forceinline__ v4u clean4(f32x4 v) { return v4u{no_sent(v[0]), no_sent(v[1]), no_sent(v[2]), no_sent(v[3])}; }

__global__ void __launch_bounds__(NTHR, 2) fused_decode(Params p) {
    extern __shared__ __attribute__((aligned(16))) unsigned char lds_raw[];
    LAS unsigned char* L = (LAS unsigned char*)lds_raw;
    const u32 lb = (u32)(size_t)L;
    const int tid = threadIdx.x, lane = tid & 63, w = __builtin_amdgcn_readfirstlane(tid >> 6);
    const int bid = blockIdx.x;
    const int pair = bid >> 4, b = pair >> 3, kvh = pair & 7, s = bid & 15;
    const int jq = 4 * (s & 7) + 2 * b + (s >> 3);
    const int m1i = (s - kvh) & 7, jm = 4 * (7 * (kvh >> 1) + 4 * (kvh & 1) + m1i) + (jq & 3);
    const int x8 = bid & 7, hi = 4 * x8 + (bid >> 6), cg = (bid >> 3) & 7;
    const bool do_m1 = m1i < 4 - (kvh & 1), do_m2 = hi < 28;
    const int g = lane >> 4, n = lane & 15;
    const __amdgpu_buffer_rsrc_t rws = mk_rsrc(p.ws, WS_END);
    const u32 myslot = WS_SLOT + bid * SLOT;

    const u32 t_start = (u32)__builtin_amdgcn_s_memrealtime();
    asm volatile("" ::"s"(p.k), "s"(p.v), "s"(p.x), "s"(p.wq), "s"(p.ws));
    if (w >= NSW) {
        __builtin_amdgcn_s_setprio(3);
        v4u xr0, xr1; v2u ep;
        {
            const void* xa = p.x + 8 * lane;
            const void* ea = p.ws + WS_EPOCH;
            asm volatile("global_load_dwordx4 %0, %3, off\n\tglobal_load_dwordx4 %1, %3, off offset:1024\n\tglobal_load_dwordx2 %2, %4, off sc1"
                         : "=&v"(xr0), "=&v"(xr1), "=&v"(ep) : "v"(xa), "v"(ea) : "memory");
        }
        {
            const v4u sv = {SENT, SENT, SENT, SENT};
#pragma unroll
            for (int u = 0; u < 3; ++u) __builtin_amdgcn_raw_buffer_store_b128(sv, rws, myslot + u * 1024 + lane * 16, 0, AUX_SC1);
            const u32 o4 = lane < 4 ? WS_ML + ((pair * 4 + lane) * 16 + s) * 8 : lane == 4 ? myslot + SL_FB : OOR;
            __builtin_amdgcn_raw_buffer_store_b64(v2u{SENT, SENT}, rws, o4, 0, AUX_SC1);
        }
        const int rot = 4 * (jq & 3);
        const u16* wqs = p.wq + (size_t)(lane >> 1) * 4096 + kvh * 512 + 16 * jq + 8 * (lane & 1);
#pragma unroll
        for (int u = 0; u < WQ; ++u) { const int ur = (u + rot) & 15; glds_sh(wqs + (size_t)ur * 32 * 4096, lb + L_WQ + ur * 1024); }
        asm volatile("s_waitcnt vmcnt(%3)" : "+v"(xr0), "+v"(xr1), "+v"(ep) : "i"(4 + WQ) : "memory");
#pragma unroll
        for (int u = WQ; u < 16; ++u) { const int ur = (u + rot) & 15; wait_vm<WQ - 1>(); glds_sh(wqs + (size_t)ur * 32 * 4096, lb + L_WQ + ur * 1024); }
        const u64 tag = ((((u64)ep.y << 32) | ep.x) + 1) & 0xFFFFFFFFFFFFull;
        float xv[2][8];
#pragma unroll
        for (int u = 0; u < 4; ++u) { xv[0][2 * u] = bf_lo(xr0[u]); xv[0][2 * u + 1] = bf_hi(xr0[u]); xv[1][2 * u] = bf_lo(xr1[u]); xv[1][2 * u + 1] = bf_hi(xr1[u]); }
        layer_norm(xv, L + L_XN, lane);
        {
            v4u B[16];
            load_act(B, L + L_XN, lane);
            __builtin_amdgcn_sched_barrier(0);
            wait_vm<0>();
            const f32x4 qv = gemv16(L + L_WQ, B, lane);
            const u32 qb = pk_bf16(sel4(qv, n >> 1), 0.f) & 0xffffu;
            GAS u64* dst = (GAS u64*)(p.ws + WS_QG) + (kvh * 2 + (n & 1)) * 512 + 16 * jq + 4 * g + (n >> 1);
            if (n < 8) __hip_atomic_store(dst, (tag << 16) | qb, RLX_AGENT);
        }
        {
            GAS u64* src = (GAS u64*)(p.ws + WS_QG) + (kvh * 2 + b) * 512 + lane;
            GAS u64* tsrc = (GAS u64*)(p.ws + WS_TG) + kvh * 32 + (lane & 31);
            u64 A[8], T;
            __builtin_amdgcn_s_sleep(60);
#pragma unroll 1
            for (u32 spin = 0; spin < SPIN_CAP; ++spin) {
                bool ok = true;
#pragma unroll
                for (int k = 0; k < 8; ++k) A[k] = __hip_atomic_load(src + 64 * k, RLX_AGENT);
                T = __hip_atomic_load(tsrc, RLX_AGENT);
#pragma unroll
                for (int k = 0; k < 8; ++k) ok &= (A[k] >> 16) == tag;
                ok &= (T >> 16) == tag;
                if (__all(ok)) break;
                __builtin_amdgcn_s_sleep(24);
            }
#pragma unroll
            for (int k = 0; k < 8; ++k) *(LAS u16*)(L + L_Q + (64 * k + lane) * 2) = (u16)A[k];
            const float mean = x16_sum(row_sum((float)(short)((u32)T - t_start))) * (1.f / 32.f);
            *(LAS v4u*)(L + L_CNT) = v4u{0u, t_start + (int)mean + T_ORG, 0u, 0u};
        }
        wg_barrier();
        __builtin_amdgcn_s_setprio(1);
        layer_norm(xv, L + L_Y, lane);
        const int fb = bid >> 7, fq = (bid >> 3) & 15, hf = lane >> 5, fc = 64 * x8 + 4 * fq + 2 * hf;
        const u32 xo = *(const GAS u32*)(p.x + fb * DM + fc), b2o = *(const GAS u32*)(p.b2 + fc);
        const int col1 = do_m1 ? 16 * jm + 4 * g + ((n >> 1) & 3) : 0;
        const float bias1 = bf_lo(p.b1[col1]);
        if (do_m1) {
            const u16* s1 = p.w1 + (size_t)(lane >> 1) * FF + 16 * jm + 8 * (lane & 1);
#pragma unroll
            for (int u = 0; u < 16; ++u) { const int ur = (u + rot) & 15; wait_vm<WW1 - 1>(); glds_sh(s1 + (size_t)ur * 32 * FF, lb + L_W1 + ur * 1024); }
        }
        if (do_m2) {
            const u16* s2 = p.w2 + (size_t)(hi * 64 + (lane >> 3)) * DM + 64 * cg + 8 * (lane & 7);
#pragma unroll
            for (int u = 0; u < 8; ++u) { wait_vm<WW2 - 1>(); glds_nt(s2 + (size_t)u * 8 * DM, lb + L_W2 + u * 1024); }
        }
        wait_vm<0>();
        if (do_m1) {
            v4u B[16];
            load_act(B, L + L_Y, lane);
            const float u = rbf(rbf(sel4(gemv16(L + L_W1, B, lane), n >> 1)) + bias1);
            const float z = 1.5957691216057308f * fmaf(0.044715f * u * u, u, u);
            const float h = u / (1.f + __expf(-z));
            if (n < 8) __hip_atomic_store((GAS u64*)(p.ws + WS_HG) + (n & 1) * FF + col1, (tag << 16) | (pk_bf16(h, 0.f) & 0xffffu), RLX_AGENT);
        }
        const int pi = lane & 31, pw = ((pi & 3) * 8 + x8) * 8 + (pi >> 2);
        const u32 pslot = WS_SLOT + pw * SLOT + fb * 256 + fq * 16 + hf * 8;
        {
            const int ek = hf ? (pw >> 4) & 7 : x8, ej = hf ? 4 * (pw & 7) + 2 * (pw >> 7) + ((pw >> 3) & 1) : pi;
            GAS u64* ev = (GAS u64*)(p.ws + WS_QG) + ek * 1024 + 16 * ej;
            GAS u64* hs = do_m2 ? (GAS u64*)(p.ws + WS_HG) + 64 * hi + lane : ev;
            const int hstr = do_m2 ? FF : 0;
            u64 e, h0, h1;
            if (!do_m1) { __builtin_amdgcn_s_sleep(64); __builtin_amdgcn_s_sleep(64); }
            __builtin_amdgcn_s_sleep(64);
#pragma unroll 1
            for (u32 spin = 0; spin < SPIN_CAP; ++spin) {
                e = __hip_atomic_load(ev, RLX_AGENT); h0 = __hip_atomic_load(hs, RLX_AGENT); h1 = __hip_atomic_load(hs + hstr, RLX_AGENT);
                if (__all(((e >> 16) == tag) & ((h0 >> 16) == tag) & ((h1 >> 16) == tag))) break;
                __builtin_amdgcn_s_sleep(64);
            }
            *(LAS u16*)(L + L_H + lane * 2) = (u16)h0;
            *(LAS u16*)(L + L_H + 128 + lane * 2) = (u16)h1;
        }
        if (do_m2) {
#pragma unroll
            for (int cb = 0; cb < 4; ++cb) {
                f32x4 mp = {0.f, 0.f, 0.f, 0.f};
#pragma unroll
                for (int u = 0; u < 2; ++u) mp = mfma32(tr_frag(L + L_W2 + 32 * cb, 128, 32 * u, lane), *(const LAS v4u*)(L + L_H + (n & 1) * 128 + 64 * u + 16 * g), mp);
                __builtin_amdgcn_raw_buffer_store_b128(clean4(mp), rws, n < 2 ? myslot + SL_PM + n * 256 + cb * 64 + g * 16 : OOR, 0, AUX_SC1);
            }
        }
        float gate[2];
        {
            v2u a;
            __builtin_amdgcn_s_sleep(64);
#pragma unroll 1
            for (u32 spin = 0; spin < SPIN_CAP; ++spin) {
                asm volatile("" ::: "memory");
                a = __builtin_amdgcn_raw_buffer_load_b64(rws, pi < 28 ? pslot + SL_PM : OOR, 0, AUX_SC1);
                if (__all(max(a.x, a.y) != SENT)) break;
                __builtin_amdgcn_s_sleep(64);
            }
#pragma unroll
            for (int k = 0; k < 2; ++k) {
                const float mm = rbf(rbf(x16_sum(row_sum(__uint_as_float(a[k])))) + (k ? bf_hi(b2o) : bf_lo(b2o)));
                gate[k] = rbf(mm / (1.f + __expf(-mm)));
            }
        }
        float x0 = bf_lo(xo), x1 = bf_hi(xo);
        GAS u32* outp = (GAS u32*)(p.out + fb * DM + fc);
        asm volatile("" : "+v"(gate[0]), "+v"(gate[1]), "+v"(x0), "+v"(x1), "+v"(outp));
        __builtin_amdgcn_s_setprio(3);
        u32 mlo = lane < 4 ? WS_ML + ((pair * 4 + lane) * 16 + s) * 8 : OOR, fme = lane == 0 ? myslot + SL_FB : OOR;
        u32 mli = lane < 32 ? WS_ML + ((((lane >> 4) * 8 + x8) * 4 + (hi & 3)) * 16 + (lane & 15)) * 8 : OOR;
        u32 fbo = lane < 32 ? WS_SLOT + pw * SLOT + SL_FB : OOR, pso = pslot + SL_PS, epo = bid == 0 && lane == 0 ? WS_EPOCH : OOR;
        const u64 en = (((u64)ep.y << 32) | ep.x) + 1;
        v2u env = {(u32)en, (u32)(en >> 32)};
        asm volatile("" : "+v"(mlo), "+v"(mli), "+v"(fme), "+v"(fbo), "+v"(pso), "+v"(epo), "+v"(env));
#pragma unroll 1
        for (u32 spin = 0; spin < 16 * SPIN_CAP; ++spin) {
            if (__hip_atomic_load((LAS u32*)(L + L_CNT), __ATOMIC_RELAXED, __HIP_MEMORY_SCOPE_WORKGROUP) >= NSW) break;
            __builtin_amdgcn_s_sleep(1);
        }
        asm volatile("" ::: "memory");
        {
            f32x2 ml[NSW];
#pragma unroll
            for (int u = 0; u < NSW; ++u) ml[u] = *(const LAS f32x2*)(L + L_MML + (u * 4 + (lane & 3)) * 8);
            const float M = fmaxf(fmaxf(ml[0].x, ml[1].x), fmaxf(ml[2].x, ml[3].x));
            __builtin_amdgcn_raw_buffer_store_b64(v2u{no_sent(M), no_sent((ml[0].y + ml[1].y) + (ml[2].y + ml[3].y))}, rws, mlo, 0, AUX_SC1);
        }
#define WEIGHTS()                                                \
    do {                                                                                                       \
        const float mp = __uint_as_float(q2.x), wt = ex2(mp - row_max(mp));                                    \
        const float wn = wt * __builtin_amdgcn_rcpf(row_sum(wt * __uint_as_float(q2.y)));                      \
        if (lane < 32) *(LAS float*)(L + L_WT + lane * 4) = wn;                                                \
    } while (0)
        __builtin_amdgcn_s_sleep(36);
        v2u q2 = __builtin_amdgcn_raw_buffer_load_b64(rws, mli, 0, AUX_SC1);
        const bool have = __all(max(q2.x, q2.y) != SENT);
        if (have) WEIGHTS();
        wg_barrier();
        if (!have) {
#pragma unroll 1
            for (u32 spin = 0; spin < SPIN_CAP; ++spin) {
                asm volatile("" ::: "memory");
                q2 = __builtin_amdgcn_raw_buffer_load_b64(rws, mli, 0, AUX_SC1);
                if (__all(max(q2.x, q2.y) != SENT)) break;
                __builtin_amdgcn_s_sleep(1);
            }
            WEIGHTS();
        }
        wg_barrier();
        __builtin_amdgcn_s_sleep(5);
        __builtin_amdgcn_raw_buffer_store_b32(1u, rws, fme, 0, AUX_SC1);
        wg_barrier();
#pragma unroll 1
        for (u32 spin = 0; spin < SPIN_CAP; ++spin) {
            asm volatile("" ::: "memory");
            if (__all(__builtin_amdgcn_raw_buffer_load_b32(rws, fbo, 0, AUX_SC1) != SENT)) break;
            __builtin_amdgcn_s_sleep(1);
        }
        {
            v2u a = __builtin_amdgcn_raw_buffer_load_b64(rws, pso, 0, AUX_SC1);
            __builtin_amdgcn_raw_buffer_store_b64(env, rws, epo, 0, AUX_SC1);
            __builtin_amdgcn_sched_barrier(0);
            if (!__all(max(a.x, a.y) != SENT)) {
#pragma unroll 1
                for (u32 spin = 0; spin < SPIN_CAP; ++spin) {
                    asm volatile("" ::: "memory");
                    a = __builtin_amdgcn_raw_buffer_load_b64(rws, pso, 0, AUX_SC1);
                    if (__all(max(a.x, a.y) != SENT)) break;
                }
            }
            const float s0 = rbf(x16_sum(row_sum(__uint_as_float(a.x)))), s1 = rbf(x16_sum(row_sum(__uint_as_float(a.y))));
            const u32 r = pk_bf16(x0 + rbf(s0 * gate[0]), x1 + rbf(s1 * gate[1]));
            if (pi == 0) *outp = r;
        }
    } else {
        const u16* kb = p.k + (size_t)pair * SK * HD;
        const u16* vb = p.v + (size_t)pair * SK * HD;
        const __amdgpu_buffer_rsrc_t rk = mk_rsrc(kb, SK * HD * 2), rv = mk_rsrc(vb, SK * HD * 2);
        constexpr u32 TSTR = NSW * 4096;
        const u32 kl = (n >> 2) * 256 + g * 64 + (n & 3) * 16, vl = lane * 16;
        u32 ko = (s * NS * NSW + w) * 4096 + kl, vo = (s * NS * NSW + w) * 4096 + vl;
        v4u R[3][4], KX[RP][4];
        u64 stub0, stub;
        asm volatile("s_getpc_b64 %0\n\ts_branch 1f\n\t"
                     "s_waitcnt vmcnt(4)\n\ts_setpc_b64 s[80:81]\n\ts_waitcnt vmcnt(5)\n\ts_setpc_b64 s[80:81]\n\t"
                     "s_waitcnt vmcnt(6)\n\ts_setpc_b64 s[80:81]\n\ts_waitcnt vmcnt(7)\n\ts_setpc_b64 s[80:81]\n1:" : "=s"(stub0));
        stub0 -= 36;
        stub = stub0 + 8 * 6;
        u32 due = t_start * 16 + T_OFF;
        int late;
#define WAITW() asm volatile("s_swappc_b64 s[80:81], %0" ::"s"(stub) : "memory", "s80", "s81")
#define CLOCK() const u32 now_ = (u32)__builtin_amdgcn_s_memrealtime(); __builtin_amdgcn_sched_barrier(0)
#define PACE2(tau, lo)                                            \
    do {                                                                                                       \
        due += (tau);                                                                                          \
        late = (int)(now_ * 16 - due);                                                                         \
        const int win = __builtin_amdgcn_readfirstlane(min(max(6 + ((late + 512) >> 10), lo), 8));             \
        stub = stub0 + 8 * win;                                                                                \
        __builtin_amdgcn_sched_barrier(0);                                                                     \
    } while (0)
#define PACE(tau) PACE2(tau, 5)
#define LK(X, u) do { WAITW(); X[u] = __builtin_amdgcn_raw_buffer_load_b128(rk, ko + 1024 * (u), 0, AUX_NT); __builtin_amdgcn_sched_barrier(0); } while (0)
#define LV(X, u) do { WAITW(); X[u] = __builtin_amdgcn_raw_buffer_load_b128(rv, vo + 1024 * (u), 0, AUX_NT); __builtin_amdgcn_sched_barrier(0); } while (0)
#define LK4(X) do { LK(X, 0); LK(X, 1); LK(X, 2); LK(X, 3); ko += TSTR; } while (0)
#define LV4(X) do { LV(X, 0); LV(X, 1); LV(X, 2); LV(X, 3); vo += TSTR; } while (0)
        const bool rep = w == 1 && lane == 0;
        const v2u e2 = __builtin_amdgcn_raw_buffer_load_b64(rws, rep ? WS_EPOCH : OOR, 0, AUX_SC1);
        __builtin_amdgcn_sched_barrier(0);
        const u32 park = lb + L_PARK + w * LP * 4096;
#pragma unroll
        for (int t = 0; t < LP; ++t) {
            const unsigned char* ks = (const unsigned char*)kb + ko;
            WAITW(); glds_nt_off<0>(ks, park + t * 4096);
            WAITW(); glds_nt_off<1024>(ks, park + t * 4096);
            WAITW(); glds_nt_off<2048>(ks, park + t * 4096);
            WAITW(); glds_nt_off<3072>(ks, park + t * 4096);
            ko += TSTR;
            CLOCK(); PACE2(T_K, 6);
        }
#pragma unroll
        for (int t = 0; t < RP; ++t) {
            LK4(KX[t]); CLOCK(); PACE2(T_K, 6);
            if (t == 0) {
                const u64 gv = (((((u64)e2.y << 32) | e2.x) + 1) << 16) | ((t_start + (late >> 4)) & 0xffffu);
                __builtin_amdgcn_raw_buffer_store_b64(v2u{(u32)gv, (u32)(gv >> 32)}, rws, rep ? WS_TG + (kvh * 32 + jq) * 8 : OOR, 0, AUX_SC1);
                __builtin_amdgcn_sched_barrier(0);
            }
        }
#pragma unroll
        for (int t = 0; t < 2; ++t) { LK4(R[t]); CLOCK(); PACE(T_K); }
        wg_barrier();
        due += (__builtin_amdgcn_readfirstlane(*(const LAS u32*)(L + L_CNT + 4)) - t_start) * 16;
        const int jj = n & 3;
        const bool j0 = jj & 1, j1 = jj & 2;
        const v4u Q = *(const LAS v4u*)(L + L_Q + (n >> 2) * 256 + 64 * g + 16 * jj);
        const u32 mask0 = jj == 0 ? 0x0000ffffu : jj == 1 ? 0xffff0000u : 0u, mask1 = jj == 2 ? 0x0000ffffu : jj == 3 ? 0xffff0000u : 0u;
        float m = -INFINITY, l = 0.f;
        f32x4 acc0 = {0.f, 0.f, 0.f, 0.f}, acc1 = {0.f, 0.f, 0.f, 0.f};
        LAS float* scp = (LAS float*)(L + L_SC + w * 8192) + lane;
#define KP(KR, idx)                                                                                            \
    do {                                                                                                       \
        f32x4 dd[4]; float sc[4];                                                                              \
        _Pragma("unroll") for (int u = 0; u < 4; ++u) dd[u] = mfma32(KR[u], Q, f32x4{0.f, 0.f, 0.f, 0.f});     \
        asm volatile("" : "+v"(dd[0]), "+v"(dd[1]), "+v"(dd[2]), "+v"(dd[3]));     \
        _Pragma("unroll") for (int u = 0; u < 4; ++u) sc[u] = sel4(dd[u], jj);                                 \
        const float y01 = (j0 ? sc[1] : sc[0]) + dpp<XOR1>(j0 ? sc[0] : sc[1]), y23 = (j0 ? sc[3] : sc[2]) + dpp<XOR1>(j0 ? sc[2] : sc[3]); \
        const float y = (j1 ? y23 : y01) + dpp<XOR2>(j1 ? y01 : y23);                                          \
        m = fmaxf(m, y);                                                                                       \
        scp[(idx) * 64] = y;                                                                                   \
        __builtin_amdgcn_sched_barrier(0);                                                                     \
    } while (0)
#define UNPARK(t, KR)                                                                                          \
    do {                                                                                                       \
        const LAS unsigned char* pk = L + L_PARK + (w * LP + (t)) * 4096 + lane * 16;                          \
        _Pragma("unroll") for (int u = 0; u < 4; ++u) KR[u] = *(const LAS v4u*)(pk + 1024 * u);                \
    } while (0)
#define VP(VR, sv) do { const float p1 = ex2(fmaf(sv, SC, -mc)); l += p1; PV(VR, p1); } while (0)
#define PV(VR, p1)                                                                                             \
    do {                                                                                                       \
        const int pb = pk_bf16(p1, p1);                                                                        \
        _Pragma("unroll") for (int r = 0; r < 4; ++r) {                                                        \
            const u32 t_ = r == 0 ? __builtin_amdgcn_mov_dpp(pb, 0x00, 0xf, 0xf, true) : r == 1 ? __builtin_amdgcn_mov_dpp(pb, 0x55, 0xf, 0xf, true) \
                         : r == 2 ? __builtin_amdgcn_mov_dpp(pb, 0xAA, 0xf, 0xf, true) : __builtin_amdgcn_mov_dpp(pb, 0xFF, 0xf, 0xf, true); \
            acc0 = mfma16(VR[r].x, VR[r].y, t_ & mask0, t_ & mask1, acc0);                                     \
            acc1 = mfma16(VR[r].z, VR[r].w, t_ & mask0, t_ & mask1, acc1);                                     \
        }                                                                                                      \
        __builtin_amdgcn_sched_barrier(0);                                                                     \
    } while (0)
#define UKK(u, idx) do { LK4(R[((u) + 2) % 3]); CLOCK(); KP(R[(u) % 3], idx); PACE(T_K); } while (0)
#define UVK(u, idx) do { LV4(R[((u) + 2) % 3]); CLOCK(); KP(R[(u) % 3], idx); PACE(T_V); } while (0)
#define UVV(u, idx) do { float sv_ = scp[(idx) * 64]; LV4(R[((u) + 2) % 3]); asm volatile("" : "+v"(sv_)); CLOCK(); VP(R[(u) % 3], sv_); PACE(T_V); } while (0)
#define UKB(u, XA, ia, XB, ib, PRE)                                                                            \
    do {                                                                                                       \
        LK(R[((u) + 2) % 3], 0); PRE; KP(XA, ia); LK(R[((u) + 2) % 3], 1); LK(R[((u) + 2) % 3], 2); KP(XB, ib); LK(R[((u) + 2) % 3], 3); ko += TSTR; \
        CLOCK(); KP(R[(u) % 3], 12 + (u)); PACE(T_K);                                                          \
    } while (0)
        static_assert(LP == 4 && RP == 8 && NBL == 6, "");
        UKB(0, KX[0], 4, KX[1], 5, (void)0);
        UKB(1, KX[2], 6, KX[3], 7, { UNPARK(0, KX[0]); UNPARK(1, KX[1]); });
        UKB(2, KX[0], 0, KX[1], 1, { UNPARK(2, KX[2]); UNPARK(3, KX[3]); });
        UKB(3, KX[2], 2, KX[3], 3, (void)0);
        UKB(4, KX[4], 8, KX[5], 9, (void)0);
        UKB(5, KX[6], 10, KX[7], 11, (void)0);
        scp += (12 + NBL) * 64;
#pragma unroll 1
        for (int it = 0; it < KA; ++it) { UKK(0, 0); UKK(1, 1); UKK(2, 2); scp += 3 * 64; }
        UVK(0, 0); UVK(1, 1);
        m = fmaxf(m, dpp<XOR1>(m)); m = fmaxf(m, dpp<XOR2>(m)); m = x32_max(x16_max(m));
        float mc = m * SC;
        const int bb = w & 1, dh = w >> 1;
        u32 cnt_a = lb + L_CNT, cnt_v = lane == 0;
        u32 a_mw = L_MRG + ((w * 4 + (n >> 2)) * 132 + 32 * g + jj) * 4, a_ml = L_MML + (n >> 2) * 8;
        u32 a_mr = L_MRG + (w * 132 + 4 * (lane & 31)) * 4, o_pa = lane < 32 ? myslot + w * 512 + lane * 16 : OOR;
        u32 o_ga = WS_SLOT + ((bb * 8 + x8) * 16 + 4 * g) * SLOT + (hi & 3) * 512 + dh * 256 + n * 16, a_wt = L_WT + bb * 64 + g * 16;
        u32 a_wo = L_WO + 32 * w + (8 * g + ((lane >> 2) & 3)) * 128 + 8 * jj, a_ow = L_O + bb * 256 + dh * 128 + n * 8 + g * 2;
        u32 a_or = L_O + (n & 1) * 256 + 16 * g, o_ps = n < 2 ? myslot + SL_PS + n * 256 + w * 64 + g * 16 : OOR;
        const u16* wo_p = p.wo + (size_t)(hi * 128 + 32 * w + (lane >> 3)) * DM + 64 * cg + 8 * (lane & 7);
        asm volatile("" : "+v"(cnt_a), "+v"(cnt_v), "+v"(a_mw), "+v"(a_ml), "+v"(a_mr), "+v"(o_pa), "+v"(o_ga));
        asm volatile("" : "+v"(a_wt), "+v"(a_wo), "+v"(a_ow), "+v"(a_or), "+v"(o_ps), "+v"(wo_p));
        *(LAS float*)(L + a_ml + w * 32) = mc;
        asm volatile("ds_add_u32 %0, %1 offset:8" ::"v"(cnt_a), "v"(cnt_v) : "memory");
        scp -= (NS - 2) * 64;
#pragma unroll 1
        for (int it = 0; it < VA; ++it) { UVV(2, 0); UVV(3, 1); UVV(4, 2); scp += 3 * 64; }
        {
#pragma unroll 1
            for (u32 spin = 0; spin < 16 * SPIN_CAP; ++spin)
                if (__hip_atomic_load((LAS u32*)(L + L_CNT + 8), __ATOMIC_RELAXED, __HIP_MEMORY_SCOPE_WORKGROUP) >= NSW) break;
            asm volatile("" ::: "memory");
            float m4[NSW];
#pragma unroll
            for (int u = 0; u < NSW; ++u) m4[u] = *(const LAS float*)(L + a_ml + u * 32);
            const float M = fmaxf(fmaxf(m4[0], m4[1]), fmaxf(m4[2], m4[3])), f = ex2(mc - M);
            acc0 *= f; acc1 *= f; l *= f; mc = M;
            __builtin_amdgcn_sched_barrier(0);
        }
        {
            float pp[5];
#pragma unroll
            for (int u = 0; u < 5; ++u) pp[u] = ex2(fmaf(scp[u * 64], SC, -mc));
            l += ((pp[0] + pp[1]) + (pp[2] + pp[3])) + pp[4];
            l += dpp<XOR1>(l); l += dpp<XOR2>(l);
            *(LAS float*)(L + a_ml + w * 32 + 4) = x32_sum(x16_sum(l));
            asm volatile("ds_add_u32 %0, %1" ::"v"(cnt_a), "v"(cnt_v) : "memory");
            __builtin_amdgcn_sched_barrier(0);
            { LV4(R[1]); CLOCK(); PV(R[2], pp[0]); PACE(T_V); }
            { LV4(R[2]); CLOCK(); PV(R[0], pp[1]); PACE(T_V); }
            LV4(R[0]);
            PV(R[1], pp[2]); PV(R[2], pp[3]); PV(R[0], pp[4]);
        }
        {
            LAS float* mr = (LAS float*)(L + a_mw);
#pragma unroll
            for (int r = 0; r < 4; ++r) { mr[8 * r] = acc0[r]; mr[8 * r + 4] = acc1[r]; }
        }
#pragma unroll
        for (int u = 0; u < 4; ++u) glds_nt(wo_p + (size_t)u * 8 * DM, lb + L_WO + (4 * w + u) * 1024);
        wg_barrier();
        {
            f32x4 ov[NSW];
#pragma unroll
            for (int u = 0; u < NSW; ++u) ov[u] = *(const LAS f32x4*)(L + a_mr + u * (4 * 132 * 4));
            __builtin_amdgcn_raw_buffer_store_b128(clean4((ov[0] + ov[1]) + (ov[2] + ov[3])), rws, o_pa, 0, AUX_SC1);
        }
        v4u WO[4];
        {
            v4u a[4];
#define GATHER() _Pragma("unroll") for (int k = 0; k < 4; ++k) a[k] = __builtin_amdgcn_raw_buffer_load_b128(rws, o_ga, k * SLOT, AUX_SC1)
#define ARRIVED() __all(max(max(umax4(a[0]), umax4(a[1])), max(umax4(a[2]), umax4(a[3]))) != SENT)
            wait_vm<1>();
            wg_barrier();
            GATHER();
            __builtin_amdgcn_sched_barrier(0);
            const f32x4 wn = *(const LAS f32x4*)(L + a_wt);
#pragma unroll
            for (int u = 0; u < 4; ++u) {
                const v2u lo = tr_read(L + a_wo + 4096 * u), hi2 = tr_read(L + a_wo + 4096 * u + 512);
                WO[u] = v4u{lo.x, lo.y, hi2.x, hi2.y};
            }
            __builtin_amdgcn_sched_barrier(0);
            if (!ARRIVED()) {
#pragma unroll 1
                for (u32 spin = 0; spin < SPIN_CAP; ++spin) {
                    asm volatile("" ::: "memory");
                    GATHER();
                    if (ARRIVED()) break;
                }
            }
            const f32x4 o = (wn[0] * __builtin_bit_cast(f32x4, a[0]) + wn[1] * __builtin_bit_cast(f32x4, a[1])) +
                            (wn[2] * __builtin_bit_cast(f32x4, a[2]) + wn[3] * __builtin_bit_cast(f32x4, a[3]));
            const auto s02 = __builtin_amdgcn_permlane32_swap(__float_as_uint(o[0]), __float_as_uint(o[2]), false, false);
            const auto s13 = __builtin_amdgcn_permlane32_swap(__float_as_uint(o[1]), __float_as_uint(o[3]), false, false);
            const float xs = __uint_as_float(s02[0]) + __uint_as_float(s02[1]), ys = __uint_as_float(s13[0]) + __uint_as_float(s13[1]);
            const auto sz = __builtin_amdgcn_permlane16_swap(__float_as_uint(xs), __float_as_uint(ys), false, false);
            const float z = __uint_as_float(sz[0]) + __uint_as_float(sz[1]);
            *(LAS u16*)(L + a_ow) = (u16)pk_bf16(z, z);
        }
        wg_barrier();
        {
            v4u ob[4];
#pragma unroll
            for (int u = 0; u < 4; ++u) ob[u] = *(const LAS v4u*)(L + a_or + 64 * u);
            __builtin_amdgcn_sched_barrier(0);
            const f32x4 z = {0.f, 0.f, 0.f, 0.f};
            const f32x4 sp = mfma32(WO[2], ob[2], mfma32(WO[0], ob[0], z)) + mfma32(WO[3], ob[3], mfma32(WO[1], ob[1], z));
            __builtin_amdgcn_raw_buffer_store_b128(clean4(sp), rws, o_ps, 0, AUX_SC1);
        }
    }
}

extern "C" void kernel_launch(void* const* d_in, const int* in_sizes, int n_in, void* d_out, int out_size, void* d_ws, size_t ws_size,
                              hipStream_t stream) {
    Params p;
    p.x = (const u16*)d_in[0]; p.k = (const u16*)d_in[1]; p.v = (const u16*)d_in[2]; p.wq = (const u16*)d_in[3]; p.wo = (const u16*)d_in[4];
    p.w1 = (const u16*)d_in[6]; p.b1 = (const u16*)d_in[7]; p.w2 = (const u16*)d_in[8]; p.b2 = (const u16*)d_in[9];
    p.out = (u16*)d_out; p.ws = (unsigned char*)d_ws;
    (void)hipFuncSetAttribute((const void*)fused_decode, hipFuncAttributeMaxDynamicSharedMemorySize, L_END);
    hipLaunchKernelGGL(fused_decode, dim3(NWG), dim3(NTHR), L_END, stream, p);
}
```

```cpp
#include <hip/hip_runtime.h>
#include <hip/hip_bf16.h>
#include <stdint.h>

#define GAS __attribute__((address_space(1)))
#define LAS __attribute__((address_space(3)))
typedef unsigned u32;
typedef unsigned long long u64;
typedef unsigned short u16;
typedef unsigned v4u __attribute__((ext_vector_type(4)));
typedef unsigned v2u __attribute__((ext_vector_type(2)));
typedef float f32x4 __attribute__((ext_vector_type(4)));
typedef float f32x2 __attribute__((ext_vector_type(2)));
typedef short s16x4 __attribute__((ext_vector_type(4)));
typedef __bf16 bf16x8 __attribute__((ext_vector_type(8)));
typedef __bf16 bf16x2 __attribute__((ext_vector_type(2)));

constexpr int DM = 512, HD = 128, SK = 32768, FF = 1792;
constexpr int NWG = 256, NSW = 4, NTHR = (NSW + 1) * 64;
constexpr int NS = SK / 16 / 16 / NSW;
constexpr int LP = 4, RP = 8, NPRO = LP + RP + 2;
constexpr int NBL = 6, KA = (NS - NPRO - NBL) / 3, VA = (NS - 2) / 3 - 1;
static_assert(NS == 32 && NPRO + NBL + 3 * KA == NS && 3 * VA + 5 == NS, "");
constexpr int T_OFF = 115 * 16, T_K = 915, T_V = 896;
constexpr int T_ORG = 45;
constexpr int WQ = 8, WW1 = 2, WW2 = 2;
constexpr float SC = 0.08838834764831845f * 1.4426950408889634f;
constexpr u32 SENT = 0xFFFFFFFFu;
constexpr u32 SPIN_CAP = 1u << 21;
constexpr u32 OOR = 0xFFFFFF00u;

constexpr u32 WS_EPOCH = 0;
constexpr u32 WS_QG = 4096;
constexpr u32 WS_HG = WS_QG + 8 * 2 * 512 * 8;
constexpr u32 WS_TG = 102400;
constexpr u32 WS_ML = 118784;
constexpr u32 WS_SLOT = 131072, SLOT = 4352;
constexpr u32 SL_PS = 2048, SL_PM = 2560, SL_FB = 3072;
constexpr u32 WS_END = WS_SLOT + NWG * SLOT;
static_assert(WS_HG + 2 * FF * 8 <= WS_TG && WS_TG + 256 * 8 <= WS_ML && WS_ML + 8192 <= WS_SLOT, "");

constexpr u32 L_WQ = 0, L_W1 = 0, L_WO = 16384, L_W2 = 32768;
constexpr u32 L_XN = 40960, L_Y = L_XN + 2048, L_Q = L_Y + 2048, L_H = L_Q + 1024, L_O = L_H + 256;
constexpr u32 L_MRG = L_O + 512;
constexpr u32 L_MML = L_MRG + NSW * 4 * 132 * 4;
constexpr u32 L_CNT = L_MML + NSW * 4 * 2 * 4;
constexpr u32 L_WT = L_CNT + 16;
constexpr u32 L_SC = 57344;
constexpr u32 L_PARK = L_SC + NSW * 8192;
constexpr u32 L_END = L_PARK + NSW * LP * 4096;
static_assert(L_WT + 128 <= L_SC && L_END > 80 * 1024 && L_END <= 160 * 1024, "more than half of a CU's LDS: one workgroup per CU");

struct Params {
    const u16 *x, *k, *v, *wq, *wo, *w1, *b1, *w2, *b2;
    u16* out;
    unsigned char* ws;
};

__device__ __forceinline__ f32x4 mfma32(v4u a, v4u b, f32x4 c) {
    return __builtin_amdgcn_mfma_f32_16x16x32_bf16(__builtin_bit_cast(bf16x8, a), __builtin_bit_cast(bf16x8, b), c, 0, 0, 0);
}
__device__ __forceinline__ f32x4 mfma16(u32 a0, u32 a1, u32 b0, u32 b1, f32x4 c) {
    return __builtin_amdgcn_mfma_f32_16x16x16bf16_1k(__builtin_bit_cast(s16x4, v2u{a0, a1}), __builtin_bit_cast(s16x4, v2u{b0, b1}), c, 0, 0, 0);
}
__device__ __forceinline__ u32 pk_bf16(float lo, float hi) {
    return __builtin_bit_cast(u32, __builtin_convertvector(f32x2{lo, hi}, bf16x2));
}
__device__ __forceinline__ float bf_lo(u32 w) { return __builtin_bit_cast(float, w << 16); }
__device__ __forceinline__ float bf_hi(u32 w) { return __builtin_bit_cast(float, w & 0xffff0000u); }
__device__ __forceinline__ float rbf(float f) { return bf_lo(pk_bf16(f, f)); }
__device__ __forceinline__ float ex2(float x) { return __builtin_amdgcn_exp2f(x); }
template <int CTRL> __device__ __forceinline__ float dpp(float x) {
    return __builtin_bit_cast(float, __builtin_amdgcn_mov_dpp(__builtin_bit_cast(int, x), CTRL, 0xf, 0xf, true));
}
constexpr int XOR1 = 0xB1, XOR2 = 0x4E, XOR7 = 0x141, ROR8 = 0x128;
__device__ __forceinline__ float x16_max(float x) {
    auto s = __builtin_amdgcn_permlane16_swap(__float_as_uint(x), __float_as_uint(x), false, false);
    return fmaxf(__uint_as_float(s[0]), __uint_as_float(s[1]));
}
__device__ __forceinline__ float x32_max(float x) {
    auto s = __builtin_amdgcn_permlane32_swap(__float_as_uint(x), __float_as_uint(x), false, false);
    return fmaxf(__uint_as_float(s[0]), __uint_as_float(s[1]));
}
__device__ __forceinline__ float x16_sum(float x) {
    auto s = __builtin_amdgcn_permlane16_swap(__float_as_uint(x), __float_as_uint(x), false, false);
    return __uint_as_float(s[0]) + __uint_as_float(s[1]);
}
__device__ __forceinline__ float x32_sum(float x) {
    auto s = __builtin_amdgcn_permlane32_swap(__float_as_uint(x), __float_as_uint(x), false, false);
    return __uint_as_float(s[0]) + __uint_as_float(s[1]);
}
__device__ __forceinline__ float row_sum(float x) {
    x += dpp<XOR1>(x); x += dpp<XOR2>(x); x += dpp<XOR7>(x); x += dpp<ROR8>(x); return x;
}
__device__ __forceinline__ float row_max(float x) {
    float r;
    asm("s_nop 1\n\tv_max_f32_dpp %0, %1, %1 quad_perm:[1,0,3,2] row_mask:0xf bank_mask:0xf\n\ts_nop 1\n\t"
        "v_max_f32_dpp %0, %0, %0 quad_perm:[2,3,0,1] row_mask:0xf bank_mask:0xf\n\ts_nop 1\n\t"
        "v_max_f32_dpp %0, %0, %0 row_half_mirror row_mask:0xf bank_mask:0xf\n\ts_nop 1\n\t"
        "v_max_f32_dpp %0, %0, %0 row_ror:8 row_mask:0xf bank_mask:0xf" : "=&v"(r) : "v"(x));
    return r;
}
__device__ __forceinline__ float wave_sum(float x) { return x32_sum(x16_sum(row_sum(x))); }
__device__ __forceinline__ u32 umax4(v4u a) { return max(max(a.x, a.y), max(a.z, a.w)); }
__device__ __forceinline__ u32 no_sent(float f) { return min(__float_as_uint(f), 0xFFFFFFFEu); }

#define RLX_AGENT __ATOMIC_RELAXED, __HIP_MEMORY_SCOPE_AGENT
constexpr int AUX_NT = 2, AUX_SC1 = 16;
__device__ __forceinline__ __amdgpu_buffer_rsrc_t mk_rsrc(const void* p, u32 bytes) {
    return __builtin_amdgcn_make_buffer_rsrc((void*)p, 0, bytes, 0x00020000);
}
__device__ __forceinline__ void glds_nt(const void* src, u32 dst) {
    asm volatile("s_mov_b32 m0, %1\n\ts_nop 0\n\tglobal_load_lds_dwordx4 %0, off nt" ::"v"(src), "s"(dst) : "memory");
}
__device__ __forceinline__ void glds_sh(const void* src, u32 dst) {
    asm volatile("s_mov_b32 m0, %1\n\ts_nop 0\n\tglobal_load_lds_dwordx4 %0, off" ::"v"(src), "s"(dst) : "memory");
}
template <int OFF> __device__ __forceinline__ void glds_nt_off(const void* src, u32 dst) {
    asm volatile("s_mov_b32 m0, %1\n\ts_nop 0\n\tglobal_load_lds_dwordx4 %0, off offset:%2 nt" ::"v"(src), "s"(dst), "i"(OFF) : "memory");
}
template <int N> __device__ __forceinline__ void wait_vm() { asm volatile("s_waitcnt vmcnt(%0)" ::"i"(N) : "memory"); }
__device__ __forceinline__ void wg_barrier() { asm volatile("s_waitcnt lgkmcnt(0)\n\ts_barrier" ::: "memory"); }
__device__ __forceinline__ v2u tr_read(const LAS unsigned char* p) {
    return __builtin_bit_cast(v2u, __builtin_amdgcn_ds_read_tr16_b64_v4i16((LAS s16x4*)p));
}
__device__ __forceinline__ v4u tr_frag(const LAS unsigned char* img, int pitch, int row0, int lane) {
    const int g = lane >> 4, q = (lane >> 2) & 3, p = lane & 3;
    const LAS unsigned char* a = img + (row0 + 8 * g + q) * pitch + 8 * p;
    const v2u lo = tr_read(a), hi = tr_read(a + 4 * pitch);
    return v4u{lo.x, lo.y, hi.x, hi.y};
}

__device__ __forceinline__ void load_act(v4u (&B)[16], const LAS unsigned char* act, int lane) {
    const LAS unsigned char* ap = act + (lane & 1) * 1024 + (lane >> 4) * 16;
#pragma unroll
    for (int s = 0; s < 16; ++s) B[s] = *(const LAS v4u*)(ap + 64 * s);
}
__device__ __forceinline__ f32x4 gemv16(const LAS unsigned char* img, const v4u (&B)[16], int lane) {
    v4u A[16];
#pragma unroll
    for (int s = 0; s < 16; ++s) A[s] = tr_frag(img, 32, 32 * s, lane);
    __builtin_amdgcn_sched_barrier(0);
    f32x4 a[4];
#pragma unroll
    for (int s = 0; s < 16; ++s) a[s & 3] = mfma32(A[s], B[s], s < 4 ? f32x4{0.f, 0.f, 0.f, 0.f} : a[s & 3]);
    return (a[0] + a[1]) + (a[2] + a[3]);
}
__device__ __forceinline__ void layer_norm(float (&v)[2][8], LAS unsigned char* dst, int lane) {
    float mean[2], rs[2];
#pragma unroll
    for (int bb = 0; bb < 2; ++bb) mean[bb] = wave_sum(((v[bb][0] + v[bb][1]) + (v[bb][2] + v[bb][3])) + ((v[bb][4] + v[bb][5]) + (v[bb][6] + v[bb][7]))) * (1.f / DM);
#pragma unroll
    for (int bb = 0; bb < 2; ++bb) {
        float sq = 0.f;
#pragma unroll
        for (int u = 0; u < 8; ++u) { v[bb][u] -= mean[bb]; sq = fmaf(v[bb][u], v[bb][u], sq); }
        rs[bb] = __builtin_amdgcn_rsqf(wave_sum(sq) * (1.f / DM) + 1e-5f);
    }
#pragma unroll
    for (int bb = 0; bb < 2; ++bb) {
        v4u o;
#pragma unroll
        for (int u = 0; u < 4; ++u) {
            o[u] = pk_bf16(v[bb][2 * u] * rs[bb], v[bb][2 * u + 1] * rs[bb]);
            v[bb][2 * u] = bf_lo(o[u]); v[bb][2 * u + 1] = bf_hi(o[u]);
        }
        *(LAS v4u*)(dst + bb * 1024 + lane * 16) = o;
    }
}
__device__ __forceinline__ float sel4(f32x4 v, int r) {
    const bool b0 = r & 1, b1 = r & 2;
    const float lo = b0 ? v[1] : v[0], hi = b0 ? v[3] : v[2];
    return b1 ? hi : lo;
}
__device__ __forceinline__ v4u clean4(f32x4 v) { return v4u{no_sent(v[0]), no_sent(v[1]), no_sent(v[2]), no_sent(v[3])}; }

__global__ void __launch_bounds__(NTHR, 2) fused_decode(Params p) {
    extern __shared__ __attribute__((aligned(16))) unsigned char lds_raw[];
    LAS unsigned char* L = (LAS unsigned char*)lds_raw;
    const u32 lb = (u32)(size_t)L;
    const int tid = threadIdx.x, lane = tid & 63, w = __builtin_amdgcn_readfirstlane(tid >> 6);
    const int bid = blockIdx.x;
    const int kq = (bid >> 4) & 7, jq = 4 * (bid & 7) + 2 * (bid >> 7) + ((bid >> 3) & 1);
    const int m1i = (bid - kq) & 7, jm = 4 * (7 * (kq >> 1) + 4 * (kq & 1) + m1i) + (jq & 3);
    const int sid = ((bid >> 5) * 8 + (bid & 7)) * 4 + ((bid >> 3) & 3);
    const int pair = sid >> 4, b = pair >> 3, kvh = pair & 7, s = sid & 15;
    const int x8 = bid & 7, hi = 4 * x8 + (bid >> 6), cg = (bid >> 3) & 7;
    const bool do_m1 = m1i < 4 - (kq & 1), do_m2 = hi < 28;
    const int g = lane >> 4, n = lane & 15;
    const __amdgpu_buffer_rsrc_t rws = mk_rsrc(p.ws, WS_END);
    const u32 myslot = WS_SLOT + bid * SLOT, aslot = WS_SLOT + sid * SLOT;

    const u32 t_start = (u32)__builtin_amdgcn_s_memrealtime();
    asm volatile("" ::"s"(p.k), "s"(p.v), "s"(p.x), "s"(p.wq), "s"(p.ws));
    if (w >= NSW) {
        __builtin_amdgcn_s_setprio(3);
        v4u xr0, xr1; v2u ep;
        {
            const void* xa = p.x + 8 * lane;
            const void* ea = p.ws + WS_EPOCH;
            asm volatile("global_load_dwordx4 %0, %3, off\n\tglobal_load_dwordx4 %1, %3, off offset:1024\n\tglobal_load_dwordx2 %2, %4, off sc1"
                         : "=&v"(xr0), "=&v"(xr1), "=&v"(ep) : "v"(xa), "v"(ea) : "memory");
        }
        {
            const v4u sv = {SENT, SENT, SENT, SENT};
#pragma unroll
            for (int u = 0; u < 3; ++u) __builtin_amdgcn_raw_buffer_store_b128(sv, rws, (u < 2 ? aslot : myslot) + u * 1024 + lane * 16, 0, AUX_SC1);
            const u32 o4 = lane < 4 ? WS_ML + ((pair * 4 + lane) * 16 + s) * 8 : lane == 4 ? myslot + SL_FB : OOR;
            __builtin_amdgcn_raw_buffer_store_b64(v2u{SENT, SENT}, rws, o4, 0, AUX_SC1);
        }
        const int rot = 4 * (jq & 3);
        const u16* wqs = p.wq + (size_t)(lane >> 1) * 4096 + kq * 512 + 16 * jq + 8 * (lane & 1);
#pragma unroll
        for (int u = 0; u < WQ; ++u) { const int ur = (u + rot) & 15; glds_sh(wqs + (size_t)ur * 32 * 4096, lb + L_WQ + ur * 1024); }
        asm volatile("s_waitcnt vmcnt(%3)" : "+v"(xr0), "+v"(xr1), "+v"(ep) : "i"(4 + WQ) : "memory");
#pragma unroll
        for (int u = WQ; u < 16; ++u) { const int ur = (u + rot) & 15; wait_vm<WQ - 1>(); glds_sh(wqs + (size_t)ur * 32 * 4096, lb + L_WQ + ur * 1024); }
        const u64 tag = ((((u64)ep.y << 32) | ep.x) + 1) & 0xFFFFFFFFFFFFull;
        float xv[2][8];
#pragma unroll
        for (int u = 0; u < 4; ++u) { xv[0][2 * u] = bf_lo(xr0[u]); xv[0][2 * u + 1] = bf_hi(xr0[u]); xv[1][2 * u] = bf_lo(xr1[u]); xv[1][2 * u + 1] = bf_hi(xr1[u]); }
        layer_norm(xv, L + L_XN, lane);
        {
            v4u B[16];
            load_act(B, L + L_XN, lane);
            __builtin_amdgcn_sched_barrier(0);
            wait_vm<0>();
            const f32x4 qv = gemv16(L + L_WQ, B, lane);
            const u32 qb = pk_bf16(sel4(qv, n >> 1), 0.f) & 0xffffu;
            GAS u64* dst = (GAS u64*)(p.ws + WS_QG) + (kq * 2 + (n & 1)) * 512 + 16 * jq + 4 * g + (n >> 1);
            if (n < 8) __hip_atomic_store(dst, (tag << 16) | qb, RLX_AGENT);
        }
        {
            GAS u64* src = (GAS u64*)(p.ws + WS_QG) + (kvh * 2 + b) * 512 + lane;
            GAS u64* tsrc = (GAS u64*)(p.ws + WS_TG) + kq * 32 + (lane & 31);
            u64 A[8], T;
            __builtin_amdgcn_s_sleep(60);
#pragma unroll 1
            for (u32 spin = 0; spin < SPIN_CAP; ++spin) {
                bool ok = true;
#pragma unroll
                for (int k = 0; k < 8; ++k) A[k] = __hip_atomic_load(src + 64 * k, RLX_AGENT);
                T = __hip_atomic_load(tsrc, RLX_AGENT);
#pragma unroll
                for (int k = 0; k < 8; ++k) ok &= (A[k] >> 16) == tag;
                ok &= (T >> 16) == tag;
                if (__all(ok)) break;
                __builtin_amdgcn_s_sleep(24);
            }
#pragma unroll
            for (int k = 0; k < 8; ++k) *(LAS u16*)(L + L_Q + (64 * k + lane) * 2) = (u16)A[k];
            const float mean = x16_sum(row_sum((float)(short)((u32)T - t_start))) * (1.f / 32.f);
            *(LAS v4u*)(L + L_CNT) = v4u{0u, t_start + (int)mean + T_ORG, 0u, 0u};
        }
        wg_barrier();
        __builtin_amdgcn_s_setprio(1);
        layer_norm(xv, L + L_Y, lane);
        const int fb = bid >> 7, fq = (bid >> 3) & 15, hf = lane >> 5, fc = 64 * x8 + 4 * fq + 2 * hf;
        const u32 xo = *(const GAS u32*)(p.x + fb * DM + fc), b2o = *(const GAS u32*)(p.b2 + fc);
        const int col1 = do_m1 ? 16 * jm + 4 * g + ((n >> 1) & 3) : 0;
        const float bias1 = bf_lo(p.b1[col1]);
        if (do_m1) {
            const u16* s1 = p.w1 + (size_t)(lane >> 1) * FF + 16 * jm + 8 * (lane & 1);
#pragma unroll
            for (int u = 0; u < 16; ++u) { const int ur = (u + rot) & 15; wait_vm<WW1 - 1>(); glds_sh(s1 + (size_t)ur * 32 * FF, lb + L_W1 + ur * 1024); }
        }
        if (do_m2) {
            const u16* s2 = p.w2 + (size_t)(hi * 64 + (lane >> 3)) * DM + 64 * cg + 8 * (lane & 7);
#pragma unroll
            for (int u = 0; u < 8; ++u) { wait_vm<WW2 - 1>(); glds_nt(s2 + (size_t)u * 8 * DM, lb + L_W2 + u * 1024); }
        }
        wait_vm<0>();
        if (do_m1) {
            v4u B[16];
            load_act(B, L + L_Y, lane);
            const float u = rbf(rbf(sel4(gemv16(L + L_W1, B, lane), n >> 1)) + bias1);
            const float z = 1.5957691216057308f * fmaf(0.044715f * u * u, u, u);
            const float h = u / (1.f + __expf(-z));
            if (n < 8) __hip_atomic_store((GAS u64*)(p.ws + WS_HG) + (n & 1) * FF + col1, (tag << 16) | (pk_bf16(h, 0.f) & 0xffffu), RLX_AGENT);
        }
        const int pi = lane & 31, pw = ((pi & 3) * 8 + x8) * 8 + (pi >> 2);
        const u32 pslot = WS_SLOT + pw * SLOT + fb * 256 + fq * 16 + hf * 8;
        {
            const int es = ((pi >> 4) * 8 + x8) * 16 + (pi & 15);
            const int eb = hf ? pw : (es >> 5) * 32 + (es & 3) * 8 + ((es >> 2) & 7);
            const int ek = (eb >> 4) & 7, ej = 4 * (eb & 7) + 2 * (eb >> 7) + ((eb >> 3) & 1);
            GAS u64* ev = (GAS u64*)(p.ws + WS_QG) + ek * 1024 + 16 * ej;
            GAS u64* hs = do_m2 ? (GAS u64*)(p.ws + WS_HG) + 64 * hi + lane : ev;
            const int hstr = do_m2 ? FF : 0;
            u64 e, h0, h1;
            if (!do_m1) { __builtin_amdgcn_s_sleep(64); __builtin_amdgcn_s_sleep(64); }
            __builtin_amdgcn_s_sleep(64);
#pragma unroll 1
            for (u32 spin = 0; spin < SPIN_CAP; ++spin) {
                e = __hip_atomic_load(ev, RLX_AGENT); h0 = __hip_atomic_load(hs, RLX_AGENT); h1 = __hip_atomic_load(hs + hstr, RLX_AGENT);
                if (__all(((e >> 16) == tag) & ((h0 >> 16) == tag) & ((h1 >> 16) == tag))) break;
                __builtin_amdgcn_s_sleep(64);
            }
            *(LAS u16*)(L + L_H + lane * 2) = (u16)h0;
            *(LAS u16*)(L + L_H + 128 + lane * 2) = (u16)h1;
        }
        if (do_m2) {
#pragma unroll
            for (int cb = 0; cb < 4; ++cb) {
                f32x4 mp = {0.f, 0.f, 0.f, 0.f};
#pragma unroll
                for (int u = 0; u < 2; ++u) mp = mfma32(tr_frag(L + L_W2 + 32 * cb, 128, 32 * u, lane), *(const LAS v4u*)(L + L_H + (n & 1) * 128 + 64 * u + 16 * g), mp);
                __builtin_amdgcn_raw_buffer_store_b128(clean4(mp), rws, n < 2 ? myslot + SL_PM + n * 256 + cb * 64 + g * 16 : OOR, 0, AUX_SC1);
            }
        }
        float gate[2];
        {
            v2u a;
            __builtin_amdgcn_s_sleep(64);
#pragma unroll 1
            for (u32 spin = 0; spin < SPIN_CAP; ++spin) {
                asm volatile("" ::: "memory");
                a = __builtin_amdgcn_raw_buffer_load_b64(rws, pi < 28 ? pslot + SL_PM : OOR, 0, AUX_SC1);
                if (__all(max(a.x, a.y) != SENT)) break;
                __builtin_amdgcn_s_sleep(64);
            }
#pragma unroll
            for (int k = 0; k < 2; ++k) {
                const float mm = rbf(rbf(x16_sum(row_sum(__uint_as_float(a[k])))) + (k ? bf_hi(b2o) : bf_lo(b2o)));
                gate[k] = rbf(mm / (1.f + __expf(-mm)));
            }
        }
        float x0 = bf_lo(xo), x1 = bf_hi(xo);
        GAS u32* outp = (GAS u32*)(p.out + fb * DM + fc);
        asm volatile("" : "+v"(gate[0]), "+v"(gate[1]), "+v"(x0), "+v"(x1), "+v"(outp));
        __builtin_amdgcn_s_setprio(3);
        u32 mlo = lane < 4 ? WS_ML + ((pair * 4 + lane) * 16 + s) * 8 : OOR, fme = lane == 0 ? myslot + SL_FB : OOR;
        u32 mli = lane < 32 ? WS_ML + ((((lane >> 4) * 8 + x8) * 4 + (hi & 3)) * 16 + (lane & 15)) * 8 : OOR;
        u32 fbo = lane < 32 ? WS_SLOT + pw * SLOT + SL_FB : OOR, pso = pslot + SL_PS, epo = bid == 0 && lane == 0 ? WS_EPOCH : OOR;
        const u64 en = (((u64)ep.y << 32) | ep.x) + 1;
        v2u env = {(u32)en, (u32)(en >> 32)};
        asm volatile("" : "+v"(mlo), "+v"(mli), "+v"(fme), "+v"(fbo), "+v"(pso), "+v"(epo), "+v"(env));
#pragma unroll 1
        for (u32 spin = 0; spin < 16 * SPIN_CAP; ++spin) {
            if (__hip_atomic_load((LAS u32*)(L + L_CNT), __ATOMIC_RELAXED, __HIP_MEMORY_SCOPE_WORKGROUP) >= NSW) break;
            __builtin_amdgcn_s_sleep(1);
        }
        asm volatile("" ::: "memory");
        {
            f32x2 ml[NSW];
#pragma unroll
            for (int u = 0; u < NSW; ++u) ml[u] = *(const LAS f32x2*)(L + L_MML + (u * 4 + (lane & 3)) * 8);
            const float M = fmaxf(fmaxf(ml[0].x, ml[1].x), fmaxf(ml[2].x, ml[3].x));
            __builtin_amdgcn_raw_buffer_store_b64(v2u{no_sent(M), no_sent((ml[0].y + ml[1].y) + (ml[2].y + ml[3].y))}, rws, mlo, 0, AUX_SC1);
        }
#define WEIGHTS()                                                \
    do {                                                                                                       \
        const float mp = __uint_as_float(q2.x), wt = ex2(mp - row_max(mp));                                    \
        const float wn = wt * __builtin_amdgcn_rcpf(row_sum(wt * __uint_as_float(q2.y)));                      \
        if (lane < 32) *(LAS float*)(L + L_WT + lane * 4) = wn;                                                \
    } while (0)
        __builtin_amdgcn_s_sleep(36);
        v2u q2 = __builtin_amdgcn_raw_buffer_load_b64(rws, mli, 0, AUX_SC1);
        const bool have = __all(max(q2.x, q2.y) != SENT);
        if (have) WEIGHTS();
        wg_barrier();
        if (!have) {
#pragma unroll 1
            for (u32 spin = 0; spin < SPIN_CAP; ++spin) {
                asm volatile("" ::: "memory");
                q2 = __builtin_amdgcn_raw_buffer_load_b64(rws, mli, 0, AUX_SC1);
                if (__all(max(q2.x, q2.y) != SENT)) break;
                __builtin_amdgcn_s_sleep(1);
            }
            WEIGHTS();
        }
        wg_barrier();
        __builtin_amdgcn_s_sleep(5);
        __builtin_amdgcn_raw_buffer_store_b32(1u, rws, fme, 0, AUX_SC1);
        wg_barrier();
#pragma unroll 1
        for (u32 spin = 0; spin < SPIN_CAP; ++spin) {
            asm volatile("" ::: "memory");
            if (__all(__builtin_amdgcn_raw_buffer_load_b32(rws, fbo, 0, AUX_SC1) != SENT)) break;
            __builtin_amdgcn_s_sleep(1);
        }
        {
            v2u a = __builtin_amdgcn_raw_buffer_load_b64(rws, pso, 0, AUX_SC1);
            __builtin_amdgcn_raw_buffer_store_b64(env, rws, epo, 0, AUX_SC1);
            __builtin_amdgcn_sched_barrier(0);
            if (!__all(max(a.x, a.y) != SENT)) {
#pragma unroll 1
                for (u32 spin = 0; spin < SPIN_CAP; ++spin) {
                    asm volatile("" ::: "memory");
                    a = __builtin_amdgcn_raw_buffer_load_b64(rws, pso, 0, AUX_SC1);
                    if (__all(max(a.x, a.y) != SENT)) break;
                }
            }
            const float s0 = rbf(x16_sum(row_sum(__uint_as_float(a.x)))), s1 = rbf(x16_sum(row_sum(__uint_as_float(a.y))));
            const u32 r = pk_bf16(x0 + rbf(s0 * gate[0]), x1 + rbf(s1 * gate[1]));
            if (pi == 0) *outp = r;
        }
    } else {
        const u16* kb = p.k + (size_t)pair * SK * HD;
        const u16* vb = p.v + (size_t)pair * SK * HD;
        const __amdgpu_buffer_rsrc_t rk = mk_rsrc(kb, SK * HD * 2), rv = mk_rsrc(vb, SK * HD * 2);
        constexpr u32 TSTR = NSW * 4096;
        const u32 kl = (n >> 2) * 256 + g * 64 + (n & 3) * 16, vl = lane * 16;
        u32 ko = (s * NS * NSW + w) * 4096 + kl, vo = (s * NS * NSW + w) * 4096 + vl;
        v4u R[3][4], KX[RP][4];
        u64 stub0, stub;
        asm volatile("s_getpc_b64 %0\n\ts_branch 1f\n\t"
                     "s_waitcnt vmcnt(4)\n\ts_setpc_b64 s[80:81]\n\ts_waitcnt vmcnt(5)\n\ts_setpc_b64 s[80:81]\n\t"
                     "s_waitcnt vmcnt(6)\n\ts_setpc_b64 s[80:81]\n\ts_waitcnt vmcnt(7)\n\ts_setpc_b64 s[80:81]\n1:" : "=s"(stub0));
        stub0 -= 36;
        stub = stub0 + 8 * 6;
        u32 due = t_start * 16 + T_OFF;
        int late;
#define WAITW() asm volatile("s_swappc_b64 s[80:81], %0" ::"s"(stub) : "memory", "s80", "s81")
#define CLOCK() const u32 now_ = (u32)__builtin_amdgcn_s_memrealtime(); __builtin_amdgcn_sched_barrier(0)
#define PACE2(tau, lo)                                            \
    do {                                                                                                       \
        due += (tau);                                                                                          \
        late = (int)(now_ * 16 - due);                                                                         \
        const int win = __builtin_amdgcn_readfirstlane(min(max(6 + ((late + 512) >> 10), lo), 8));             \
        stub = stub0 + 8 * win;                                                                                \
        __builtin_amdgcn_sched_barrier(0);                                                                     \
    } while (0)
#define PACE(tau) PACE2(tau, 5)
#define LK(X, u) do { WAITW(); X[u] = __builtin_amdgcn_raw_buffer_load_b128(rk, ko + 1024 * (u), 0, AUX_NT); __builtin_amdgcn_sched_barrier(0); } while (0)
#define LV(X, u) do { WAITW(); X[u] = __builtin_amdgcn_raw_buffer_load_b128(rv, vo + 1024 * (u), 0, AUX_NT); __builtin_amdgcn_sched_barrier(0); } while (0)
#define LK4(X) do { LK(X, 0); LK(X, 1); LK(X, 2); LK(X, 3); ko += TSTR; } while (0)
#define LV4(X) do { LV(X, 0); LV(X, 1); LV(X, 2); LV(X, 3); vo += TSTR; } while (0)
        const bool rep = w == 1 && lane == 0;
        const v2u e2 = __builtin_amdgcn_raw_buffer_load_b64(rws, rep ? WS_EPOCH : OOR, 0, AUX_SC1);
        __builtin_amdgcn_sched_barrier(0);
        const u32 park = lb + L_PARK + w * LP * 4096;
#pragma unroll
        for (int t = 0; t < LP; ++t) {
            const unsigned char* ks = (const unsigned char*)kb + ko;
            WAITW(); glds_nt_off<0>(ks, park + t * 4096);
            WAITW(); glds_nt_off<1024>(ks, park + t * 4096);
            WAITW(); glds_nt_off<2048>(ks, park + t * 4096);
            WAITW(); glds_nt_off<3072>(ks, park + t * 4096);
            ko += TSTR;
            CLOCK(); PACE2(T_K, 6);
        }
#pragma unroll
        for (int t = 0; t < RP; ++t) {
            LK4(KX[t]); CLOCK(); PACE2(T_K, 6);
            if (t == 0) {
                const u64 gv = (((((u64)e2.y << 32) | e2.x) + 1) << 16) | ((t_start + (late >> 4)) & 0xffffu);
                __builtin_amdgcn_raw_buffer_store_b64(v2u{(u32)gv, (u32)(gv >> 32)}, rws, rep ? WS_TG + (kq * 32 + jq) * 8 : OOR, 0, AUX_SC1);
                __builtin_amdgcn_sched_barrier(0);
            }
        }
#pragma unroll
        for (int t = 0; t < 2; ++t) { LK4(R[t]); CLOCK(); PACE(T_K); }
        wg_barrier();
        due += (__builtin_amdgcn_readfirstlane(*(const LAS u32*)(L + L_CNT + 4)) - t_start) * 16;
        const int jj = n & 3;
        const bool j0 = jj & 1, j1 = jj & 2;
        const v4u Q = *(const LAS v4u*)(L + L_Q + (n >> 2) * 256 + 64 * g + 16 * jj);
        const u32 mask0 = jj == 0 ? 0x0000ffffu : jj == 1 ? 0xffff0000u : 0u, mask1 = jj == 2 ? 0x0000ffffu : jj == 3 ? 0xffff0000u : 0u;
        float m = -INFINITY, l = 0.f;
        f32x4 acc0 = {0.f, 0.f, 0.f, 0.f}, acc1 = {0.f, 0.f, 0.f, 0.f};
        LAS float* scp = (LAS float*)(L + L_SC + w * 8192) + lane;
#define KP(KR, idx)                                                                                            \
    do {                                                                                                       \
        f32x4 dd[4]; float sc[4];                                                                              \
        _Pragma("unroll") for (int u = 0; u < 4; ++u) dd[u] = mfma32(KR[u], Q, f32x4{0.f, 0.f, 0.f, 0.f});     \
        asm volatile("" : "+v"(dd[0]), "+v"(dd[1]), "+v"(dd[2]), "+v"(dd[3]));     \
        _Pragma("unroll") for (int u = 0; u < 4; ++u) sc[u] = sel4(dd[u], jj);                                 \
        const float y01 = (j0 ? sc[1] : sc[0]) + dpp<XOR1>(j0 ? sc[0] : sc[1]), y23 = (j0 ? sc[3] : sc[2]) + dpp<XOR1>(j0 ? sc[2] : sc[3]); \
        const float y = (j1 ? y23 : y01) + dpp<XOR2>(j1 ? y01 : y23);                                          \
        m = fmaxf(m, y);                                                                                       \
        scp[(idx) * 64] = y;                                                                                   \
        __builtin_amdgcn_sched_barrier(0);                                                                     \
    } while (0)
#define UNPARK(t, KR)                                                                                          \
    do {                                                                                                       \
        const LAS unsigned char* pk = L + L_PARK + (w * LP + (t)) * 4096 + lane * 16;                          \
        _Pragma("unroll") for (int u = 0; u < 4; ++u) KR[u] = *(const LAS v4u*)(pk + 1024 * u);                \
    } while (0)
#define VP(VR, sv) do { const float p1 = ex2(fmaf(sv, SC, -mc)); l += p1; PV(VR, p1); } while (0)
#define PV(VR, p1)                                                                                             \
    do {                                                                                                       \
        const int pb = pk_bf16(p1, p1);                                                                        \
        _Pragma("unroll") for (int r = 0; r < 4; ++r) {                                                        \
            const u32 t_ = r == 0 ? __builtin_amdgcn_mov_dpp(pb, 0x00, 0xf, 0xf, true) : r == 1 ? __builtin_amdgcn_mov_dpp(pb, 0x55, 0xf, 0xf, true) \
                         : r == 2 ? __builtin_amdgcn_mov_dpp(pb, 0xAA, 0xf, 0xf, true) : __builtin_amdgcn_mov_dpp(pb, 0xFF, 0xf, 0xf, true); \
            acc0 = mfma16(VR[r].x, VR[r].y, t_ & mask0, t_ & mask1, acc0);                                     \
            acc1 = mfma16(VR[r].z, VR[r].w, t_ & mask0, t_ & mask1, acc1);                                     \
        }                                                                                                      \
        __builtin_amdgcn_sched_barrier(0);                                                                     \
    } while (0)
#define UKK(u, idx) do { LK4(R[((u) + 2) % 3]); CLOCK(); KP(R[(u) % 3], idx); PACE(T_K); } while (0)
#define UVK(u, idx) do { LV4(R[((u) + 2) % 3]); CLOCK(); KP(R[(u) % 3], idx); PACE(T_V); } while (0)
#define UVV(u, idx) do { float sv_ = scp[(idx) * 64]; LV4(R[((u) + 2) % 3]); asm volatile("" : "+v"(sv_)); CLOCK(); VP(R[(u) % 3], sv_); PACE(T_V); } while (0)
#define UKB(u, XA, ia, XB, ib, PRE)                                                                            \
    do {                                                                                                       \
        LK(R[((u) + 2) % 3], 0); PRE; KP(XA, ia); LK(R[((u) + 2) % 3], 1); LK(R[((u) + 2) % 3], 2); KP(XB, ib); LK(R[((u) + 2) % 3], 3); ko += TSTR; \
        CLOCK(); KP(R[(u) % 3], 12 + (u)); PACE(T_K);                                                          \
    } while (0)
        static_assert(LP == 4 && RP == 8 && NBL == 6, "");
        UKB(0, KX[0], 4, KX[1], 5, (void)0);
        UKB(1, KX[2], 6, KX[3], 7, { UNPARK(0, KX[0]); UNPARK(1, KX[1]); });
        UKB(2, KX[0], 0, KX[1], 1, { UNPARK(2, KX[2]); UNPARK(3, KX[3]); });
        UKB(3, KX[2], 2, KX[3], 3, (void)0);
        UKB(4, KX[4], 8, KX[5], 9, (void)0);
        UKB(5, KX[6], 10, KX[7], 11, (void)0);
        scp += (12 + NBL) * 64;
#pragma unroll 1
        for (int it = 0; it < KA; ++it) { UKK(0, 0); UKK(1, 1); UKK(2, 2); scp += 3 * 64; }
        UVK(0, 0); UVK(1, 1);
        m = fmaxf(m, dpp<XOR1>(m)); m = fmaxf(m, dpp<XOR2>(m)); m = x32_max(x16_max(m));
        float mc = m * SC;
        const int bb = w & 1, dh = w >> 1;
        u32 cnt_a = lb + L_CNT, cnt_v = lane == 0;
        u32 a_mw = L_MRG + ((w * 4 + (n >> 2)) * 132 + 32 * g + jj) * 4, a_ml = L_MML + (n >> 2) * 8;
        u32 a_mr = L_MRG + (w * 132 + 4 * (lane & 31)) * 4, o_pa = lane < 32 ? aslot + w * 512 + lane * 16 : OOR;
        u32 o_ga = WS_SLOT + ((bb * 8 + x8) * 16 + 4 * g) * SLOT + (hi & 3) * 512 + dh * 256 + n * 16, a_wt = L_WT + bb * 64 + g * 16;
        u32 a_wo = L_WO + 32 * w + (8 * g + ((lane >> 2) & 3)) * 128 + 8 * jj, a_ow = L_O + bb * 256 + dh * 128 + n * 8 + g * 2;
        u32 a_or = L_O + (n & 1) * 256 + 16 * g, o_ps = n < 2 ? myslot + SL_PS + n * 256 + w * 64 + g * 16 : OOR;
        const u16* wo_p = p.wo + (size_t)(hi * 128 + 32 * w + (lane >> 3)) * DM + 64 * cg + 8 * (lane & 7);
        asm volatile("" : "+v"(cnt_a), "+v"(cnt_v), "+v"(a_mw), "+v"(a_ml), "+v"(a_mr), "+v"(o_pa), "+v"(o_ga));
        asm volatile("" : "+v"(a_wt), "+v"(a_wo), "+v"(a_ow), "+v"(a_or), "+v"(o_ps), "+v"(wo_p));
        *(LAS float*)(L + a_ml + w * 32) = mc;
        asm volatile("ds_add_u32 %0, %1 offset:8" ::"v"(cnt_a), "v"(cnt_v) : "memory");
        scp -= (NS - 2) * 64;
#pragma unroll 1
        for (int it = 0; it < VA; ++it) { UVV(2, 0); UVV(3, 1); UVV(4, 2); scp += 3 * 64; }
        {
#pragma unroll 1
            for (u32 spin = 0; spin < 16 * SPIN_CAP; ++spin)
                if (__hip_atomic_load((LAS u32*)(L + L_CNT + 8), __ATOMIC_RELAXED, __HIP_MEMORY_SCOPE_WORKGROUP) >= NSW) break;
            asm volatile("" ::: "memory");
            float m4[NSW];
#pragma unroll
            for (int u = 0; u < NSW; ++u) m4[u] = *(const LAS float*)(L + a_ml + u * 32);
            const float M = fmaxf(fmaxf(m4[0], m4[1]), fmaxf(m4[2], m4[3])), f = ex2(mc - M);
            acc0 *= f; acc1 *= f; l *= f; mc = M;
            __builtin_amdgcn_sched_barrier(0);
        }
        {
            float pp[5];
#pragma unroll
            for (int u = 0; u < 5; ++u) pp[u] = ex2(fmaf(scp[u * 64], SC, -mc));
            l += ((pp[0] + pp[1]) + (pp[2] + pp[3])) + pp[4];
            l += dpp<XOR1>(l); l += dpp<XOR2>(l);
            *(LAS float*)(L + a_ml + w * 32 + 4) = x32_sum(x16_sum(l));
            asm volatile("ds_add_u32 %0, %1" ::"v"(cnt_a), "v"(cnt_v) : "memory");
            __builtin_amdgcn_sched_barrier(0);
            { LV4(R[1]); CLOCK(); PV(R[2], pp[0]); PACE(T_V); }
            { LV4(R[2]); CLOCK(); PV(R[0], pp[1]); PACE(T_V); }
            LV4(R[0]);
            PV(R[1], pp[2]); PV(R[2], pp[3]); PV(R[0], pp[4]);
        }
        {
            LAS float* mr = (LAS float*)(L + a_mw);
#pragma unroll
            for (int r = 0; r < 4; ++r) { mr[8 * r] = acc0[r]; mr[8 * r + 4] = acc1[r]; }
        }
#pragma unroll
        for (int u = 0; u < 4; ++u) glds_nt(wo_p + (size_t)u * 8 * DM, lb + L_WO + (4 * w + u) * 1024);
        wg_barrier();
        {
            f32x4 ov[NSW];
#pragma unroll
            for (int u = 0; u < NSW; ++u) ov[u] = *(const LAS f32x4*)(L + a_mr + u * (4 * 132 * 4));
            __builtin_amdgcn_raw_buffer_store_b128(clean4((ov[0] + ov[1]) + (ov[2] + ov[3])), rws, o_pa, 0, AUX_SC1);
        }
        v4u WO[4];
        {
            v4u a[4];
#define GATHER() _Pragma("unroll") for (int k = 0; k < 4; ++k) a[k] = __builtin_amdgcn_raw_buffer_load_b128(rws, o_ga, k * SLOT, AUX_SC1)
#define ARRIVED() __all(max(max(umax4(a[0]), umax4(a[1])), max(umax4(a[2]), umax4(a[3]))) != SENT)
            wait_vm<0>();
            wg_barrier();
            GATHER();
            __builtin_amdgcn_sched_barrier(0);
            const f32x4 wn = *(const LAS f32x4*)(L + a_wt);
#pragma unroll
            for (int u = 0; u < 4; ++u) {
                const v2u lo = tr_read(L + a_wo + 4096 * u), hi2 = tr_read(L + a_wo + 4096 * u + 512);
                WO[u] = v4u{lo.x, lo.y, hi2.x, hi2.y};
            }
            __builtin_amdgcn_sched_barrier(0);
            if (!ARRIVED()) {
#pragma unroll 1
                for (u32 spin = 0; spin < SPIN_CAP; ++spin) {
                    asm volatile("" ::: "memory");
                    GATHER();
                    if (ARRIVED()) break;
                }
            }
            const f32x4 o = (wn[0] * __builtin_bit_cast(f32x4, a[0]) + wn[1] * __builtin_bit_cast(f32x4, a[1])) +
                            (wn[2] * __builtin_bit_cast(f32x4, a[2]) + wn[3] * __builtin_bit_cast(f32x4, a[3]));
            const auto s02 = __builtin_amdgcn_permlane32_swap(__float_as_uint(o[0]), __float_as_uint(o[2]), false, false);
            const auto s13 = __builtin_amdgcn_permlane32_swap(__float_as_uint(o[1]), __float_as_uint(o[3]), false, false);
            const float xs = __uint_as_float(s02[0]) + __uint_as_float(s02[1]), ys = __uint_as_float(s13[0]) + __uint_as_float(s13[1]);
            const auto sz = __builtin_amdgcn_permlane16_swap(__float_as_uint(xs), __float_as_uint(ys), false, false);
            const float z = __uint_as_float(sz[0]) + __uint_as_float(sz[1]);
            *(LAS u16*)(L + a_ow) = (u16)pk_bf16(z, z);
        }
        wg_barrier();
        {
            v4u ob[4];
#pragma unroll
            for (int u = 0; u < 4; ++u) ob[u] = *(const LAS v4u*)(L + a_or + 64 * u);
            __builtin_amdgcn_sched_barrier(0);
            const f32x4 z = {0.f, 0.f, 0.f, 0.f};
            const f32x4 sp = mfma32(WO[2], ob[2], mfma32(WO[0], ob[0], z)) + mfma32(WO[3], ob[3], mfma32(WO[1], ob[1], z));
            __builtin_amdgcn_raw_buffer_store_b128(clean4(sp), rws, o_ps, 0, AUX_SC1);
        }
    }
}

extern "C" void kernel_launch(void* const* d_in, const int* in_sizes, int n_in, void* d_out, int out_size, void* d_ws, size_t ws_size,
                              hipStream_t stream) {
    Params p;
    p.x = (const u16*)d_in[0]; p.k = (const u16*)d_in[1]; p.v = (const u16*)d_in[2]; p.wq = (const u16*)d_in[3]; p.wo = (const u16*)d_in[4];
    p.w1 = (const u16*)d_in[6]; p.b1 = (const u16*)d_in[7]; p.w2 = (const u16*)d_in[8]; p.b2 = (const u16*)d_in[9];
    p.out = (u16*)d_out; p.ws = (unsigned char*)d_ws;
    (void)hipFuncSetAttribute((const void*)fused_decode, hipFuncAttributeMaxDynamicSharedMemorySize, L_END);
    hipLaunchKernelGGL(fused_decode, dim3(NWG), dim3(NTHR), L_END, stream, p);
}
```

```cpp
#include <hip/hip_runtime.h>
#include <hip/hip_bf16.h>
#include <stdint.h>

#define GAS __attribute__((address_space(1)))
#define LAS __attribute__((address_space(3)))
typedef unsigned u32;
typedef unsigned long long u64;
typedef unsigned short u16;
typedef unsigned v4u __attribute__((ext_vector_type(4)));
typedef unsigned v2u __attribute__((ext_vector_type(2)));
typedef float f32x4 __attribute__((ext_vector_type(4)));
typedef float f32x2 __attribute__((ext_vector_type(2)));
typedef short s16x4 __attribute__((ext_vector_type(4)));
typedef __bf16 bf16x8 __attribute__((ext_vector_type(8)));
typedef __bf16 bf16x2 __attribute__((ext_vector_type(2)));

constexpr int DM = 512, HD = 128, SK = 32768, FF = 1792;
constexpr int NWG = 256, NSW = 4, NTHR = (NSW + 1) * 64;
constexpr int NS = SK / 16 / 16 / NSW;
constexpr int LP = 4, RP = 8, NPRO = LP + RP + 2;
constexpr int NBL = 6, KA = (NS - NPRO - NBL) / 3, VA = (NS - 2) / 3 - 1;
static_assert(NS == 32 && NPRO + NBL + 3 * KA == NS && 3 * VA + 5 == NS, "");
constexpr int T_OFF = 115 * 16, T_K = 921, T_V = 904;
constexpr int T_ORG = 45;
constexpr int WQ = 8, WW1 = 2, WW2 = 2;
constexpr float SC = 1.4426950408889634f;
constexpr u32 SENT = 0xFFFFFFFFu;
constexpr u32 SPIN_CAP = 1u << 21;
constexpr u32 OOR = 0xFFFFFF00u;

constexpr u32 WS_EPOCH = 0;
constexpr u32 WS_QG = 4096;
constexpr u32 WS_HG = WS_QG + 8 * 2 * 512 * 8;
constexpr u32 WS_TG = 102400;
constexpr u32 WS_ML = 118784;
constexpr u32 WS_SLOT = 131072, SLOT = 4352;
constexpr u32 SL_PS = 2048, SL_PM = 2560, SL_FB = 3072;
constexpr u32 WS_END = WS_SLOT + NWG * SLOT;
static_assert(WS_HG + 2 * FF * 8 <= WS_TG && WS_TG + 256 * 8 <= WS_ML && WS_ML + 8192 <= WS_SLOT, "");

constexpr u32 L_WQ = 0, L_W1 = 0, L_WO = 16384, L_W2 = 32768;
constexpr u32 L_XN = 40960, L_Y = L_XN + 2048, L_Q = L_Y + 2048, L_H = L_Q + 1024, L_O = L_H + 256;
constexpr u32 L_MRG = L_O + 512;
constexpr u32 L_MML = L_MRG + NSW * 4 * 132 * 4;
constexpr u32 L_CNT = L_MML + NSW * 4 * 2 * 4;
constexpr u32 L_WT = L_CNT + 16;
constexpr u32 L_SC = 57344;
constexpr u32 L_PARK = L_SC + NSW * 8192;
constexpr u32 L_END = L_PARK + NSW * LP * 4096;
static_assert(L_WT + 128 <= L_SC && L_END > 80 * 1024 && L_END <= 160 * 1024, "more than half of a CU's LDS: one workgroup per CU");

struct Params {
    const u16 *x, *k, *v, *wq, *wo, *w1, *b1, *w2, *b2;
    u16* out;
    unsigned char* ws;
};

__device__ __forceinline__ f32x4 mfma32(v4u a, v4u b, f32x4 c) {
    return __builtin_amdgcn_mfma_f32_16x16x32_bf16(__builtin_bit_cast(bf16x8, a), __builtin_bit_cast(bf16x8, b), c, 0, 0, 0);
}
__device__ __forceinline__ f32x4 mfma16(u32 a0, u32 a1, u32 b0, u32 b1, f32x4 c) {
    return __builtin_amdgcn_mfma_f32_16x16x16bf16_1k(__builtin_bit_cast(s16x4, v2u{a0, a1}), __builtin_bit_cast(s16x4, v2u{b0, b1}), c, 0, 0, 0);
}
__device__ __forceinline__ u32 pk_bf16(float lo, float hi) {
    return __builtin_bit_cast(u32, __builtin_convertvector(f32x2{lo, hi}, bf16x2));
}
__device__ __forceinline__ float bf_lo(u32 w) { return __builtin_bit_cast(float, w << 16); }
__device__ __forceinline__ float bf_hi(u32 w) { return __builtin_bit_cast(float, w & 0xffff0000u); }
__device__ __forceinline__ float rbf(float f) { return bf_lo(pk_bf16(f, f)); }
__device__ __forceinline__ float score(float qk) { return rbf(rbf(qk) * 0.08838834764831845f); }
__device__ __forceinline__ float ex2(float x) { return __builtin_amdgcn_exp2f(x); }
template <int CTRL> __device__ __forceinline__ float dpp(float x) {
    return __builtin_bit_cast(float, __builtin_amdgcn_mov_dpp(__builtin_bit_cast(int, x), CTRL, 0xf, 0xf, true));
}
constexpr int XOR1 = 0xB1, XOR2 = 0x4E, XOR7 = 0x141, ROR8 = 0x128;
__device__ __forceinline__ float x16_max(float x) {
    auto s = __builtin_amdgcn_permlane16_swap(__float_as_uint(x), __float_as_uint(x), false, false);
    return fmaxf(__uint_as_float(s[0]), __uint_as_float(s[1]));
}
__device__ __forceinline__ float x32_max(float x) {
    auto s = __builtin_amdgcn_permlane32_swap(__float_as_uint(x), __float_as_uint(x), false, false);
    return fmaxf(__uint_as_float(s[0]), __uint_as_float(s[1]));
}
__device__ __forceinline__ float x16_sum(float x) {
    auto s = __builtin_amdgcn_permlane16_swap(__float_as_uint(x), __float_as_uint(x), false, false);
    return __uint_as_float(s[0]) + __uint_as_float(s[1]);
}
__device__ __forceinline__ float x32_sum(float x) {
    auto s = __builtin_amdgcn_permlane32_swap(__float_as_uint(x), __float_as_uint(x), false, false);
    return __uint_as_float(s[0]) + __uint_as_float(s[1]);
}
__device__ __forceinline__ float row_sum(float x) {
    x += dpp<XOR1>(x); x += dpp<XOR2>(x); x += dpp<XOR7>(x); x += dpp<ROR8>(x); return x;
}
__device__ __forceinline__ float row_max(float x) {
    float r;
    asm("s_nop 1\n\tv_max_f32_dpp %0, %1, %1 quad_perm:[1,0,3,2] row_mask:0xf bank_mask:0xf\n\ts_nop 1\n\t"
        "v_max_f32_dpp %0, %0, %0 quad_perm:[2,3,0,1] row_mask:0xf bank_mask:0xf\n\ts_nop 1\n\t"
        "v_max_f32_dpp %0, %0, %0 row_half_mirror row_mask:0xf bank_mask:0xf\n\ts_nop 1\n\t"
        "v_max_f32_dpp %0, %0, %0 row_ror:8 row_mask:0xf bank_mask:0xf" : "=&v"(r) : "v"(x));
    return r;
}
__device__ __forceinline__ float wave_sum(float x) { return x32_sum(x16_sum(row_sum(x))); }
__device__ __forceinline__ u32 umax4(v4u a) { return max(max(a.x, a.y), max(a.z, a.w)); }
__device__ __forceinline__ u32 no_sent(float f) { return min(__float_as_uint(f), 0xFFFFFFFEu); }

#define RLX_AGENT __ATOMIC_RELAXED, __HIP_MEMORY_SCOPE_AGENT
constexpr int AUX_NT = 2, AUX_SC1 = 16;
__device__ __forceinline__ __amdgpu_buffer_rsrc_t mk_rsrc(const void* p, u32 bytes) {
    return __builtin_amdgcn_make_buffer_rsrc((void*)p, 0, bytes, 0x00020000);
}
__device__ __forceinline__ void glds_nt(const void* src, u32 dst) {
    asm volatile("s_mov_b32 m0, %1\n\ts_nop 0\n\tglobal_load_lds_dwordx4 %0, off nt" ::"v"(src), "s"(dst) : "memory");
}
__device__ __forceinline__ void glds_sh(const void* src, u32 dst) {
    asm volatile("s_mov_b32 m0, %1\n\ts_nop 0\n\tglobal_load_lds_dwordx4 %0, off" ::"v"(src), "s"(dst) : "memory");
}
template <int OFF> __device__ __forceinline__ void glds_nt_off(const void* src, u32 dst) {
    asm volatile("s_mov_b32 m0, %1\n\ts_nop 0\n\tglobal_load_lds_dwordx4 %0, off offset:%2 nt" ::"v"(src), "s"(dst), "i"(OFF) : "memory");
}
template <int N> __device__ __forceinline__ void wait_vm() { asm volatile("s_waitcnt vmcnt(%0)" ::"i"(N) : "memory"); }
__device__ __forceinline__ void wg_barrier() { asm volatile("s_waitcnt lgkmcnt(0)\n\ts_barrier" ::: "memory"); }
__device__ __forceinline__ v2u tr_read(const LAS unsigned char* p) {
    return __builtin_bit_cast(v2u, __builtin_amdgcn_ds_read_tr16_b64_v4i16((LAS s16x4*)p));
}
__device__ __forceinline__ v4u tr_frag(const LAS unsigned char* img, int pitch, int row0, int lane) {
    const int g = lane >> 4, q = (lane >> 2) & 3, p = lane & 3;
    const LAS unsigned char* a = img + (row0 + 8 * g + q) * pitch + 8 * p;
    const v2u lo = tr_read(a), hi = tr_read(a + 4 * pitch);
    return v4u{lo.x, lo.y, hi.x, hi.y};
}

__device__ __forceinline__ void load_act(v4u (&B)[16], const LAS unsigned char* act, int lane) {
    const LAS unsigned char* ap = act + (lane & 1) * 1024 + (lane >> 4) * 16;
#pragma unroll
    for (int s = 0; s < 16; ++s) B[s] = *(const LAS v4u*)(ap + 64 * s);
}
__device__ __forceinline__ f32x4 gemv16(const LAS unsigned char* img, const v4u (&B)[16], int lane) {
    v4u A[16];
#pragma unroll
    for (int s = 0; s < 16; ++s) A[s] = tr_frag(img, 32, 32 * s, lane);
    __builtin_amdgcn_sched_barrier(0);
    f32x4 a[4];
#pragma unroll
    for (int s = 0; s < 16; ++s) a[s & 3] = mfma32(A[s], B[s], s < 4 ? f32x4{0.f, 0.f, 0.f, 0.f} : a[s & 3]);
    return (a[0] + a[1]) + (a[2] + a[3]);
}
__device__ __forceinline__ void layer_norm(float (&v)[2][8], LAS unsigned char* dst, int lane) {
    float mean[2], rden[2];
#pragma unroll
    for (int bb = 0; bb < 2; ++bb) mean[bb] = wave_sum(((v[bb][0] + v[bb][1]) + (v[bb][2] + v[bb][3])) + ((v[bb][4] + v[bb][5]) + (v[bb][6] + v[bb][7]))) * (1.f / DM);
#pragma unroll
    for (int bb = 0; bb < 2; ++bb) {
        float sq = 0.f;
#pragma unroll
        for (int u = 0; u < 8; ++u) { const float d = v[bb][u] - mean[bb]; sq = fmaf(d, d, sq); }
        rden[bb] = __builtin_amdgcn_rcpf(rbf(__builtin_amdgcn_sqrtf(rbf(rbf(wave_sum(sq) * (1.f / DM)) + 1e-5f))));
        mean[bb] = rbf(mean[bb]);
    }
#pragma unroll
    for (int bb = 0; bb < 2; ++bb) {
        v4u o;
#pragma unroll
        for (int u = 0; u < 4; ++u) {
            const u32 d2 = pk_bf16(v[bb][2 * u] - mean[bb], v[bb][2 * u + 1] - mean[bb]);
            o[u] = pk_bf16(bf_lo(d2) * rden[bb], bf_hi(d2) * rden[bb]);
            v[bb][2 * u] = bf_lo(o[u]); v[bb][2 * u + 1] = bf_hi(o[u]);
        }
        *(LAS v4u*)(dst + bb * 1024 + lane * 16) = o;
    }
}
__device__ __forceinline__ float sel4(f32x4 v, int r) {
    const bool b0 = r & 1, b1 = r & 2;
    const float lo = b0 ? v[1] : v[0], hi = b0 ? v[3] : v[2];
    return b1 ? hi : lo;
}
__device__ __forceinline__ v4u clean4(f32x4 v) { return v4u{no_sent(v[0]), no_sent(v[1]), no_sent(v[2]), no_sent(v[3])}; }

__global__ void __launch_bounds__(NTHR, 2) fused_decode(Params p) {
    extern __shared__ __attribute__((aligned(16))) unsigned char lds_raw[];
    LAS unsigned char* L = (LAS unsigned char*)lds_raw;
    const u32 lb = (u32)(size_t)L;
    const int tid = threadIdx.x, lane = tid & 63, w = __builtin_amdgcn_readfirstlane(tid >> 6);
    const int bid = blockIdx.x;
    const int kq = (bid >> 4) & 7, jq = 4 * (bid & 7) + 2 * (bid >> 7) + ((bid >> 3) & 1);
    const int m1i = (bid - kq) & 7, jm = 4 * (7 * (kq >> 1) + 4 * (kq & 1) + m1i) + (jq & 3);
    const int sid = ((bid >> 5) * 8 + (bid & 7)) * 4 + ((bid >> 3) & 3);
    const int pair = sid >> 4, b = pair >> 3, kvh = pair & 7, s = sid & 15;
    const int x8 = bid & 7, hi = 4 * x8 + (bid >> 6), cg = (bid >> 3) & 7;
    const bool do_m1 = m1i < 4 - (kq & 1), do_m2 = hi < 28;
    const int g = lane >> 4, n = lane & 15;
    const __amdgpu_buffer_rsrc_t rws = mk_rsrc(p.ws, WS_END);
    const u32 myslot = WS_SLOT + bid * SLOT, aslot = WS_SLOT + sid * SLOT;

    const u32 t_start = (u32)__builtin_amdgcn_s_memrealtime();
    asm volatile("" ::"s"(p.k), "s"(p.v), "s"(p.x), "s"(p.wq), "s"(p.ws));
    if (w >= NSW) {
        __builtin_amdgcn_s_setprio(3);
        v4u xr0, xr1; v2u ep;
        {
            const void* xa = p.x + 8 * lane;
            const void* ea = p.ws + WS_EPOCH;
            asm volatile("global_load_dwordx4 %0, %3, off\n\tglobal_load_dwordx4 %1, %3, off offset:1024\n\tglobal_load_dwordx2 %2, %4, off sc1"
                         : "=&v"(xr0), "=&v"(xr1), "=&v"(ep) : "v"(xa), "v"(ea) : "memory");
        }
        {
            const v4u sv = {SENT, SENT, SENT, SENT};
#pragma unroll
            for (int u = 0; u < 3; ++u) __builtin_amdgcn_raw_buffer_store_b128(sv, rws, (u < 2 ? aslot : myslot) + u * 1024 + lane * 16, 0, AUX_SC1);
            const u32 o4 = lane < 4 ? WS_ML + ((pair * 4 + lane) * 16 + s) * 8 : lane == 4 ? myslot + SL_FB : OOR;
            __builtin_amdgcn_raw_buffer_store_b64(v2u{SENT, SENT}, rws, o4, 0, AUX_SC1);
        }
        const int rot = 4 * (jq & 3);
        const u16* wqs = p.wq + (size_t)(lane >> 1) * 4096 + kq * 512 + 16 * jq + 8 * (lane & 1);
#pragma unroll
        for (int u = 0; u < WQ; ++u) { const int ur = (u + rot) & 15; glds_sh(wqs + (size_t)ur * 32 * 4096, lb + L_WQ + ur * 1024); }
        asm volatile("s_waitcnt vmcnt(%3)" : "+v"(xr0), "+v"(xr1), "+v"(ep) : "i"(4 + WQ) : "memory");
#pragma unroll
        for (int u = WQ; u < 16; ++u) { const int ur = (u + rot) & 15; wait_vm<WQ - 1>(); glds_sh(wqs + (size_t)ur * 32 * 4096, lb + L_WQ + ur * 1024); }
        const u64 tag = ((((u64)ep.y << 32) | ep.x) + 1) & 0xFFFFFFFFFFFFull;
        float xv[2][8];
#pragma unroll
        for (int u = 0; u < 4; ++u) { xv[0][2 * u] = bf_lo(xr0[u]); xv[0][2 * u + 1] = bf_hi(xr0[u]); xv[1][2 * u] = bf_lo(xr1[u]); xv[1][2 * u + 1] = bf_hi(xr1[u]); }
        layer_norm(xv, L + L_XN, lane);
        {
            v4u B[16];
            load_act(B, L + L_XN, lane);
            __builtin_amdgcn_sched_barrier(0);
            wait_vm<0>();
            const f32x4 qv = gemv16(L + L_WQ, B, lane);
            const u32 qb = pk_bf16(sel4(qv, n >> 1), 0.f) & 0xffffu;
            GAS u64* dst = (GAS u64*)(p.ws + WS_QG) + (kq * 2 + (n & 1)) * 512 + 16 * jq + 4 * g + (n >> 1);
            if (n < 8) __hip_atomic_store(dst, (tag << 16) | qb, RLX_AGENT);
        }
        {
            GAS u64* src = (GAS u64*)(p.ws + WS_QG) + (kvh * 2 + b) * 512 + lane;
            GAS u64* tsrc = (GAS u64*)(p.ws + WS_TG) + kq * 32 + (lane & 31);
            u64 A[8], T;
            __builtin_amdgcn_s_sleep(60);
#pragma unroll 1
            for (u32 spin = 0; spin < SPIN_CAP; ++spin) {
                bool ok = true;
#pragma unroll
                for (int k = 0; k < 8; ++k) A[k] = __hip_atomic_load(src + 64 * k, RLX_AGENT);
                T = __hip_atomic_load(tsrc, RLX_AGENT);
#pragma unroll
                for (int k = 0; k < 8; ++k) ok &= (A[k] >> 16) == tag;
                ok &= (T >> 16) == tag;
                if (__all(ok)) break;
                __builtin_amdgcn_s_sleep(24);
            }
#pragma unroll
            for (int k = 0; k < 8; ++k) *(LAS u16*)(L + L_Q + (64 * k + lane) * 2) = (u16)A[k];
            const float mean = x16_sum(row_sum((float)(short)((u32)T - t_start))) * (1.f / 32.f);
            *(LAS v4u*)(L + L_CNT) = v4u{0u, t_start + (int)mean + T_ORG, 0u, 0u};
        }
        wg_barrier();
        __builtin_amdgcn_s_setprio(1);
        layer_norm(xv, L + L_Y, lane);
        const int fb = bid >> 7, fq = (bid >> 3) & 15, hf = lane >> 5, fc = 64 * x8 + 4 * fq + 2 * hf;
        const u32 xo = *(const GAS u32*)(p.x + fb * DM + fc), b2o = *(const GAS u32*)(p.b2 + fc);
        const int col1 = do_m1 ? 16 * jm + 4 * g + ((n >> 1) & 3) : 0;
        const float bias1 = bf_lo(p.b1[col1]);
        if (do_m1) {
            const u16* s1 = p.w1 + (size_t)(lane >> 1) * FF + 16 * jm + 8 * (lane & 1);
#pragma unroll
            for (int u = 0; u < 16; ++u) { const int ur = (u + rot) & 15; wait_vm<WW1 - 1>(); glds_sh(s1 + (size_t)ur * 32 * FF, lb + L_W1 + ur * 1024); }
        }
        if (do_m2) {
            const u16* s2 = p.w2 + (size_t)(hi * 64 + (lane >> 3)) * DM + 64 * cg + 8 * (lane & 7);
#pragma unroll
            for (int u = 0; u < 8; ++u) { wait_vm<WW2 - 1>(); glds_nt(s2 + (size_t)u * 8 * DM, lb + L_W2 + u * 1024); }
        }
        wait_vm<0>();
        if (do_m1) {
            v4u B[16];
            load_act(B, L + L_Y, lane);
            const float u = rbf(rbf(sel4(gemv16(L + L_W1, B, lane), n >> 1)) + bias1);
            const float z = 1.5957691216057308f * fmaf(0.044715f * u * u, u, u);
            const float h = u / (1.f + __expf(-z));
            if (n < 8) __hip_atomic_store((GAS u64*)(p.ws + WS_HG) + (n & 1) * FF + col1, (tag << 16) | (pk_bf16(h, 0.f) & 0xffffu), RLX_AGENT);
        }
        const int pi = lane & 31, pw = ((pi & 3) * 8 + x8) * 8 + (pi >> 2);
        const u32 pslot = WS_SLOT + pw * SLOT + fb * 256 + fq * 16 + hf * 8;
        {
            const int es = ((pi >> 4) * 8 + x8) * 16 + (pi & 15);
            const int eb = hf ? pw : (es >> 5) * 32 + (es & 3) * 8 + ((es >> 2) & 7);
            const int ek = (eb >> 4) & 7, ej = 4 * (eb & 7) + 2 * (eb >> 7) + ((eb >> 3) & 1);
            GAS u64* ev = (GAS u64*)(p.ws + WS_QG) + ek * 1024 + 16 * ej;
            GAS u64* hs = do_m2 ? (GAS u64*)(p.ws + WS_HG) + 64 * hi + lane : ev;
            const int hstr = do_m2 ? FF : 0;
            u64 e, h0, h1;
            if (!do_m1) { __builtin_amdgcn_s_sleep(64); __builtin_amdgcn_s_sleep(64); }
            __builtin_amdgcn_s_sleep(64);
#pragma unroll 1
            for (u32 spin = 0; spin < SPIN_CAP; ++spin) {
                e = __hip_atomic_load(ev, RLX_AGENT); h0 = __hip_atomic_load(hs, RLX_AGENT); h1 = __hip_atomic_load(hs + hstr, RLX_AGENT);
                if (__all(((e >> 16) == tag) & ((h0 >> 16) == tag) & ((h1 >> 16) == tag))) break;
                __builtin_amdgcn_s_sleep(64);
            }
            *(LAS u16*)(L + L_H + lane * 2) = (u16)h0;
            *(LAS u16*)(L + L_H + 128 + lane * 2) = (u16)h1;
        }
        if (do_m2) {
#pragma unroll
            for (int cb = 0; cb < 4; ++cb) {
                f32x4 mp = {0.f, 0.f, 0.f, 0.f};
#pragma unroll
                for (int u = 0; u < 2; ++u) mp = mfma32(tr_frag(L + L_W2 + 32 * cb, 128, 32 * u, lane), *(const LAS v4u*)(L + L_H + (n & 1) * 128 + 64 * u + 16 * g), mp);
                __builtin_amdgcn_raw_buffer_store_b128(clean4(mp), rws, n < 2 ? myslot + SL_PM + n * 256 + cb * 64 + g * 16 : OOR, 0, AUX_SC1);
            }
        }
        float gate[2];
        {
            v2u a;
            __builtin_amdgcn_s_sleep(64);
#pragma unroll 1
            for (u32 spin = 0; spin < SPIN_CAP; ++spin) {
                asm volatile("" ::: "memory");
                a = __builtin_amdgcn_raw_buffer_load_b64(rws, pi < 28 ? pslot + SL_PM : OOR, 0, AUX_SC1);
                if (__all(max(a.x, a.y) != SENT)) break;
                __builtin_amdgcn_s_sleep(64);
            }
#pragma unroll
            for (int k = 0; k < 2; ++k) {
                const float mm = rbf(rbf(x16_sum(row_sum(__uint_as_float(a[k])))) + (k ? bf_hi(b2o) : bf_lo(b2o)));
                gate[k] = rbf(mm / (1.f + __expf(-mm)));
            }
        }
        float x0 = bf_lo(xo), x1 = bf_hi(xo);
        GAS u32* outp = (GAS u32*)(p.out + fb * DM + fc);
        asm volatile("" : "+v"(gate[0]), "+v"(gate[1]), "+v"(x0), "+v"(x1), "+v"(outp));
        __builtin_amdgcn_s_setprio(3);
        u32 mlo = lane < 4 ? WS_ML + ((pair * 4 + lane) * 16 + s) * 8 : OOR, fme = lane == 0 ? myslot + SL_FB : OOR;
        u32 mli = lane < 32 ? WS_ML + ((((lane >> 4) * 8 + x8) * 4 + (hi & 3)) * 16 + (lane & 15)) * 8 : OOR;
        u32 fbo = lane < 32 ? WS_SLOT + pw * SLOT + SL_FB : OOR, pso = pslot + SL_PS, epo = bid == 0 && lane == 0 ? WS_EPOCH : OOR;
        const u64 en = (((u64)ep.y << 32) | ep.x) + 1;
        v2u env = {(u32)en, (u32)(en >> 32)};
        asm volatile("" : "+v"(mlo), "+v"(mli), "+v"(fme), "+v"(fbo), "+v"(pso), "+v"(epo), "+v"(env));
#pragma unroll 1
        for (u32 spin = 0; spin < 16 * SPIN_CAP; ++spin) {
            if (__hip_atomic_load((LAS u32*)(L + L_CNT), __ATOMIC_RELAXED, __HIP_MEMORY_SCOPE_WORKGROUP) >= NSW) break;
            __builtin_amdgcn_s_sleep(1);
        }
        asm volatile("" ::: "memory");
        {
            f32x2 ml[NSW];
#pragma unroll
            for (int u = 0; u < NSW; ++u) ml[u] = *(const LAS f32x2*)(L + L_MML + (u * 4 + (lane & 3)) * 8);
            const float M = fmaxf(fmaxf(ml[0].x, ml[1].x), fmaxf(ml[2].x, ml[3].x));
            __builtin_amdgcn_raw_buffer_store_b64(v2u{no_sent(M), no_sent((ml[0].y + ml[1].y) + (ml[2].y + ml[3].y))}, rws, mlo, 0, AUX_SC1);
        }
#define WEIGHTS()                                                \
    do {                                                                                                       \
        const float mp = __uint_as_float(q2.x), wt = ex2(mp - row_max(mp));                                    \
        const float wn = wt * __builtin_amdgcn_rcpf(row_sum(wt * __uint_as_float(q2.y)));                      \
        if (lane < 32) *(LAS float*)(L + L_WT + lane * 4) = wn;                                                \
    } while (0)
        __builtin_amdgcn_s_sleep(36);
        v2u q2 = __builtin_amdgcn_raw_buffer_load_b64(rws, mli, 0, AUX_SC1);
        const bool have = __all(max(q2.x, q2.y) != SENT);
        if (have) WEIGHTS();
        wg_barrier();
        if (!have) {
#pragma unroll 1
            for (u32 spin = 0; spin < SPIN_CAP; ++spin) {
                asm volatile("" ::: "memory");
                q2 = __builtin_amdgcn_raw_buffer_load_b64(rws, mli, 0, AUX_SC1);
                if (__all(max(q2.x, q2.y) != SENT)) break;
                __builtin_amdgcn_s_sleep(1);
            }
            WEIGHTS();
        }
        wg_barrier();
        __builtin_amdgcn_s_sleep(5);
        __builtin_amdgcn_raw_buffer_store_b32(1u, rws, fme, 0, AUX_SC1);
        wg_barrier();
#pragma unroll 1
        for (u32 spin = 0; spin < SPIN_CAP; ++spin) {
            asm volatile("" ::: "memory");
            if (__all(__builtin_amdgcn_raw_buffer_load_b32(rws, fbo, 0, AUX_SC1) != SENT)) break;
            __builtin_amdgcn_s_sleep(1);
        }
        {
            v2u a = __builtin_amdgcn_raw_buffer_load_b64(rws, pso, 0, AUX_SC1);
            __builtin_amdgcn_raw_buffer_store_b64(env, rws, epo, 0, AUX_SC1);
            __builtin_amdgcn_sched_barrier(0);
            if (!__all(max(a.x, a.y) != SENT)) {
#pragma unroll 1
                for (u32 spin = 0; spin < SPIN_CAP; ++spin) {
                    asm volatile("" ::: "memory");
                    a = __builtin_amdgcn_raw_buffer_load_b64(rws, pso, 0, AUX_SC1);
                    if (__all(max(a.x, a.y) != SENT)) break;
                }
            }
            const float s0 = rbf(x16_sum(row_sum(__uint_as_float(a.x)))), s1 = rbf(x16_sum(row_sum(__uint_as_float(a.y))));
            const u32 r = pk_bf16(x0 + rbf(s0 * gate[0]), x1 + rbf(s1 * gate[1]));
            if (pi == 0) *outp = r;
        }
    } else {
        const u16* kb = p.k + (size_t)pair * SK * HD;
        const u16* vb = p.v + (size_t)pair * SK * HD;
        const __amdgpu_buffer_rsrc_t rk = mk_rsrc(kb, SK * HD * 2), rv = mk_rsrc(vb, SK * HD * 2);
        constexpr u32 TSTR = NSW * 4096;
        const u32 kl = (n >> 2) * 256 + g * 64 + (n & 3) * 16, vl = lane * 16;
        u32 ko = (s * NS * NSW + w) * 4096 + kl, vo = (s * NS * NSW + w) * 4096 + vl;
        v4u R[3][4], KX[RP][4];
        u64 stub0, stub;
        asm volatile("s_getpc_b64 %0\n\ts_branch 1f\n\t"
                     "s_waitcnt vmcnt(4)\n\ts_setpc_b64 s[80:81]\n\ts_waitcnt vmcnt(5)\n\ts_setpc_b64 s[80:81]\n\t"
                     "s_waitcnt vmcnt(6)\n\ts_setpc_b64 s[80:81]\n\ts_waitcnt vmcnt(7)\n\ts_setpc_b64 s[80:81]\n1:" : "=s"(stub0));
        stub0 -= 36;
        stub = stub0 + 8 * 6;
        u32 due = t_start * 16 + T_OFF;
        int late;
#define WAITW() asm volatile("s_swappc_b64 s[80:81], %0" ::"s"(stub) : "memory", "s80", "s81")
#define CLOCK() const u32 now_ = (u32)__builtin_amdgcn_s_memrealtime(); __builtin_amdgcn_sched_barrier(0)
#define PACE2(tau, lo)                                            \
    do {                                                                                                       \
        due += (tau);                                                                                          \
        late = (int)(now_ * 16 - due);                                                                         \
        const int win = __builtin_amdgcn_readfirstlane(min(max(6 + ((late + 512) >> 10), lo), 8));             \
        stub = stub0 + 8 * win;                                                                                \
        __builtin_amdgcn_sched_barrier(0);                                                                     \
    } while (0)
#define PACE(tau) PACE2(tau, 5)
#define LK(X, u) do { WAITW(); X[u] = __builtin_amdgcn_raw_buffer_load_b128(rk, ko + 1024 * (u), 0, AUX_NT); __builtin_amdgcn_sched_barrier(0); } while (0)
#define LV(X, u) do { WAITW(); X[u] = __builtin_amdgcn_raw_buffer_load_b128(rv, vo + 1024 * (u), 0, AUX_NT); __builtin_amdgcn_sched_barrier(0); } while (0)
#define LK4(X) do { LK(X, 0); LK(X, 1); LK(X, 2); LK(X, 3); ko += TSTR; } while (0)
#define LV4(X) do { LV(X, 0); LV(X, 1); LV(X, 2); LV(X, 3); vo += TSTR; } while (0)
        const bool rep = w == 1 && lane == 0;
        const v2u e2 = __builtin_amdgcn_raw_buffer_load_b64(rws, rep ? WS_EPOCH : OOR, 0, AUX_SC1);
        __builtin_amdgcn_sched_barrier(0);
        const u32 park = lb + L_PARK + w * LP * 4096;
#pragma unroll
        for (int t = 0; t < LP; ++t) {
            const unsigned char* ks = (const unsigned char*)kb + ko;
            WAITW(); glds_nt_off<0>(ks, park + t * 4096);
            WAITW(); glds_nt_off<1024>(ks, park + t * 4096);
            WAITW(); glds_nt_off<2048>(ks, park + t * 4096);
            WAITW(); glds_nt_off<3072>(ks, park + t * 4096);
            ko += TSTR;
            CLOCK(); PACE2(T_K, 6);
        }
#pragma unroll
        for (int t = 0; t < RP; ++t) {
            LK4(KX[t]); CLOCK(); PACE2(T_K, 6);
            if (t == 0) {
                const u64 gv = (((((u64)e2.y << 32) | e2.x) + 1) << 16) | ((t_start + (late >> 4)) & 0xffffu);
                __builtin_amdgcn_raw_buffer_store_b64(v2u{(u32)gv, (u32)(gv >> 32)}, rws, rep ? WS_TG + (kq * 32 + jq) * 8 : OOR, 0, AUX_SC1);
                __builtin_amdgcn_sched_barrier(0);
            }
        }
#pragma unroll
        for (int t = 0; t < 2; ++t) { LK4(R[t]); CLOCK(); PACE(T_K); }
        wg_barrier();
        due += (__builtin_amdgcn_readfirstlane(*(const LAS u32*)(L + L_CNT + 4)) - t_start) * 16;
        const int jj = n & 3;
        const bool j0 = jj & 1, j1 = jj & 2;
        const v4u Q = *(const LAS v4u*)(L + L_Q + (n >> 2) * 256 + 64 * g + 16 * jj);
        const u32 mask0 = jj == 0 ? 0x0000ffffu : jj == 1 ? 0xffff0000u : 0u, mask1 = jj == 2 ? 0x0000ffffu : jj == 3 ? 0xffff0000u : 0u;
        float m = -INFINITY, l = 0.f;
        f32x4 acc0 = {0.f, 0.f, 0.f, 0.f}, acc1 = {0.f, 0.f, 0.f, 0.f};
        LAS float* scp = (LAS float*)(L + L_SC + w * 8192) + lane;
#define KP(KR, idx)                                                                                            \
    do {                                                                                                       \
        f32x4 dd[4]; float sc[4];                                                                              \
        _Pragma("unroll") for (int u = 0; u < 4; ++u) dd[u] = mfma32(KR[u], Q, f32x4{0.f, 0.f, 0.f, 0.f});     \
        asm volatile("" : "+v"(dd[0]), "+v"(dd[1]), "+v"(dd[2]), "+v"(dd[3]));     \
        _Pragma("unroll") for (int u = 0; u < 4; ++u) sc[u] = sel4(dd[u], jj);                                 \
        const float y01 = (j0 ? sc[1] : sc[0]) + dpp<XOR1>(j0 ? sc[0] : sc[1]), y23 = (j0 ? sc[3] : sc[2]) + dpp<XOR1>(j0 ? sc[2] : sc[3]); \
        const float y = (j1 ? y23 : y01) + dpp<XOR2>(j1 ? y01 : y23);                                          \
        m = fmaxf(m, y);                                                                                       \
        scp[(idx) * 64] = y;                                                                                   \
        __builtin_amdgcn_sched_barrier(0);                                                                     \
    } while (0)
#define UNPARK(t, KR)                                                                                          \
    do {                                                                                                       \
        const LAS unsigned char* pk = L + L_PARK + (w * LP + (t)) * 4096 + lane * 16;                          \
        _Pragma("unroll") for (int u = 0; u < 4; ++u) KR[u] = *(const LAS v4u*)(pk + 1024 * u);                \
    } while (0)
#define VP(VR, sv) do { const float p1 = ex2(fmaf(score(sv), SC, -mc)); l += p1; PV(VR, p1); } while (0)
#define PV(VR, p1)                                                                                             \
    do {                                                                                                       \
        const int pb = pk_bf16(p1, p1);                                                                        \
        _Pragma("unroll") for (int r = 0; r < 4; ++r) {                                                        \
            const u32 t_ = r == 0 ? __builtin_amdgcn_mov_dpp(pb, 0x00, 0xf, 0xf, true) : r == 1 ? __builtin_amdgcn_mov_dpp(pb, 0x55, 0xf, 0xf, true) \
                         : r == 2 ? __builtin_amdgcn_mov_dpp(pb, 0xAA, 0xf, 0xf, true) : __builtin_amdgcn_mov_dpp(pb, 0xFF, 0xf, 0xf, true); \
            acc0 = mfma16(VR[r].x, VR[r].y, t_ & mask0, t_ & mask1, acc0);                                     \
            acc1 = mfma16(VR[r].z, VR[r].w, t_ & mask0, t_ & mask1, acc1);                                     \
        }                                                                                                      \
        __builtin_amdgcn_sched_barrier(0);                                                                     \
    } while (0)
#define UKK(u, idx) do { LK4(R[((u) + 2) % 3]); CLOCK(); KP(R[(u) % 3], idx); PACE(T_K); } while (0)
#define UVK(u, idx) do { LV4(R[((u) + 2) % 3]); CLOCK(); KP(R[(u) % 3], idx); PACE(T_V); } while (0)
#define UVV(u, idx) do { float sv_ = scp[(idx) * 64]; LV4(R[((u) + 2) % 3]); asm volatile("" : "+v"(sv_)); CLOCK(); VP(R[(u) % 3], sv_); PACE(T_V); } while (0)
#define UKB(u, XA, ia, XB, ib, PRE)                                                                            \
    do {                                                                                                       \
        LK(R[((u) + 2) % 3], 0); PRE; KP(XA, ia); LK(R[((u) + 2) % 3], 1); LK(R[((u) + 2) % 3], 2); KP(XB, ib); LK(R[((u) + 2) % 3], 3); ko += TSTR; \
        CLOCK(); KP(R[(u) % 3], 12 + (u)); PACE(T_K);                                                          \
    } while (0)
        static_assert(LP == 4 && RP == 8 && NBL == 6, "");
        UKB(0, KX[0], 4, KX[1], 5, (void)0);
        UKB(1, KX[2], 6, KX[3], 7, { UNPARK(0, KX[0]); UNPARK(1, KX[1]); });
        UKB(2, KX[0], 0, KX[1], 1, { UNPARK(2, KX[2]); UNPARK(3, KX[3]); });
        UKB(3, KX[2], 2, KX[3], 3, (void)0);
        UKB(4, KX[4], 8, KX[5], 9, (void)0);
        UKB(5, KX[6], 10, KX[7], 11, (void)0);
        scp += (12 + NBL) * 64;
#pragma unroll 1
        for (int it = 0; it < KA; ++it) { UKK(0, 0); UKK(1, 1); UKK(2, 2); scp += 3 * 64; }
        UVK(0, 0); UVK(1, 1);
        m = fmaxf(m, dpp<XOR1>(m)); m = fmaxf(m, dpp<XOR2>(m)); m = x32_max(x16_max(m));
        float mc = score(m) * SC;
        const int bb = w & 1, dh = w >> 1;
        u32 cnt_a = lb + L_CNT, cnt_v = lane == 0;
        u32 a_mw = L_MRG + ((w * 4 + (n >> 2)) * 132 + 32 * g + jj) * 4, a_ml = L_MML + (n >> 2) * 8;
        u32 a_mr = L_MRG + (w * 132 + 4 * (lane & 31)) * 4, o_pa = lane < 32 ? aslot + w * 512 + lane * 16 : OOR;
        u32 o_ga = WS_SLOT + ((bb * 8 + x8) * 16 + 4 * g) * SLOT + (hi & 3) * 512 + dh * 256 + n * 16, a_wt = L_WT + bb * 64 + g * 16;
        u32 a_wo = L_WO + 32 * w + (8 * g + ((lane >> 2) & 3)) * 128 + 8 * jj, a_ow = L_O + bb * 256 + dh * 128 + n * 8 + g * 2;
        u32 a_or = L_O + (n & 1) * 256 + 16 * g, o_ps = n < 2 ? myslot + SL_PS + n * 256 + w * 64 + g * 16 : OOR;
        const u16* wo_p = p.wo + (size_t)(hi * 128 + 32 * w + (lane >> 3)) * DM + 64 * cg + 8 * (lane & 7);
        asm volatile("" : "+v"(cnt_a), "+v"(cnt_v), "+v"(a_mw), "+v"(a_ml), "+v"(a_mr), "+v"(o_pa), "+v"(o_ga));
        asm volatile("" : "+v"(a_wt), "+v"(a_wo), "+v"(a_ow), "+v"(a_or), "+v"(o_ps), "+v"(wo_p));
        *(LAS float*)(L + a_ml + w * 32) = mc;
        asm volatile("ds_add_u32 %0, %1 offset:8" ::"v"(cnt_a), "v"(cnt_v) : "memory");
        scp -= (NS - 2) * 64;
#pragma unroll 1
        for (int it = 0; it < VA; ++it) { UVV(2, 0); UVV(3, 1); UVV(4, 2); scp += 3 * 64; }
        {
#pragma unroll 1
            for (u32 spin = 0; spin < 16 * SPIN_CAP; ++spin)
                if (__hip_atomic_load((LAS u32*)(L + L_CNT + 8), __ATOMIC_RELAXED, __HIP_MEMORY_SCOPE_WORKGROUP) >= NSW) break;
            asm volatile("" ::: "memory");
            float m4[NSW];
#pragma unroll
            for (int u = 0; u < NSW; ++u) m4[u] = *(const LAS float*)(L + a_ml + u * 32);
            const float M = fmaxf(fmaxf(m4[0], m4[1]), fmaxf(m4[2], m4[3])), f = ex2(mc - M);
            acc0 *= f; acc1 *= f; l *= f; mc = M;
            __builtin_amdgcn_sched_barrier(0);
        }
        {
            float pp[5];
#pragma unroll
            for (int u = 0; u < 5; ++u) pp[u] = ex2(fmaf(score(scp[u * 64]), SC, -mc));
            l += ((pp[0] + pp[1]) + (pp[2] + pp[3])) + pp[4];
            l += dpp<XOR1>(l); l += dpp<XOR2>(l);
            *(LAS float*)(L + a_ml + w * 32 + 4) = x32_sum(x16_sum(l));
            asm volatile("ds_add_u32 %0, %1" ::"v"(cnt_a), "v"(cnt_v) : "memory");
            __builtin_amdgcn_sched_barrier(0);
            { LV4(R[1]); CLOCK(); PV(R[2], pp[0]); PACE(T_V); }
            { LV4(R[2]); CLOCK(); PV(R[0], pp[1]); PACE(T_V); }
            LV4(R[0]);
            PV(R[1], pp[2]); PV(R[2], pp[3]); PV(R[0], pp[4]);
        }
        {
            LAS float* mr = (LAS float*)(L + a_mw);
#pragma unroll
            for (int r = 0; r < 4; ++r) { mr[8 * r] = acc0[r]; mr[8 * r + 4] = acc1[r]; }
        }
#pragma unroll
        for (int u = 0; u < 4; ++u) glds_nt(wo_p + (size_t)u * 8 * DM, lb + L_WO + (4 * w + u) * 1024);
        wg_barrier();
        {
            f32x4 ov[NSW];
#pragma unroll
            for (int u = 0; u < NSW; ++u) ov[u] = *(const LAS f32x4*)(L + a_mr + u * (4 * 132 * 4));
            __builtin_amdgcn_raw_buffer_store_b128(clean4((ov[0] + ov[1]) + (ov[2] + ov[3])), rws, o_pa, 0, AUX_SC1);
        }
        v4u WO[4];
        {
            v4u a[4];
#define GATHER() _Pragma("unroll") for (int k = 0; k < 4; ++k) a[k] = __builtin_amdgcn_raw_buffer_load_b128(rws, o_ga, k * SLOT, AUX_SC1)
#define ARRIVED() __all(max(max(umax4(a[0]), umax4(a[1])), max(umax4(a[2]), umax4(a[3]))) != SENT)
            wait_vm<0>();
            wg_barrier();
            GATHER();
            __builtin_amdgcn_sched_barrier(0);
            const f32x4 wn = *(const LAS f32x4*)(L + a_wt);
#pragma unroll
            for (int u = 0; u < 4; ++u) {
                const v2u lo = tr_read(L + a_wo + 4096 * u), hi2 = tr_read(L + a_wo + 4096 * u + 512);
                WO[u] = v4u{lo.x, lo.y, hi2.x, hi2.y};
            }
            __builtin_amdgcn_sched_barrier(0);
            if (!ARRIVED()) {
#pragma unroll 1
                for (u32 spin = 0; spin < SPIN_CAP; ++spin) {
                    asm volatile("" ::: "memory");
                    GATHER();
                    if (ARRIVED()) break;
                }
            }
            const f32x4 o = (wn[0] * __builtin_bit_cast(f32x4, a[0]) + wn[1] * __builtin_bit_cast(f32x4, a[1])) +
                            (wn[2] * __builtin_bit_cast(f32x4, a[2]) + wn[3] * __builtin_bit_cast(f32x4, a[3]));
            const auto s02 = __builtin_amdgcn_permlane32_swap(__float_as_uint(o[0]), __float_as_uint(o[2]), false, false);
            const auto s13 = __builtin_amdgcn_permlane32_swap(__float_as_uint(o[1]), __float_as_uint(o[3]), false, false);
            const float xs = __uint_as_float(s02[0]) + __uint_as_float(s02[1]), ys = __uint_as_float(s13[0]) + __uint_as_float(s13[1]);
            const auto sz = __builtin_amdgcn_permlane16_swap(__float_as_uint(xs), __float_as_uint(ys), false, false);
            const float z = __uint_as_float(sz[0]) + __uint_as_float(sz[1]);
            *(LAS u16*)(L + a_ow) = (u16)pk_bf16(z, z);
        }
        wg_barrier();
        {
            v4u ob[4];
#pragma unroll
            for (int u = 0; u < 4; ++u) ob[u] = *(const LAS v4u*)(L + a_or + 64 * u);
            __builtin_amdgcn_sched_barrier(0);
            const f32x4 z = {0.f, 0.f, 0.f, 0.f};
            const f32x4 sp = mfma32(WO[2], ob[2], mfma32(WO[0], ob[0], z)) + mfma32(WO[3], ob[3], mfma32(WO[1], ob[1], z));
            __builtin_amdgcn_raw_buffer_store_b128(clean4(sp), rws, o_ps, 0, AUX_SC1);
        }
    }
}

extern "C" void kernel_launch(void* const* d_in, const int* in_sizes, int n_in, void* d_out, int out_size, void* d_ws, size_t ws_size,
                              hipStream_t stream) {
    Params p;
    p.x = (const u16*)d_in[0]; p.k = (const u16*)d_in[1]; p.v = (const u16*)d_in[2]; p.wq = (const u16*)d_in[3]; p.wo = (const u16*)d_in[4];
    p.w1 = (const u16*)d_in[6]; p.b1 = (const u16*)d_in[7]; p.w2 = (const u16*)d_in[8]; p.b2 = (const u16*)d_in[9];
    p.out = (u16*)d_out; p.ws = (unsigned char*)d_ws;
    (void)hipFuncSetAttribute((const void*)fused_decode, hipFuncAttributeMaxDynamicSharedMemorySize, L_END);
    hipLaunchKernelGGL(fused_decode, dim3(NWG), dim3(NTHR), L_END, stream, p);
}
```
